```python
import torch

def _softmax(x, axis=-1):
    return torch.softmax(x, dim=axis)
def _gelu(x):
    return torch.nn.functional.gelu(x, approximate="tanh")
def _silu(x):
    return torch.nn.functional.silu(x)
def _sigmoid(x):
    return torch.sigmoid(x)
def _layernorm(x, axis=-1, eps=1e-5):
    m = x.mean(dim=axis, keepdim=True)
    v = x.var(dim=axis, keepdim=True, unbiased=False)
    return (x - m) / torch.sqrt(v + eps)
def _scatter_add(tgt, idx, src, axis=0):
    return tgt.clone().index_add_(axis, idx, src)
def _segment_sum(src, seg, n):
    out = torch.zeros((n,) + src.shape[1:], dtype=src.dtype)
    return out.index_add_(0, seg, src)
def _pad(x, pw):
    flat = tuple(v for pair in reversed(pw) for v in pair)
    return torch.nn.functional.pad(x, flat)

kernel_name = 'parallel_sum__ssm_conv_s5__mixer_channel_mlp__prompt'

def setup_inputs():

    _w = (lambda a: (lambda t: t.to(torch.bfloat16) if t.is_floating_point() else t)(torch.as_tensor(a)))
    def _A_setup():
        B, S, D = 1, 2048, 576
        P = D
        Lambda_re = -0.5 * (lambda _x: torch.exp(torch.as_tensor(_x)))((lambda *s: torch.randn(*s, dtype=torch.bfloat16) * 0.2)(P))
        Lambda_im = (lambda *s: torch.randn(*s, dtype=torch.bfloat16) * 0.8)(P) * 1.0
        B_re = (lambda *s: torch.randn(*s, dtype=torch.bfloat16) * 0.2)(P, D) * (D ** -0.5)
        B_im = (lambda *s: torch.randn(*s, dtype=torch.bfloat16) * 0.2)(P, D) * (D ** -0.5)
        C_re = (lambda *s: torch.randn(*s, dtype=torch.bfloat16) * 0.2)(D, P) * (P ** -0.5)
        C_im = (lambda *s: torch.randn(*s, dtype=torch.bfloat16) * 0.2)(D, P) * (P ** -0.5)
        log_dt = (lambda *s: torch.randn(*s, dtype=torch.bfloat16) * 0.2)(P) * 0.3 - 2.0
        Dskip = (lambda *s: torch.randn(*s, dtype=torch.bfloat16) * 0.05)(D)
        return {
            "x": _w((lambda *s: torch.randn(*s, dtype=torch.bfloat16) * 0.05)(B, S, D)),
            "Lambda_re": _w(Lambda_re),
            "Lambda_im": _w(Lambda_im),
            "B_re": _w(B_re),
            "B_im": _w(B_im),
            "C_re": _w(C_re),
            "C_im": _w(C_im),
            "log_dt": _w(log_dt),
            "Dskip": _w(Dskip),
        }
    _A_in = _A_setup()

    def _B_setup():
        B, S, D, F = 1, 2048, 576, 4608
        return {
            "x": _w((lambda *s: torch.randn(*s, dtype=torch.bfloat16) * 0.05)(B, S, D)),
            "w1": _w((lambda *s: torch.randn(*s, dtype=torch.bfloat16) * 1.6)(D, F) * (D ** -0.5)),
            "b1": _w((lambda *s: torch.randn(*s, dtype=torch.bfloat16) * 0.8)(F)),
            "w2": _w((lambda *s: torch.randn(*s, dtype=torch.bfloat16) * 0.05)(F, D) * (F ** -0.5)),
            "b2": _w((lambda *s: torch.randn(*s, dtype=torch.bfloat16) * 0.05)(D)),
        }
    _B_in = _B_setup()

    return {
        "A_x": _A_in["x"],
        "A_Lambda_re": _A_in["Lambda_re"],
        "A_Lambda_im": _A_in["Lambda_im"],
        "A_B_re": _A_in["B_re"],
        "A_B_im": _A_in["B_im"],
        "A_C_re": _A_in["C_re"],
        "A_C_im": _A_in["C_im"],
        "A_log_dt": _A_in["log_dt"],
        "A_Dskip": _A_in["Dskip"],
        "B_x": _B_in["x"],
        "B_w1": _B_in["w1"],
        "B_b1": _B_in["b1"],
        "B_w2": _B_in["w2"],
        "B_b2": _B_in["b2"],
    }

def reference(A_x, A_Lambda_re, A_Lambda_im, A_B_re, A_B_im, A_C_re, A_C_im, A_log_dt, A_Dskip, B_x, B_w1, B_b1, B_w2, B_b2):
    def _A_ref(x, Lambda_re, Lambda_im, B_re, B_im, C_re, C_im, log_dt, Dskip):
        Bsz, L, H = x.shape
        Lam = Lambda_re + 1j * Lambda_im
        Bmat = B_re + 1j * B_im
        Cmat = C_re + 1j * C_im
        dt = torch.exp(log_dt)
        Lam_bar = torch.exp(dt * Lam)
        B_bar = ((Lam_bar - 1.0) / Lam)[:, None] * Bmat
        Bu = torch.einsum("bld,pd->blp", x + 0j, B_bar)
        h = torch.zeros_like(Bu[:, 0, :])
        hs = []
        for t in range(L):
            h = Lam_bar[None, :] * h + Bu[:, t, :]
            hs.append(h)
        hseq = torch.stack(hs, 1)
        y = 2.0 * torch.einsum("blp,dp->bld", hseq, Cmat).real + x * Dskip[None, None, :]
        return y

    def _B_ref(x, w1, b1, w2, b2):
        y = _layernorm(x, axis=-1)
        h = _gelu(torch.einsum("bsd,df->bsf", y, w1) + b1)
        return torch.einsum("bsf,fd->bsd", h, w2) + b2

    x = A_x
    xn = _layernorm(x, axis=-1)
    return x + _A_ref(xn, A_Lambda_re, A_Lambda_im, A_B_re, A_B_im, A_C_re, A_C_im, A_log_dt, A_Dskip) + _B_ref(xn, B_w1, B_b1, B_w2, B_b2)
```

```cpp
#include <hip/hip_runtime.h>
#include <hip/hip_bf16.h>
typedef unsigned short u16;
__device__ __forceinline__ float bf2f(u16 v){ return __uint_as_float(((unsigned)v)<<16); }
constexpr int L=2048, D=576, F=4608, P=576;

__global__ void k_ln(const u16* x, float* xn, float* y){
  int t=blockIdx.x; int lane=threadIdx.x;
  float v[9]; float s=0;
  for(int i=0;i<9;i++){ v[i]=bf2f(x[t*D+lane+64*i]); s+=v[i]; }
  for(int o=32;o;o>>=1) s+=__shfl_xor(s,o);
  float m=s/D, q=0;
  for(int i=0;i<9;i++){ v[i]-=m; q+=v[i]*v[i]; }
  for(int o=32;o;o>>=1) q+=__shfl_xor(q,o);
  float r=rsqrtf(q/D+1e-5f); s=0;
  for(int i=0;i<9;i++){ v[i]*=r; xn[t*D+lane+64*i]=v[i]; s+=v[i]; }
  for(int o=32;o;o>>=1) s+=__shfl_xor(s,o);
  m=s/D; q=0;
  for(int i=0;i<9;i++){ v[i]-=m; q+=v[i]*v[i]; }
  for(int o=32;o;o>>=1) q+=__shfl_xor(q,o);
  r=rsqrtf(q/D+1e-5f);
  for(int i=0;i<9;i++){ y[t*D+lane+64*i]=v[i]*r; }
}
__global__ void k_g1(const float* y, const u16* w1, const u16* b1, float* h){
  int t=blockIdx.y; int f=blockIdx.x*256+threadIdx.x;
  float a=0; for(int d=0;d<D;d++) a+=y[t*D+d]*bf2f(w1[d*F+f]);
  a+=bf2f(b1[f]);
  float u=0.7978845608f*(a+0.044715f*a*a*a);
  h[(size_t)t*F+f]=0.5f*a*(1.f+tanhf(u));
}
__global__ void k_bu(const float* xn, const u16* Bre, const u16* Bim, float* bu){
  int t=blockIdx.y; int p=blockIdx.x*64+threadIdx.x;
  float a=0,b=0; for(int d=0;d<D;d++){ float v=xn[t*D+d]; a+=v*bf2f(Bre[p*D+d]); b+=v*bf2f(Bim[p*D+d]); }
  bu[(size_t)t*2*P+p]=a; bu[(size_t)t*2*P+P+p]=b;
}
__global__ void k_scan(float* bu, const u16* Lre, const u16* Lim, const u16* ldt){
  int p=blockIdx.x*64+threadIdx.x;
  float lr=bf2f(Lre[p]), li=bf2f(Lim[p]); float dt=expf(bf2f(ldt[p]));
  float e=expf(dt*lr), ar=e*cosf(dt*li), ai=e*sinf(dt*li);
  float nr=ar-1.f, ni=ai, den=lr*lr+li*li;
  float cr=(nr*lr+ni*li)/den, ci=(ni*lr-nr*li)/den;
  float hr=0,hi=0;
  for(int t=0;t<L;t++){ float a=bu[(size_t)t*2*P+p], b=bu[(size_t)t*2*P+P+p];
    float ur=cr*a-ci*b, ui=cr*b+ci*a;
    float nr2=ar*hr-ai*hi+ur, ni2=ar*hi+ai*hr+ui; hr=nr2; hi=ni2;
    bu[(size_t)t*2*P+p]=hr; bu[(size_t)t*2*P+P+p]=hi; }
}
__global__ void k_out(const u16* x, const float* xn, const float* h, const float* hs, const u16* w2, const u16* b2,
                      const u16* Cre, const u16* Cim, const u16* Dsk, float* out){
  int t=blockIdx.y; int d=blockIdx.x*64+threadIdx.x;
  float a=0; for(int f=0;f<F;f++) a+=h[(size_t)t*F+f]*bf2f(w2[f*D+d]);
  float c=0; for(int p=0;p<P;p++) c+=hs[(size_t)t*2*P+p]*bf2f(Cre[d*P+p])-hs[(size_t)t*2*P+P+p]*bf2f(Cim[d*P+p]);
  out[t*D+d]=bf2f(x[t*D+d])+2.f*c+xn[t*D+d]*bf2f(Dsk[d])+a+bf2f(b2[d]);
}
extern "C" void kernel_launch(void* const* d_in, const int* in_sizes, int n_in, void* d_out, int out_size, void* d_ws, size_t ws_size, hipStream_t stream){
  const u16* x=(const u16*)d_in[0];
  float* ws=(float*)d_ws; float* xn=ws; float* y=xn+L*D; float* h=y+L*D; float* bu=h+(size_t)L*F;
  k_ln<<<L,64,0,stream>>>(x,xn,y);
  k_g1<<<dim3(F/256,L),256,0,stream>>>(y,(const u16*)d_in[10],(const u16*)d_in[11],h);
  k_bu<<<dim3(P/64,L),64,0,stream>>>(xn,(const u16*)d_in[3],(const u16*)d_in[4],bu);
  k_scan<<<P/64,64,0,stream>>>(bu,(const u16*)d_in[1],(const u16*)d_in[2],(const u16*)d_in[7]);
  k_out<<<dim3(D/64,L),64,0,stream>>>(x,xn,h,bu,(const u16*)d_in[12],(const u16*)d_in[13],(const u16*)d_in[5],(const u16*)d_in[6],(const u16*)d_in[8],(float*)d_out);
}
```

```cpp
#include <hip/hip_runtime.h>

__global__ __launch_bounds__(768) __attribute__((aligned(4096))) void fused_s5_mlp_kernel(
    const void* x, const void* lam_re, const void* lam_im, const void* b_re, const void* b_im,
    const void* c_re, const void* c_im, const void* log_dt, const void* dskip, const void* w1,
    const void* b1, const void* w2, const void* b2, void* out, void* ws, void* ws_again) {
  const int wave = __builtin_amdgcn_readfirstlane(threadIdx.x >> 6);
  asm volatile(
    "s_mov_b32 s8, %1\n"
    "s_mov_b32 s9, %2\n"
    "s_mov_b64 s[28:29], %0\n"
    "s_load_dwordx16 s[36:51], s[28:29], 0x0\n"
    "s_load_dwordx16 s[52:67], s[28:29], 0x40\n"
    "s_mov_b32 s5, 0xffff0000\n"
    "s_getpc_b64 s[76:77]\n"
    ".Lcode_pc:\n"
    "v_mbcnt_lo_u32_b32 v0, -1, 0\n"
    "v_mbcnt_hi_u32_b32 v0, -1, v0\n"
    "s_and_b32 s11, s8, 7\n"
    "s_lshr_b32 s10, s8, 3\n"
    "s_cmp_ge_u32 s8, 234\n"
    "s_cbranch_scc1 .Lhelper\n"
    "s_sub_u32 s30, s8, 208\n"
    "s_cmp_lt_u32 s10, 26\n"
    "s_cselect_b32 s10, s10, s30\n"
    "s_cselect_b32 s11, s11, 8\n"
    "s_and_b32 s12, s9, 3\n"
    "s_lshr_b32 s13, s9, 2\n"
    "s_lshr_b32 s14, s12, 1\n"
    "s_and_b32 s15, s12, 1\n"
    "s_mul_i32 s31, s11, 4\n"
    "s_add_u32 s31, s31, s10\n"
    "s_mul_i32 s30, s31, 57\n"
    "s_lshr_b32 s30, s30, 9\n"
    "s_mul_i32 s30, s30, 9\n"
    "s_sub_u32 s16, s31, s30\n"
    "s_cmp_eq_u32 s13, 1\n"
    "s_cbranch_scc1 .LheadS\n"
    "s_cmp_eq_u32 s13, 2\n"
    "s_cbranch_scc1 .LheadT\n"
    "s_lshr_b32 s30, s8, 3\n"
    "s_lshl_b32 s30, s30, 2\n"
    "s_add_u32 s30, s30, s12\n"
    "s_lshl_b32 s30, s30, 10\n"
    "s_and_b32 s77, s77, 0xffff\n"
    "s_mov_b32 s78, .Lcode_end-.Lcode_pc\n"
    "s_mov_b32 s79, 0x20000\n"
    "v_lshlrev_b32 v4, 4, v0\n"
    "buffer_load_dwordx4 v[8:11], v4, s[76:79], s30 offen\n"
    "s_add_u32 s31, s12, 0\n"
    "s_getpc_b64 s[34:35]\n"
    "s_add_u32 s34, s34, 16\n"
    "s_addc_u32 s35, s35, 0\n"
    "s_mov_b64 exec, 0\n"
    "s_branch .Ltouch\n"
    "s_mov_b64 exec, -1\n"
    "s_add_u32 s31, s12, 8\n"
    "s_getpc_b64 s[34:35]\n"
    "s_add_u32 s34, s34, 16\n"
    "s_addc_u32 s35, s35, 0\n"
    "s_mov_b64 exec, 0\n"
    "s_branch .Ltouch\n"
    "s_mov_b64 exec, -1\n"
    "s_add_u32 s31, s12, 12\n"
    "s_getpc_b64 s[34:35]\n"
    "s_add_u32 s34, s34, 16\n"
    "s_addc_u32 s35, s35, 0\n"
    "s_mov_b64 exec, 0\n"
    "s_branch .Ltouch\n"
    "s_mov_b64 exec, -1\n"
    "s_branch .LroleE\n"
    ".LheadS:\n"
    "s_lshl_b32 s30, s15, 3\n"
    "v_lshrrev_b32 v2, 3, v0\n"
    "v_add_u32 v2, s30, v2\n"
    "v_and_b32 v3, 6, v2\n"
    "v_and_b32 v4, 7, v0\n"
    "v_xor_b32 v3, v3, v4\n"
    "v_lshlrev_b32 v3, 4, v3\n"
    "v_lshrrev_b32 v4, 2, v2\n"
    "v_mul_u32_u24 v4, 20, v4\n"
    "v_and_b32 v2, 3, v2\n"
    "v_add_u32 v2, v2, v4\n"
    "v_mul_u32_u24 v2, 1152, v2\n"
    "v_add_u32 v82, v2, v3\n"
    "s_lshl_b32 s22, s14, 12\n"
    "s_lshl_b32 s30, s15, 10\n"
    "s_add_u32 s22, s22, s30\n"
    "s_mul_i32 s24, s10, 91008\n"
    "s_mul_i32 s31, s14, 9216\n"
    "s_lshl_b32 s30, s16, 7\n"
    "s_add_u32 s30, s30, s31\n"
    "s_add_u32 s23, s24, s30\n"
    "s_add_u32 s24, s24, 1152\n"
    "s_cmp_eq_u32 s15, 0\n"
    "s_cselect_b32 s34, -1, 0x00ffffff\n"
    "s_cmp_eq_u32 s12, 3\n"
    "s_cselect_b32 s98, 0x00ffffff, -1\n"
    "v_bfe_u32 v4, v0, 1, 2\n"
    "v_lshrrev_b32 v5, 4, v0\n"
    "v_xor_b32 v4, v4, v5\n"
    "v_and_b32 v5, 1, v0\n"
    "v_lshlrev_b32 v5, 4, v5\n"
    "v_lshl_add_u32 v2, v4, 5, v5\n"
    "v_lshrrev_b32 v4, 5, v0\n"
    "v_bfe_u32 v5, v0, 3, 2\n"
    "v_lshl_add_u32 v4, v4, 3, v5\n"
    "v_mul_u32_u24 v4, 9216, v4\n"
    "v_add_u32 v2, v2, v4\n"
    "v_add_u32 v3, 146432, v2\n"
    "s_mul_i32 s26, s15, 294912\n"
    "s_lshl_b32 s30, s11, 10\n"
    "s_add_u32 s26, s26, s30\n"
    "s_lshl_b32 s30, s14, 7\n"
    "s_add_u32 s26, s26, s30\n"
    "s_lshl_b32 s27, s12, 11\n"
    "s_add_u32 s27, s27, 96256\n"
    "s_add_u32 s25, s16, 0\n"
    "s_sub_u32 s19, s25, 9\n"
    "s_cmp_ge_u32 s25, 9\n"
    "s_cselect_b32 s25, s19, s25\n"
    "s_mul_i32 s25, s25, 589824\n"
    "s_add_u32 s25, s25, s26\n"
    "s_waitcnt lgkmcnt(0)\n"
    "s_mov_b32 s72, s36\n"
    "s_and_b32 s73, s37, 0xffff\n"
    "s_mov_b32 s74, 2359296\n"
    "s_mov_b32 s75, 0x20000\n"
    "s_mov_b32 s84, s54\n"
    "s_and_b32 s85, s55, 0xffff\n"
    "s_mov_b32 s86, 5308416\n"
    "s_mov_b32 s87, 0x20000\n"
    "s_add_u32 m0, s22, 0\n"
    "s_add_u32 s19, s23, 0\n"
    "buffer_load_dwordx4 v82, s[72:75], s19 offen lds\n"
    "s_add_u32 m0, s22, 2048\n"
    "s_add_u32 s19, s23, 4608\n"
    "buffer_load_dwordx4 v82, s[72:75], s19 offen lds\n"
    "s_add_u32 m0, s22, 4096\n"
    "s_add_u32 s19, s23, 9216\n"
    "s_mov_b32 exec_hi, s98\n"
    "buffer_load_dwordx4 v82, s[72:75], s19 offen lds\n"
    "s_mov_b32 exec_hi, -1\n"
    "s_mul_i32 s19, s14, 6144\n"
    "s_add_u32 s22, s22, s19\n"
    "s_mul_i32 s19, s14, 9088\n"
    "s_sub_u32 s23, s23, s19\n"
    "s_add_u32 s23, s23, 128\n"
    "s_sub_u32 s19, s23, 1152\n"
    "s_cmp_ge_u32 s23, s24\n"
    "s_cselect_b32 s23, s19, s23\n"
    "s_add_u32 m0, s27, 0\n"
    "s_add_u32 s19, s25, 0\n"
    "buffer_load_dwordx4 v2, s[84:87], s19 offen offset:0 lds\n"
    "buffer_load_dwordx4 v3, s[84:87], s19 offen offset:1024 lds\n"
    "s_add_u32 m0, s27, 8192\n"
    "s_add_u32 s19, s25, 36864\n"
    "buffer_load_dwordx4 v2, s[84:87], s19 offen offset:0 lds\n"
    "buffer_load_dwordx4 v3, s[84:87], s19 offen offset:1024 lds\n"
    "s_add_u32 m0, s27, 16384\n"
    "s_add_u32 s19, s25, 256\n"
    "buffer_load_dwordx4 v2, s[84:87], s19 offen offset:0 lds\n"
    "buffer_load_dwordx4 v3, s[84:87], s19 offen offset:1024 lds\n"
    "s_add_u32 m0, s27, 24576\n"
    "s_add_u32 s19, s25, 37120\n"
    "buffer_load_dwordx4 v2, s[84:87], s19 offen offset:0 lds\n"
    "buffer_load_dwordx4 v3, s[84:87], s19 offen offset:1024 lds\n"
    "s_branch .LroleS\n"
    ".Ltouch:\n"
    "s_cmp_eq_u32 s31, 0\n"
    "s_cbranch_scc1 .Lpage9\n"
    "s_cmp_eq_u32 s31, 1\n"
    "s_cbranch_scc1 .Lpage10\n"
    "s_cmp_eq_u32 s31, 2\n"
    "s_cbranch_scc1 .Lpage4\n"
    "s_cmp_eq_u32 s31, 3\n"
    "s_cbranch_scc1 .Lpage5\n"
    "s_cmp_eq_u32 s31, 4\n"
    "s_cbranch_scc1 .Lpage1\n"
    "s_cmp_eq_u32 s31, 5\n"
    "s_cbranch_scc1 .Lpage11\n"
    "s_cmp_eq_u32 s31, 6\n"
    "s_cbranch_scc1 .Lpage2\n"
    "s_cmp_eq_u32 s31, 7\n"
    "s_cbranch_scc1 .Lpage6\n"
    "s_cmp_eq_u32 s31, 8\n"
    "s_cbranch_scc1 .Lpage12\n"
    "s_cmp_eq_u32 s31, 9\n"
    "s_cbranch_scc1 .Lpage3\n"
    "s_cmp_eq_u32 s31, 10\n"
    "s_cbranch_scc1 .Lpage7\n"
    "s_cmp_eq_u32 s31, 11\n"
    "s_cbranch_scc1 .Lpage13\n"
    "s_cmp_eq_u32 s31, 12\n"
    "s_cbranch_scc1 .Lpage8\n"
    "s_cmp_eq_u32 s31, 13\n"
    "s_cbranch_scc1 .Lpage14\n"
    "s_cmp_eq_u32 s31, 14\n"
    "s_nop 0\n"
    "s_cmp_eq_u32 s31, 15\n"
    "s_nop 0\n"
    ".Ltouch_next:\n"
    "s_setpc_b64 s[34:35]\n"
    ".LheadT:\n"
    "v_mov_b32 v8, 0x3f803f80\n"
    "v_mov_b32 v9, 0x3f803f80\n"
    "v_mov_b32 v10, 0x3f803f80\n"
    "v_mov_b32 v11, 0x3f803f80\n"
    "s_mul_i32 s30, s12, 30720\n"
    "s_add_u32 s30, s30, 10112\n"
    "v_lshrrev_b32 v4, 3, v0\n"
    "v_mul_u32_u24 v4, 10240, v4\n"
    "v_and_b32 v5, 7, v0\n"
    "v_lshl_add_u32 v4, v5, 4, v4\n"
    "v_add_u32 v4, s30, v4\n"
    "s_cmp_eq_u32 s12, 3\n"
    "s_cbranch_scc1 .LT_noones\n"
    "s_mov_b64 exec, 0xffffff\n"
    "ds_write_b128 v4, v[8:11]\n"
    "s_mov_b64 exec, -1\n"
    ".LT_noones:\n"
    "s_add_u32 s31, s12, 4\n"
    "s_getpc_b64 s[34:35]\n"
    "s_add_u32 s34, s34, 16\n"
    "s_addc_u32 s35, s35, 0\n"
    "s_mov_b64 exec, 0\n"
    "s_branch .Ltouch\n"
    "s_mov_b64 exec, -1\n"
    "s_waitcnt lgkmcnt(0)\n"
    ".LroleT:\n"
    "s_load_dword s17, s[64:65], 0x0\n"
    "v_lshlrev_b32 v126, 4, v0\n"
    "v_lshrrev_b32 v129, 4, v0\n"
    "v_and_b32 v128, 15, v0\n"
    "v_lshlrev_b32 v128, 2, v128\n"
    "s_lshl_b32 s30, s11, 7\n"
    "s_lshl_b32 s31, s12, 5\n"
    "s_add_u32 s30, s30, s31\n"
    "v_lshrrev_b32 v4, 1, v128\n"
    "v_add_u32 v4, s30, v4\n"
    "v_mov_b32 v2, s50\n"
    "v_mov_b32 v3, s51\n"
    "s_mov_b64 exec, 0xffff\n"
    "v_mov_b32 v2, s38\n"
    "v_mov_b32 v3, s39\n"
    "s_mov_b32 exec_lo, 0xffff0000\n"
    "v_mov_b32 v2, s40\n"
    "v_mov_b32 v3, s41\n"
    "s_mov_b64 exec, -1\n"
    "v_add_co_u32 v2, vcc, v2, v4\n"
    "s_nop 1\n"
    "v_addc_co_u32 v3, vcc, 0, v3, vcc\n"
    "s_mov_b32 s20, s56\n"
    "s_and_b32 s21, s57, 0xffff\n"
    "s_mov_b32 s22, 9216\n"
    "s_mov_b32 s23, 0x20000\n"
    "s_lshl_b32 s24, s11, 10\n"
    "s_lshl_b32 s30, s12, 8\n"
    "s_add_u32 s24, s24, s30\n"
    "v_lshlrev_b32 v57, 2, v0\n"
    "s_lshl_b32 s30, s12, 11\n"
    "v_lshl_add_u32 v110, v0, 4, s30\n"
    "v_add_u32 v111, 51200, v110\n"
    "s_lshl_b32 s30, s12, 9\n"
    "s_add_u32 s30, s30, 8192\n"
    "v_and_b32 v100, 31, v0\n"
    "v_lshl_add_u32 v100, v100, 4, s30\n"
    "v_lshrrev_b32 v101, 5, v0\n"
    "v_lshl_add_u32 v112, v101, 3, v100\n"
    "v_mul_u32_u24 v101, 10240, v101\n"
    "v_add_u32 v100, v100, v101\n"
    "v_add_u32 v113, 10240, v100\n"
    "v_mov_b32 v4, 0\n"
    "v_mov_b32 v10, 0\n"
    "v_mov_b32 v5, 0\n"
    "v_mov_b32 v11, 0\n"
    "v_mov_b32 v6, 0\n"
    "v_mov_b32 v12, 0\n"
    "s_lshl_b32 s27, s12, 11\n"
    "s_add_u32 s27, s27, 96256\n"
    "s_mov_b32 s68, s64\n"
    "s_and_b32 s69, s65, 0xffff\n"
    "s_mov_b32 s70, 50331648\n"
    "s_mov_b32 s71, 0x20000\n"
    "s_mov_b32 s76, s58\n"
    "s_and_b32 s77, s59, 0xffff\n"
    "s_mov_b32 s78, 5308416\n"
    "s_mov_b32 s79, 0x20000\n"
    "s_mov_b32 s72, s54\n"
    "s_and_b32 s73, s55, 0xffff\n"
    "s_mov_b32 s74, 5308416\n"
    "s_mov_b32 s75, 0x20000\n"
    "s_mul_i32 s26, s15, 294912\n"
    "s_lshl_b32 s30, s11, 10\n"
    "s_add_u32 s26, s26, s30\n"
    "s_lshl_b32 s30, s14, 7\n"
    "s_add_u32 s26, s26, s30\n"
    "s_lshl_b32 s27, s12, 11\n"
    "s_add_u32 s27, s27, 96256\n"
    "s_mov_b32 s80, s46\n"
    "s_and_b32 s81, s47, 0xffff\n"
    "s_mov_b32 s82, 663552\n"
    "s_mov_b32 s83, 0x20000\n"
    "s_mov_b32 s84, s48\n"
    "s_and_b32 s85, s49, 0xffff\n"
    "s_mov_b32 s86, 663552\n"
    "s_mov_b32 s87, 0x20000\n"
    "s_cmp_eq_u32 s14, 0\n"
    "s_cselect_b64 s[92:93], s[80:81], s[84:85]\n"
    "s_mov_b64 s[94:95], s[82:83]\n"
    "s_mul_i32 s30, s11, 26624\n"
    "s_add_u32 s30, s30, 65536\n"
    "s_add_u32 s88, s64, s30\n"
    "s_addc_u32 s89, s65, 0\n"
    "s_and_b32 s89, s89, 0xffff\n"
    "s_mov_b32 s90, 26624\n"
    "s_mov_b32 s91, 0x20000\n"
    "v_and_b32 v44, 15, v0\n"
    "v_lshrrev_b32 v45, 4, v0\n"
    "v_and_b32 v117, 6, v0\n"
    "v_xor_b32 v45, v45, v117\n"
    "v_lshlrev_b32 v45, 4, v45\n"
    "v_lshl_add_u32 v116, v44, 7, v45\n"
    "v_xor_b32 v117, 64, v116\n"
    "s_lshl_b32 s30, s12, 11\n"
    "s_add_u32 s30, s30, 96256\n"
    "v_add_u32 v118, s30, v116\n"
    "v_add_u32 v119, s30, v117\n"
    "v_bfe_u32 v44, v0, 1, 2\n"
    "v_lshrrev_b32 v45, 4, v0\n"
    "v_xor_b32 v44, v44, v45\n"
    "v_and_b32 v45, 1, v0\n"
    "v_lshlrev_b32 v45, 4, v45\n"
    "v_lshl_add_u32 v122, v44, 5, v45\n"
    "v_lshrrev_b32 v44, 3, v0\n"
    "v_mul_u32_u24 v44, 1152, v44\n"
    "v_add_u32 v122, v122, v44\n"
    "v_add_u32 v123, 8192, v122\n"
    "s_lshl_b32 s30, s14, 12\n"
    "s_add_u32 s30, s30, 96256\n"
    "v_lshrrev_b32 v44, 5, v0\n"
    "v_lshlrev_b32 v44, 10, v44\n"
    "v_bfe_u32 v45, v0, 2, 3\n"
    "v_lshl_add_u32 v44, v45, 7, v44\n"
    "v_and_b32 v120, 3, v0\n"
    "v_lshl_add_u32 v44, v120, 3, v44\n"
    "v_add_u32 v44, s30, v44\n"
    "v_lshrrev_b32 v45, 1, v45\n"
    "s_lshl_b32 s30, s15, 1\n"
    "v_xor_b32 v45, s30, v45\n"
    "v_lshl_add_u32 v120, v45, 5, v44\n"
    "v_xor_b32 v121, 32, v120\n"
    "s_lshl_b32 s30, s12, 8\n"
    "v_lshl_add_u32 v133, v128, 2, s30\n"
    "v_lshl_add_u32 v133, v129, 10, v133\n"
    "v_subrev_u32 v120, 55296, v120\n"
    "v_subrev_u32 v121, 55296, v121\n"
    "s_add_u32 s30, s11, 9\n"
    "s_sub_u32 s30, s30, s16\n"
    "s_sub_u32 s31, s30, 9\n"
    "s_cmp_ge_u32 s30, 9\n"
    "s_cselect_b32 s30, s31, s30\n"
    "s_mul_i32 s30, s30, 10240\n"
    "s_lshl_b32 s31, s15, 2\n"
    "s_lshl_b32 s29, s14, 1\n"
    "s_add_u32 s31, s31, s29\n"
    "v_lshrrev_b32 v1, 5, v0\n"
    "v_add_u32 v1, s31, v1\n"
    "v_and_b32 v46, 6, v0\n"
    "v_xor_b32 v1, v1, v46\n"
    "v_lshl_add_u32 v1, v1, 4, s30\n"
    "v_lshl_add_u32 v1, v128, 5, v1\n"
    "v_bfe_u32 v46, v0, 4, 1\n"
    "v_lshl_add_u32 v1, v46, 3, v1\n"
    "s_barrier\n"
    "s_waitcnt lgkmcnt(0)\n"
    "s_add_u32 s17, s17, 1\n"
    "s_xor_b32 s18, s17, 1540483477\n"
    "global_load_ushort v150, v[2:3], off\n"
    "buffer_load_dword v56, v57, s[20:23], s24 offen\n"
    "s_lshl_b32 s30, s11, 7\n"
    "s_lshl_b32 s31, s15, 6\n"
    "s_add_u32 s30, s30, s31\n"
    "s_lshl_b32 s31, s14, 5\n"
    "s_add_u32 s30, s30, s31\n"
    "v_lshlrev_b32 v46, 3, v129\n"
    "s_mov_b32 s20, s52\n"
    "s_and_b32 s21, s53, 0xffff\n"
    "s_mov_b32 s22, 1152\n"
    "s_mov_b32 s23, 0x20000\n"
    "buffer_load_dwordx2 v[130:131], v46, s[20:23], s30 offen\n"
    "s_mov_b32 s20, s60\n"
    "s_and_b32 s21, s61, 0xffff\n"
    "s_mov_b32 s22, 1152\n"
    "s_mov_b32 s23, 0x20000\n"
    "buffer_load_dwordx2 v[164:165], v46, s[20:23], s30 offen\n"
    "ds_read_b128 v[60:63], v110 offset:0\n"
    "ds_read_b128 v[64:67], v110 offset:1024\n"
    "ds_read_b64 v[68:69], v112\n"
    "s_barrier\n"
    "s_waitcnt lgkmcnt(2)\n"
    "v_lshlrev_b32 v100, 16, v60\n"
    "v_and_b32 v101, s5, v60\n"
    "v_lshlrev_b32 v102, 16, v61\n"
    "v_and_b32 v103, s5, v61\n"
    "v_add_f32 v4, v4, v100\n"
    "v_fmac_f32 v10, v100, v100\n"
    "v_add_f32 v4, v4, v101\n"
    "v_fmac_f32 v10, v101, v101\n"
    "v_add_f32 v4, v4, v102\n"
    "v_fmac_f32 v10, v102, v102\n"
    "v_add_f32 v4, v4, v103\n"
    "v_fmac_f32 v10, v103, v103\n"
    "v_lshlrev_b32 v100, 16, v62\n"
    "v_and_b32 v101, s5, v62\n"
    "v_lshlrev_b32 v102, 16, v63\n"
    "v_and_b32 v103, s5, v63\n"
    "v_add_f32 v4, v4, v100\n"
    "v_fmac_f32 v10, v100, v100\n"
    "v_add_f32 v4, v4, v101\n"
    "v_fmac_f32 v10, v101, v101\n"
    "v_add_f32 v4, v4, v102\n"
    "v_fmac_f32 v10, v102, v102\n"
    "v_add_f32 v4, v4, v103\n"
    "v_fmac_f32 v10, v103, v103\n"
    "s_waitcnt lgkmcnt(1)\n"
    "v_lshlrev_b32 v100, 16, v64\n"
    "v_and_b32 v101, s5, v64\n"
    "v_lshlrev_b32 v102, 16, v65\n"
    "v_and_b32 v103, s5, v65\n"
    "v_add_f32 v5, v5, v100\n"
    "v_fmac_f32 v11, v100, v100\n"
    "v_add_f32 v5, v5, v101\n"
    "v_fmac_f32 v11, v101, v101\n"
    "v_add_f32 v5, v5, v102\n"
    "v_fmac_f32 v11, v102, v102\n"
    "v_add_f32 v5, v5, v103\n"
    "v_fmac_f32 v11, v103, v103\n"
    "v_lshlrev_b32 v100, 16, v66\n"
    "v_and_b32 v101, s5, v66\n"
    "v_lshlrev_b32 v102, 16, v67\n"
    "v_and_b32 v103, s5, v67\n"
    "v_add_f32 v5, v5, v100\n"
    "v_fmac_f32 v11, v100, v100\n"
    "v_add_f32 v5, v5, v101\n"
    "v_fmac_f32 v11, v101, v101\n"
    "v_add_f32 v5, v5, v102\n"
    "v_fmac_f32 v11, v102, v102\n"
    "v_add_f32 v5, v5, v103\n"
    "v_fmac_f32 v11, v103, v103\n"
    "s_waitcnt lgkmcnt(0)\n"
    "v_lshlrev_b32 v100, 16, v68\n"
    "s_barrier\n"
    "v_and_b32 v101, s5, v68\n"
    "v_lshlrev_b32 v102, 16, v69\n"
    "v_and_b32 v103, s5, v69\n"
    "v_add_f32 v6, v6, v100\n"
    "v_fmac_f32 v12, v100, v100\n"
    "v_add_f32 v6, v6, v101\n"
    "v_fmac_f32 v12, v101, v101\n"
    "v_add_f32 v6, v6, v102\n"
    "v_fmac_f32 v12, v102, v102\n"
    "v_add_f32 v6, v6, v103\n"
    "v_fmac_f32 v12, v103, v103\n"
    "s_sleep 2\n"
    "s_nop 5\n"
    "ds_read_b128 v[72:75], v110 offset:10240\n"
    "ds_read_b128 v[76:79], v110 offset:11264\n"
    "ds_read_b128 v[80:83], v110 offset:20480\n"
    "ds_read_b128 v[84:87], v110 offset:21504\n"
    "ds_read_b128 v[88:91], v113 offset:0\n"
    "s_barrier\n"
    "s_waitcnt lgkmcnt(4)\n"
    "v_lshlrev_b32 v100, 16, v72\n"
    "v_and_b32 v101, s5, v72\n"
    "v_lshlrev_b32 v102, 16, v73\n"
    "v_and_b32 v103, s5, v73\n"
    "v_add_f32 v4, v4, v100\n"
    "v_fmac_f32 v10, v100, v100\n"
    "v_add_f32 v4, v4, v101\n"
    "v_fmac_f32 v10, v101, v101\n"
    "v_add_f32 v4, v4, v102\n"
    "v_fmac_f32 v10, v102, v102\n"
    "v_add_f32 v4, v4, v103\n"
    "v_fmac_f32 v10, v103, v103\n"
    "v_lshlrev_b32 v100, 16, v74\n"
    "v_and_b32 v101, s5, v74\n"
    "v_lshlrev_b32 v102, 16, v75\n"
    "v_and_b32 v103, s5, v75\n"
    "v_add_f32 v4, v4, v100\n"
    "v_fmac_f32 v10, v100, v100\n"
    "v_add_f32 v4, v4, v101\n"
    "v_fmac_f32 v10, v101, v101\n"
    "v_add_f32 v4, v4, v102\n"
    "v_fmac_f32 v10, v102, v102\n"
    "v_add_f32 v4, v4, v103\n"
    "v_fmac_f32 v10, v103, v103\n"
    "s_waitcnt lgkmcnt(3)\n"
    "v_lshlrev_b32 v100, 16, v76\n"
    "v_and_b32 v101, s5, v76\n"
    "v_lshlrev_b32 v102, 16, v77\n"
    "v_and_b32 v103, s5, v77\n"
    "v_add_f32 v5, v5, v100\n"
    "v_fmac_f32 v11, v100, v100\n"
    "v_add_f32 v5, v5, v101\n"
    "v_fmac_f32 v11, v101, v101\n"
    "v_add_f32 v5, v5, v102\n"
    "v_fmac_f32 v11, v102, v102\n"
    "v_add_f32 v5, v5, v103\n"
    "v_fmac_f32 v11, v103, v103\n"
    "v_lshlrev_b32 v100, 16, v78\n"
    "v_and_b32 v101, s5, v78\n"
    "v_lshlrev_b32 v102, 16, v79\n"
    "v_and_b32 v103, s5, v79\n"
    "v_add_f32 v5, v5, v100\n"
    "v_fmac_f32 v11, v100, v100\n"
    "v_add_f32 v5, v5, v101\n"
    "v_fmac_f32 v11, v101, v101\n"
    "v_add_f32 v5, v5, v102\n"
    "s_waitcnt vmcnt(0)\n"
    "s_lshl_b32 s28, s12, 9\n"
    "s_add_u32 s28, s28, 92160\n"
    "s_barrier\n"
    "v_fmac_f32 v11, v102, v102\n"
    "v_add_f32 v5, v5, v103\n"
    "v_fmac_f32 v11, v103, v103\n"
    "s_waitcnt lgkmcnt(2)\n"
    "v_lshlrev_b32 v100, 16, v80\n"
    "v_and_b32 v101, s5, v80\n"
    "v_lshlrev_b32 v102, 16, v81\n"
    "v_and_b32 v103, s5, v81\n"
    "v_add_f32 v4, v4, v100\n"
    "v_fmac_f32 v10, v100, v100\n"
    "v_add_f32 v4, v4, v101\n"
    "v_fmac_f32 v10, v101, v101\n"
    "v_add_f32 v4, v4, v102\n"
    "v_fmac_f32 v10, v102, v102\n"
    "v_add_f32 v4, v4, v103\n"
    "v_fmac_f32 v10, v103, v103\n"
    "v_lshlrev_b32 v100, 16, v82\n"
    "v_and_b32 v101, s5, v82\n"
    "v_lshlrev_b32 v102, 16, v83\n"
    "v_and_b32 v103, s5, v83\n"
    "v_add_f32 v4, v4, v100\n"
    "v_fmac_f32 v10, v100, v100\n"
    "v_add_f32 v4, v4, v101\n"
    "v_fmac_f32 v10, v101, v101\n"
    "v_add_f32 v4, v4, v102\n"
    "v_fmac_f32 v10, v102, v102\n"
    "v_add_f32 v4, v4, v103\n"
    "v_fmac_f32 v10, v103, v103\n"
    "s_waitcnt lgkmcnt(1)\n"
    "v_lshlrev_b32 v100, 16, v84\n"
    "v_and_b32 v101, s5, v84\n"
    "v_lshlrev_b32 v102, 16, v85\n"
    "v_and_b32 v103, s5, v85\n"
    "v_add_f32 v5, v5, v100\n"
    "v_fmac_f32 v11, v100, v100\n"
    "v_add_f32 v5, v5, v101\n"
    "v_fmac_f32 v11, v101, v101\n"
    "v_add_f32 v5, v5, v102\n"
    "v_fmac_f32 v11, v102, v102\n"
    "v_add_f32 v5, v5, v103\n"
    "v_fmac_f32 v11, v103, v103\n"
    "v_lshlrev_b32 v100, 16, v86\n"
    "v_and_b32 v101, s5, v86\n"
    "v_lshlrev_b32 v102, 16, v87\n"
    "v_and_b32 v103, s5, v87\n"
    "v_add_f32 v5, v5, v100\n"
    "v_fmac_f32 v11, v100, v100\n"
    "v_add_f32 v5, v5, v101\n"
    "v_fmac_f32 v11, v101, v101\n"
    "v_add_f32 v5, v5, v102\n"
    "v_fmac_f32 v11, v102, v102\n"
    "v_add_f32 v5, v5, v103\n"
    "v_fmac_f32 v11, v103, v103\n"
    "s_waitcnt lgkmcnt(0)\n"
    "v_lshlrev_b32 v100, 16, v88\n"
    "v_and_b32 v101, s5, v88\n"
    "v_lshlrev_b32 v102, 16, v89\n"
    "v_and_b32 v103, s5, v89\n"
    "s_barrier\n"
    "v_add_f32 v6, v6, v100\n"
    "v_fmac_f32 v12, v100, v100\n"
    "v_add_f32 v6, v6, v101\n"
    "v_fmac_f32 v12, v101, v101\n"
    "v_add_f32 v6, v6, v102\n"
    "v_fmac_f32 v12, v102, v102\n"
    "v_add_f32 v6, v6, v103\n"
    "v_fmac_f32 v12, v103, v103\n"
    "v_lshlrev_b32 v100, 16, v90\n"
    "v_and_b32 v101, s5, v90\n"
    "v_lshlrev_b32 v102, 16, v91\n"
    "v_and_b32 v103, s5, v91\n"
    "v_add_f32 v6, v6, v100\n"
    "v_fmac_f32 v12, v100, v100\n"
    "v_add_f32 v6, v6, v101\n"
    "v_fmac_f32 v12, v101, v101\n"
    "v_add_f32 v6, v6, v102\n"
    "v_fmac_f32 v12, v102, v102\n"
    "v_add_f32 v6, v6, v103\n"
    "v_fmac_f32 v12, v103, v103\n"
    "v_lshlrev_b32 v58, 16, v56\n"
    "v_and_b32 v59, 0xffff0000, v56\n"
    "v_lshl_add_u32 v57, v0, 3, s28\n"
    "v_mul_f32 v58, 0x3eeff558, v58\n"
    "v_mul_f32 v59, 0x3eeff558, v59\n"
    "v_mov_b32 v44, v150\n"
    "v_mov_b32 v46, v150\n"
    "ds_write_b64 v57, v[58:59]\n"
    "s_nop 0\n"
    "v_permlane16_swap_b32 v44, v46\n"
    "v_mov_b32 v48, v44\n"
    "v_mov_b32 v50, v46\n"
    "s_nop 0\n"
    "v_permlane32_swap_b32 v44, v48\n"
    "v_permlane32_swap_b32 v46, v50\n"
    "v_lshlrev_b32 v150, 16, v44\n"
    "v_lshlrev_b32 v151, 16, v46\n"
    "v_lshlrev_b32 v152, 16, v48\n"
    "v_mul_f32 v44, 0x3fb8aa3b, v152\n"
    "v_exp_f32 v44, v44\n"
    "s_nop 0\n"
    "v_cvt_pk_bf16_f32 v44, v44, v44\n"
    "v_lshlrev_b32 v44, 16, v44\n"
    "v_mul_f32 v45, v44, v150\n"
    "v_mul_f32 v46, v44, v151\n"
    "v_mul_f32 v45, 0x3fb8aa3b, v45\n"
    "s_barrier\n"
    "v_mul_f32 v46, 0x3e22f983, v46\n"
    "v_cvt_f32_u32 v47, v129\n"
    "v_mul_f32 v48, 0x41a00000, v47\n"
    "v_mul_f32 v49, 0x429e0000, v47\n"
    "v_mul_f32 v50, 1.0, v45\n"
    "v_mul_f32 v137, 1.0, v46\n"
    "v_exp_f32 v50, v50\n"
    "v_fract_f32 v137, v137\n"
    "v_cos_f32 v136, v137\n"
    "v_sin_f32 v137, v137\n"
    "v_mul_f32 v136, v50, v136\n"
    "v_mul_f32 v137, v50, v137\n"
    "v_mul_f32 v50, 0x41980000, v45\n"
    "v_mul_f32 v139, 0x41980000, v46\n"
    "v_exp_f32 v50, v50\n"
    "v_fract_f32 v139, v139\n"
    "v_cos_f32 v138, v139\n"
    "v_sin_f32 v139, v139\n"
    "v_mul_f32 v138, v50, v138\n"
    "v_mul_f32 v139, v50, v139\n"
    "v_mul_f32 v50, 0x41a00000, v45\n"
    "v_mul_f32 v141, 0x41a00000, v46\n"
    "ds_read_b128 v[92:95], v110 offset:30720\n"
    "ds_read_b128 v[96:99], v110 offset:31744\n"
    "ds_read_b128 v[60:63], v110 offset:40960\n"
    "ds_read_b128 v[64:67], v110 offset:41984\n"
    "ds_read_b128 v[68:71], v113 offset:20480\n"
    "v_exp_f32 v50, v50\n"
    "v_fract_f32 v141, v141\n"
    "v_cos_f32 v140, v141\n"
    "v_sin_f32 v141, v141\n"
    "v_mul_f32 v140, v50, v140\n"
    "v_mul_f32 v141, v50, v141\n"
    "v_mul_f32 v50, v48, v45\n"
    "v_mul_f32 v143, v48, v46\n"
    "v_exp_f32 v50, v50\n"
    "v_fract_f32 v143, v143\n"
    "s_barrier\n"
    "s_waitcnt lgkmcnt(4)\n"
    "v_lshlrev_b32 v100, 16, v92\n"
    "v_and_b32 v101, s5, v92\n"
    "v_lshlrev_b32 v102, 16, v93\n"
    "v_and_b32 v103, s5, v93\n"
    "v_add_f32 v4, v4, v100\n"
    "v_fmac_f32 v10, v100, v100\n"
    "v_add_f32 v4, v4, v101\n"
    "v_fmac_f32 v10, v101, v101\n"
    "v_add_f32 v4, v4, v102\n"
    "v_fmac_f32 v10, v102, v102\n"
    "v_add_f32 v4, v4, v103\n"
    "v_fmac_f32 v10, v103, v103\n"
    "v_lshlrev_b32 v100, 16, v94\n"
    "v_and_b32 v101, s5, v94\n"
    "v_lshlrev_b32 v102, 16, v95\n"
    "v_and_b32 v103, s5, v95\n"
    "v_add_f32 v4, v4, v100\n"
    "v_fmac_f32 v10, v100, v100\n"
    "v_add_f32 v4, v4, v101\n"
    "v_fmac_f32 v10, v101, v101\n"
    "v_add_f32 v4, v4, v102\n"
    "v_fmac_f32 v10, v102, v102\n"
    "v_add_f32 v4, v4, v103\n"
    "v_fmac_f32 v10, v103, v103\n"
    "s_waitcnt lgkmcnt(3)\n"
    "v_lshlrev_b32 v100, 16, v96\n"
    "v_and_b32 v101, s5, v96\n"
    "v_lshlrev_b32 v102, 16, v97\n"
    "v_and_b32 v103, s5, v97\n"
    "v_add_f32 v5, v5, v100\n"
    "v_fmac_f32 v11, v100, v100\n"
    "v_add_f32 v5, v5, v101\n"
    "v_fmac_f32 v11, v101, v101\n"
    "v_add_f32 v5, v5, v102\n"
    "v_fmac_f32 v11, v102, v102\n"
    "v_add_f32 v5, v5, v103\n"
    "v_fmac_f32 v11, v103, v103\n"
    "v_lshlrev_b32 v100, 16, v98\n"
    "v_and_b32 v101, s5, v98\n"
    "v_lshlrev_b32 v102, 16, v99\n"
    "v_and_b32 v103, s5, v99\n"
    "v_add_f32 v5, v5, v100\n"
    "v_fmac_f32 v11, v100, v100\n"
    "v_add_f32 v5, v5, v101\n"
    "v_fmac_f32 v11, v101, v101\n"
    "v_add_f32 v5, v5, v102\n"
    "v_fmac_f32 v11, v102, v102\n"
    "v_add_f32 v5, v5, v103\n"
    "v_fmac_f32 v11, v103, v103\n"
    "s_waitcnt lgkmcnt(2)\n"
    "v_lshlrev_b32 v100, 16, v60\n"
    "v_and_b32 v101, s5, v60\n"
    "s_barrier\n"
    "v_lshlrev_b32 v102, 16, v61\n"
    "v_and_b32 v103, s5, v61\n"
    "v_add_f32 v4, v4, v100\n"
    "v_fmac_f32 v10, v100, v100\n"
    "v_add_f32 v4, v4, v101\n"
    "v_fmac_f32 v10, v101, v101\n"
    "v_add_f32 v4, v4, v102\n"
    "v_fmac_f32 v10, v102, v102\n"
    "v_add_f32 v4, v4, v103\n"
    "v_fmac_f32 v10, v103, v103\n"
    "v_lshlrev_b32 v100, 16, v62\n"
    "v_and_b32 v101, s5, v62\n"
    "v_lshlrev_b32 v102, 16, v63\n"
    "v_and_b32 v103, s5, v63\n"
    "v_add_f32 v4, v4, v100\n"
    "v_fmac_f32 v10, v100, v100\n"
    "v_add_f32 v4, v4, v101\n"
    "v_fmac_f32 v10, v101, v101\n"
    "v_add_f32 v4, v4, v102\n"
    "v_fmac_f32 v10, v102, v102\n"
    "v_add_f32 v4, v4, v103\n"
    "v_fmac_f32 v10, v103, v103\n"
    "s_waitcnt lgkmcnt(1)\n"
    "v_lshlrev_b32 v100, 16, v64\n"
    "v_and_b32 v101, s5, v64\n"
    "v_lshlrev_b32 v102, 16, v65\n"
    "v_and_b32 v103, s5, v65\n"
    "v_add_f32 v5, v5, v100\n"
    "v_fmac_f32 v11, v100, v100\n"
    "v_add_f32 v5, v5, v101\n"
    "v_fmac_f32 v11, v101, v101\n"
    "v_add_f32 v5, v5, v102\n"
    "v_fmac_f32 v11, v102, v102\n"
    "v_add_f32 v5, v5, v103\n"
    "v_fmac_f32 v11, v103, v103\n"
    "v_lshlrev_b32 v100, 16, v66\n"
    "v_and_b32 v101, s5, v66\n"
    "v_lshlrev_b32 v102, 16, v67\n"
    "v_and_b32 v103, s5, v67\n"
    "v_add_f32 v5, v5, v100\n"
    "v_fmac_f32 v11, v100, v100\n"
    "v_add_f32 v5, v5, v101\n"
    "v_fmac_f32 v11, v101, v101\n"
    "v_add_f32 v5, v5, v102\n"
    "v_fmac_f32 v11, v102, v102\n"
    "v_add_f32 v5, v5, v103\n"
    "v_fmac_f32 v11, v103, v103\n"
    "s_waitcnt lgkmcnt(0)\n"
    "v_lshlrev_b32 v100, 16, v68\n"
    "v_and_b32 v101, s5, v68\n"
    "v_lshlrev_b32 v102, 16, v69\n"
    "v_and_b32 v103, s5, v69\n"
    "v_add_f32 v6, v6, v100\n"
    "v_fmac_f32 v12, v100, v100\n"
    "v_add_f32 v6, v6, v101\n"
    "v_fmac_f32 v12, v101, v101\n"
    "v_add_f32 v6, v6, v102\n"
    "s_barrier\n"
    "v_fmac_f32 v12, v102, v102\n"
    "v_add_f32 v6, v6, v103\n"
    "v_fmac_f32 v12, v103, v103\n"
    "v_lshlrev_b32 v100, 16, v70\n"
    "v_and_b32 v101, s5, v70\n"
    "v_lshlrev_b32 v102, 16, v71\n"
    "v_and_b32 v103, s5, v71\n"
    "v_add_f32 v6, v6, v100\n"
    "v_fmac_f32 v12, v100, v100\n"
    "v_add_f32 v6, v6, v101\n"
    "v_fmac_f32 v12, v101, v101\n"
    "v_add_f32 v6, v6, v102\n"
    "v_fmac_f32 v12, v102, v102\n"
    "v_add_f32 v6, v6, v103\n"
    "v_fmac_f32 v12, v103, v103\n"
    "v_cos_f32 v142, v143\n"
    "v_sin_f32 v143, v143\n"
    "v_mul_f32 v142, v50, v142\n"
    "v_mul_f32 v143, v50, v143\n"
    "v_mul_f32 v50, v49, v45\n"
    "v_mul_f32 v145, v49, v46\n"
    "v_exp_f32 v50, v50\n"
    "v_fract_f32 v145, v145\n"
    "v_cos_f32 v144, v145\n"
    "v_sin_f32 v145, v145\n"
    "v_mul_f32 v144, v50, v144\n"
    "v_mul_f32 v145, v50, v145\n"
    "v_mul_f32 v50, 0x439e0000, v45\n"
    "v_mul_f32 v147, 0x439e0000, v46\n"
    "v_exp_f32 v50, v50\n"
    "v_fract_f32 v147, v147\n"
    "v_cos_f32 v146, v147\n"
    "v_sin_f32 v147, v147\n"
    "v_mul_f32 v146, v50, v146\n"
    "v_mul_f32 v147, v50, v147\n"
    "v_add_f32 v44, -1.0, v136\n"
    "v_mul_f32 v45, v150, v150\n"
    "v_fmac_f32 v45, v151, v151\n"
    "v_rcp_f32 v45, v45\n"
    "v_mul_f32 v148, v44, v150\n"
    "s_barrier\n"
    "v_fmac_f32 v148, v137, v151\n"
    "v_mul_f32 v149, v137, v150\n"
    "v_fma_f32 v149, -v44, v151, v149\n"
    "v_add_f32 v45, v45, v45\n"
    "v_mul_f32 v148, v148, v45\n"
    "v_mul_f32 v149, v149, v45\n"
    "s_mul_i32 s28, s14, 5120\n"
    "s_lshl_b32 s29, s15, 9\n"
    "s_add_u32 s28, s28, s29\n"
    "s_add_u32 s28, s28, 81920\n"
    "v_bfe_u32 v44, v0, 2, 2\n"
    "v_xor_b32 v44, v44, v129\n"
    "v_lshl_add_u32 v127, v128, 3, s28\n"
    "v_lshl_add_u32 v127, v44, 3, v127\n"
    "v_and_b32 v44, 8, v0\n"
    "v_lshlrev_b32 v44, 4, v44\n"
    "v_xor_b32 v127, v127, v44\n"
    "s_mul_i32 s28, s14, 10240\n"
    "s_add_u32 s28, s28, 81920\n"
    "v_and_b32 v44, 15, v0\n"
    "v_lshl_add_u32 v124, v44, 3, s28\n"
    "v_lshrrev_b32 v44, 4, v0\n"
    "v_lshl_add_u32 v124, v44, 8, v124\n"
    "v_and_b32 v44, 1, v44\n"
    "v_mul_u32_u24 v44, 144, v44\n"
    "v_xor_b32 v124, v124, v44\n"
    "ds_read_b128 v[72:75], v111 offset:0\n"
    "ds_read_b128 v[76:79], v111 offset:1024\n"
    "ds_read_b128 v[80:83], v111 offset:10240\n"
    "v_xor_b32 v125, 136, v124\n"
    "v_bfe_u32 v44, v0, 1, 2\n"
    "v_lshrrev_b32 v45, 4, v0\n"
    "v_xor_b32 v44, v44, v45\n"
    "v_and_b32 v45, 1, v0\n"
    "v_lshlrev_b32 v45, 4, v45\n"
    "v_lshl_add_u32 v154, v44, 5, v45\n"
    ".Lpage1:\n"
    "s_cbranch_execz .Ltouch_next\n"
    "v_lshrrev_b32 v44, 5, v0\n"
    "v_bfe_u32 v45, v0, 3, 2\n"
    "v_lshl_add_u32 v44, v44, 3, v45\n"
    "v_mul_u32_u24 v44, 9216, v44\n"
    "s_barrier\n"
    "s_waitcnt lgkmcnt(2)\n"
    "v_lshlrev_b32 v100, 16, v72\n"
    "v_and_b32 v101, s5, v72\n"
    "v_lshlrev_b32 v102, 16, v73\n"
    "v_and_b32 v103, s5, v73\n"
    "v_add_f32 v4, v4, v100\n"
    "v_fmac_f32 v10, v100, v100\n"
    "v_add_f32 v4, v4, v101\n"
    "v_fmac_f32 v10, v101, v101\n"
    "v_add_f32 v4, v4, v102\n"
    "v_fmac_f32 v10, v102, v102\n"
    "v_add_f32 v4, v4, v103\n"
    "v_fmac_f32 v10, v103, v103\n"
    "v_lshlrev_b32 v100, 16, v74\n"
    "v_and_b32 v101, s5, v74\n"
    "v_lshlrev_b32 v102, 16, v75\n"
    "v_and_b32 v103, s5, v75\n"
    "v_add_f32 v4, v4, v100\n"
    "v_fmac_f32 v10, v100, v100\n"
    "v_add_f32 v4, v4, v101\n"
    "v_fmac_f32 v10, v101, v101\n"
    "v_add_f32 v4, v4, v102\n"
    "ds_read_b128 v[84:87], v111 offset:11264\n"
    "ds_read_b128 v[88:91], v113 offset:40960\n"
    "v_fmac_f32 v10, v102, v102\n"
    "v_add_f32 v4, v4, v103\n"
    "v_fmac_f32 v10, v103, v103\n"
    "s_waitcnt lgkmcnt(3)\n"
    "v_lshlrev_b32 v100, 16, v76\n"
    "v_and_b32 v101, s5, v76\n"
    "v_lshlrev_b32 v102, 16, v77\n"
    "v_and_b32 v103, s5, v77\n"
    "v_add_f32 v5, v5, v100\n"
    "v_fmac_f32 v11, v100, v100\n"
    "v_add_f32 v5, v5, v101\n"
    "v_fmac_f32 v11, v101, v101\n"
    "v_add_f32 v5, v5, v102\n"
    "v_fmac_f32 v11, v102, v102\n"
    "v_add_f32 v5, v5, v103\n"
    "v_fmac_f32 v11, v103, v103\n"
    "v_lshlrev_b32 v100, 16, v78\n"
    "v_and_b32 v101, s5, v78\n"
    "v_lshlrev_b32 v102, 16, v79\n"
    "v_and_b32 v103, s5, v79\n"
    "v_add_f32 v5, v5, v100\n"
    "v_fmac_f32 v11, v100, v100\n"
    "v_add_f32 v5, v5, v101\n"
    "v_fmac_f32 v11, v101, v101\n"
    "v_add_f32 v5, v5, v102\n"
    "s_barrier\n"
    "v_fmac_f32 v11, v102, v102\n"
    "v_add_f32 v5, v5, v103\n"
    "v_fmac_f32 v11, v103, v103\n"
    "s_waitcnt lgkmcnt(2)\n"
    "v_lshlrev_b32 v100, 16, v80\n"
    "v_and_b32 v101, s5, v80\n"
    "v_lshlrev_b32 v102, 16, v81\n"
    "v_and_b32 v103, s5, v81\n"
    "v_add_f32 v4, v4, v100\n"
    "v_fmac_f32 v10, v100, v100\n"
    "v_add_f32 v4, v4, v101\n"
    "v_fmac_f32 v10, v101, v101\n"
    "v_add_f32 v4, v4, v102\n"
    "v_fmac_f32 v10, v102, v102\n"
    "v_add_f32 v4, v4, v103\n"
    "v_fmac_f32 v10, v103, v103\n"
    "v_lshlrev_b32 v100, 16, v82\n"
    "v_and_b32 v101, s5, v82\n"
    "v_lshlrev_b32 v102, 16, v83\n"
    "v_and_b32 v103, s5, v83\n"
    "v_add_f32 v4, v4, v100\n"
    "v_fmac_f32 v10, v100, v100\n"
    "v_add_f32 v4, v4, v101\n"
    "v_fmac_f32 v10, v101, v101\n"
    "v_add_f32 v4, v4, v102\n"
    "v_fmac_f32 v10, v102, v102\n"
    "v_add_f32 v4, v4, v103\n"
    "v_fmac_f32 v10, v103, v103\n"
    "s_waitcnt lgkmcnt(1)\n"
    "v_lshlrev_b32 v100, 16, v84\n"
    "v_and_b32 v101, s5, v84\n"
    "v_lshlrev_b32 v102, 16, v85\n"
    "v_and_b32 v103, s5, v85\n"
    "v_add_f32 v5, v5, v100\n"
    "v_fmac_f32 v11, v100, v100\n"
    "v_add_f32 v5, v5, v101\n"
    "v_fmac_f32 v11, v101, v101\n"
    "v_add_f32 v5, v5, v102\n"
    "v_fmac_f32 v11, v102, v102\n"
    "v_add_f32 v5, v5, v103\n"
    "v_fmac_f32 v11, v103, v103\n"
    "v_lshlrev_b32 v100, 16, v86\n"
    "v_and_b32 v101, s5, v86\n"
    "v_lshlrev_b32 v102, 16, v87\n"
    "v_and_b32 v103, s5, v87\n"
    "v_add_f32 v5, v5, v100\n"
    "v_fmac_f32 v11, v100, v100\n"
    "v_add_f32 v5, v5, v101\n"
    "v_fmac_f32 v11, v101, v101\n"
    "v_add_f32 v5, v5, v102\n"
    "v_fmac_f32 v11, v102, v102\n"
    "v_add_f32 v5, v5, v103\n"
    "v_fmac_f32 v11, v103, v103\n"
    "s_waitcnt lgkmcnt(0)\n"
    "v_lshlrev_b32 v100, 16, v88\n"
    "v_and_b32 v101, s5, v88\n"
    "v_lshlrev_b32 v102, 16, v89\n"
    "v_and_b32 v103, s5, v89\n"
    "s_barrier\n"
    "v_add_f32 v6, v6, v100\n"
    "v_fmac_f32 v12, v100, v100\n"
    "v_add_f32 v6, v6, v101\n"
    "v_fmac_f32 v12, v101, v101\n"
    "v_add_f32 v6, v6, v102\n"
    "v_fmac_f32 v12, v102, v102\n"
    "v_add_f32 v6, v6, v103\n"
    "v_fmac_f32 v12, v103, v103\n"
    "v_lshlrev_b32 v100, 16, v90\n"
    "v_and_b32 v101, s5, v90\n"
    "v_lshlrev_b32 v102, 16, v91\n"
    "v_and_b32 v103, s5, v91\n"
    "v_add_f32 v6, v6, v100\n"
    "v_fmac_f32 v12, v100, v100\n"
    "v_add_f32 v6, v6, v101\n"
    "v_fmac_f32 v12, v101, v101\n"
    "v_add_f32 v6, v6, v102\n"
    "v_fmac_f32 v12, v102, v102\n"
    "v_add_f32 v6, v6, v103\n"
    "v_fmac_f32 v12, v103, v103\n"
    "v_add_u32 v154, v154, v44\n"
    "v_add_u32 v155, 146432, v154\n"
    "s_add_u32 s28, s10, 3\n"
    "v_sub_u32 v135, s28, v129\n"
    "v_lshrrev_b32 v135, 2, v135\n"
    "ds_read_b128 v[92:95], v111 offset:20480\n"
    "ds_read_b128 v[96:99], v111 offset:21504\n"
    "ds_read_b128 v[60:63], v111 offset:30720\n"
    "ds_read_b128 v[64:67], v111 offset:31744\n"
    "ds_read_b128 v[68:71], v113 offset:61440\n"
    "v_sub_u32 v135, 7, v135\n"
    "s_mov_b32 s28, 162432\n"
    "v_lshl_add_u32 v163, v129, 4, s28\n"
    "s_barrier\n"
    "s_waitcnt lgkmcnt(4)\n"
    "v_lshlrev_b32 v100, 16, v92\n"
    "v_and_b32 v101, s5, v92\n"
    "v_lshlrev_b32 v102, 16, v93\n"
    "v_and_b32 v103, s5, v93\n"
    "v_add_f32 v4, v4, v100\n"
    "v_fmac_f32 v10, v100, v100\n"
    "v_add_f32 v4, v4, v101\n"
    "v_fmac_f32 v10, v101, v101\n"
    "v_add_f32 v4, v4, v102\n"
    "v_fmac_f32 v10, v102, v102\n"
    "v_add_f32 v4, v4, v103\n"
    "v_fmac_f32 v10, v103, v103\n"
    "v_lshlrev_b32 v100, 16, v94\n"
    "v_and_b32 v101, s5, v94\n"
    "v_lshlrev_b32 v102, 16, v95\n"
    "v_and_b32 v103, s5, v95\n"
    "v_add_f32 v4, v4, v100\n"
    "v_fmac_f32 v10, v100, v100\n"
    "v_add_f32 v4, v4, v101\n"
    "v_fmac_f32 v10, v101, v101\n"
    "v_add_f32 v4, v4, v102\n"
    "v_fmac_f32 v10, v102, v102\n"
    "v_add_f32 v4, v4, v103\n"
    "v_fmac_f32 v10, v103, v103\n"
    "s_waitcnt lgkmcnt(3)\n"
    "v_lshlrev_b32 v100, 16, v96\n"
    "v_and_b32 v101, s5, v96\n"
    "v_lshlrev_b32 v102, 16, v97\n"
    "v_and_b32 v103, s5, v97\n"
    "v_add_f32 v5, v5, v100\n"
    "v_fmac_f32 v11, v100, v100\n"
    "v_add_f32 v5, v5, v101\n"
    "v_fmac_f32 v11, v101, v101\n"
    "v_add_f32 v5, v5, v102\n"
    "v_fmac_f32 v11, v102, v102\n"
    "v_add_f32 v5, v5, v103\n"
    "v_fmac_f32 v11, v103, v103\n"
    "v_lshlrev_b32 v100, 16, v98\n"
    "v_and_b32 v101, s5, v98\n"
    "v_lshlrev_b32 v102, 16, v99\n"
    "v_and_b32 v103, s5, v99\n"
    "v_add_f32 v5, v5, v100\n"
    "v_fmac_f32 v11, v100, v100\n"
    "v_add_f32 v5, v5, v101\n"
    "v_fmac_f32 v11, v101, v101\n"
    "v_add_f32 v5, v5, v102\n"
    "v_fmac_f32 v11, v102, v102\n"
    "v_add_f32 v5, v5, v103\n"
    "v_fmac_f32 v11, v103, v103\n"
    "s_waitcnt lgkmcnt(2)\n"
    "v_lshlrev_b32 v100, 16, v60\n"
    "s_barrier\n"
    "v_and_b32 v101, s5, v60\n"
    "v_lshlrev_b32 v102, 16, v61\n"
    "v_and_b32 v103, s5, v61\n"
    "v_add_f32 v4, v4, v100\n"
    "v_fmac_f32 v10, v100, v100\n"
    "v_add_f32 v4, v4, v101\n"
    "v_fmac_f32 v10, v101, v101\n"
    "v_add_f32 v4, v4, v102\n"
    "v_fmac_f32 v10, v102, v102\n"
    "v_add_f32 v4, v4, v103\n"
    "v_fmac_f32 v10, v103, v103\n"
    "v_lshlrev_b32 v100, 16, v62\n"
    "v_and_b32 v101, s5, v62\n"
    "v_lshlrev_b32 v102, 16, v63\n"
    "v_and_b32 v103, s5, v63\n"
    "v_add_f32 v4, v4, v100\n"
    "v_fmac_f32 v10, v100, v100\n"
    "v_add_f32 v4, v4, v101\n"
    "v_fmac_f32 v10, v101, v101\n"
    "v_add_f32 v4, v4, v102\n"
    "v_fmac_f32 v10, v102, v102\n"
    "v_add_f32 v4, v4, v103\n"
    "v_fmac_f32 v10, v103, v103\n"
    "s_waitcnt lgkmcnt(1)\n"
    "v_lshlrev_b32 v100, 16, v64\n"
    "v_and_b32 v101, s5, v64\n"
    "v_lshlrev_b32 v102, 16, v65\n"
    "v_and_b32 v103, s5, v65\n"
    "v_add_f32 v5, v5, v100\n"
    "v_fmac_f32 v11, v100, v100\n"
    "v_add_f32 v5, v5, v101\n"
    "v_fmac_f32 v11, v101, v101\n"
    "v_add_f32 v5, v5, v102\n"
    "v_fmac_f32 v11, v102, v102\n"
    "v_add_f32 v5, v5, v103\n"
    "v_fmac_f32 v11, v103, v103\n"
    "v_lshlrev_b32 v100, 16, v66\n"
    "v_and_b32 v101, s5, v66\n"
    "v_lshlrev_b32 v102, 16, v67\n"
    "v_and_b32 v103, s5, v67\n"
    "v_add_f32 v5, v5, v100\n"
    "v_fmac_f32 v11, v100, v100\n"
    "v_add_f32 v5, v5, v101\n"
    "v_fmac_f32 v11, v101, v101\n"
    "v_add_f32 v5, v5, v102\n"
    "v_fmac_f32 v11, v102, v102\n"
    "v_add_f32 v5, v5, v103\n"
    "v_fmac_f32 v11, v103, v103\n"
    "s_waitcnt lgkmcnt(0)\n"
    "v_lshlrev_b32 v100, 16, v68\n"
    "v_and_b32 v101, s5, v68\n"
    "v_lshlrev_b32 v102, 16, v69\n"
    "v_and_b32 v103, s5, v69\n"
    "v_add_f32 v6, v6, v100\n"
    "v_fmac_f32 v12, v100, v100\n"
    "v_add_f32 v6, v6, v101\n"
    "v_fmac_f32 v12, v101, v101\n"
    "s_barrier\n"
    "v_add_f32 v6, v6, v102\n"
    "v_fmac_f32 v12, v102, v102\n"
    "v_add_f32 v6, v6, v103\n"
    "v_fmac_f32 v12, v103, v103\n"
    "v_lshlrev_b32 v100, 16, v70\n"
    "v_and_b32 v101, s5, v70\n"
    "v_lshlrev_b32 v102, 16, v71\n"
    "v_and_b32 v103, s5, v71\n"
    "v_add_f32 v6, v6, v100\n"
    "v_fmac_f32 v12, v100, v100\n"
    "v_add_f32 v6, v6, v101\n"
    "v_fmac_f32 v12, v101, v101\n"
    "v_add_f32 v6, v6, v102\n"
    "v_fmac_f32 v12, v102, v102\n"
    "v_add_f32 v6, v6, v103\n"
    "v_fmac_f32 v12, v103, v103\n"
    "v_mov_b32 v100, v6\n"
    "v_mov_b32 v101, v12\n"
    "v_lshrrev_b32 v108, 3, v0\n"
    "v_and_b32 v109, 1, v0\n"
    "v_permlane32_swap_b32 v100, v6\n"
    "v_permlane32_swap_b32 v101, v12\n"
    "v_add_f32 v6, v6, v100\n"
    "v_add_f32 v12, v12, v101\n"
    "v_add_f32_dpp v4, v4, v4 quad_perm:[1,0,3,2] row_mask:0xf bank_mask:0xf\n"
    "v_add_f32_dpp v10, v10, v10 quad_perm:[1,0,3,2] row_mask:0xf bank_mask:0xf\n"
    "v_add_f32_dpp v5, v5, v5 quad_perm:[1,0,3,2] row_mask:0xf bank_mask:0xf\n"
    "v_add_f32_dpp v11, v11, v11 quad_perm:[1,0,3,2] row_mask:0xf bank_mask:0xf\n"
    "v_add_f32_dpp v6, v6, v6 quad_perm:[1,0,3,2] row_mask:0xf bank_mask:0xf\n"
    "v_add_f32_dpp v12, v12, v12 quad_perm:[1,0,3,2] row_mask:0xf bank_mask:0xf\n"
    "v_add_f32_dpp v4, v4, v4 quad_perm:[2,3,0,1] row_mask:0xf bank_mask:0xf\n"
    "v_add_f32_dpp v10, v10, v10 quad_perm:[2,3,0,1] row_mask:0xf bank_mask:0xf\n"
    "v_add_f32_dpp v5, v5, v5 quad_perm:[2,3,0,1] row_mask:0xf bank_mask:0xf\n"
    "v_add_f32_dpp v11, v11, v11 quad_perm:[2,3,0,1] row_mask:0xf bank_mask:0xf\n"
    "v_add_f32_dpp v6, v6, v6 quad_perm:[2,3,0,1] row_mask:0xf bank_mask:0xf\n"
    "v_add_f32_dpp v12, v12, v12 quad_perm:[2,3,0,1] row_mask:0xf bank_mask:0xf\n"
    "v_add_f32_dpp v4, v4, v4 row_half_mirror row_mask:0xf bank_mask:0xf\n"
    "v_add_f32_dpp v10, v10, v10 row_half_mirror row_mask:0xf bank_mask:0xf\n"
    "v_add_f32_dpp v5, v5, v5 row_half_mirror row_mask:0xf bank_mask:0xf\n"
    "v_add_f32_dpp v11, v11, v11 row_half_mirror row_mask:0xf bank_mask:0xf\n"
    "v_add_f32_dpp v6, v6, v6 row_half_mirror row_mask:0xf bank_mask:0xf\n"
    "v_add_f32_dpp v12, v12, v12 row_half_mirror row_mask:0xf bank_mask:0xf\n"
    "s_lshl_b32 s30, s12, 4\n"
    "s_lshl_b32 s31, s12, 2\n"
    "s_add_u32 s31, s31, 64\n"
    "v_lshl_add_u32 v100, v109, 3, v108\n"
    "v_add_u32 v100, s30, v100\n"
    "s_barrier\n"
    "s_mov_b32 exec_lo, 0x02020202\n"
    "s_mov_b32 exec_hi, 0x02020202\n"
    "v_mov_b32 v4, v5\n"
    "v_mov_b32 v10, v11\n"
    "s_mov_b64 exec, 0x04040404\n"
    "v_mov_b32 v4, v6\n"
    "v_mov_b32 v10, v12\n"
    "v_add_u32 v100, s31, v108\n"
    "s_mov_b64 exec, -1\n"
    "s_mov_b32 s30, 161792\n"
    "s_mov_b32 s31, 162432\n"
    "v_lshl_add_u32 v101, v100, 3, s30\n"
    "v_lshl_add_u32 v102, v100, 2, s31\n"
    "v_mul_f32 v104, 0x3ae38e39, v4\n"
    "v_mul_f32 v105, 0x3ae38e39, v10\n"
    "v_fma_f32 v105, -v104, v104, v105\n"
    "v_max_f32 v105, 0, v105\n"
    "v_add_f32 v106, 0x3727c5ac, v105\n"
    "v_rsq_f32 v106, v106\n"
    "s_nop 0\n"
    "v_mul_f32 v107, v105, v106\n"
    "v_fmaak_f32 v107, v107, v106, 0x3727c5ac\n"
    "v_rsq_f32 v107, v107\n"
    "v_mul_f32 v105, v106, v104\n"
    "v_mul_f32 v107, 0x3eeff558, v107\n"
    "v_mul_f32 v4, v107, v106\n"
    "v_mul_f32_e64 v5, -v107, v105\n"
    "s_mov_b32 exec_lo, 0x07070707\n"
    "s_mov_b32 exec_hi, 0x03030303\n"
    "ds_write_b64 v101, v[4:5]\n"
    "ds_write_b32 v102, v106\n"
    "ds_write_b32 v102, v105 offset:320\n"
    "s_mov_b64 exec, -1\n"
    "ds_read_b64 v[150:151], v1 offset:0\n"
    "ds_read_b64 v[152:153], v1 offset:2048\n"
    "ds_read_b64 v[156:157], v1 offset:4096\n"
    "ds_read_b64 v[158:159], v1 offset:6144\n"
    "ds_read_b64 v[160:161], v1 offset:8192\n"
    "s_waitcnt lgkmcnt(5)\n"
    "s_barrier\n"
    "s_setprio 2\n"
    "ds_read_b128 v[84:87], v118 offset:32768\n"
    "ds_read_b128 v[88:91], v119 offset:32768\n"
    "ds_read_b128 v[96:99], v119 offset:40960\n"
    "ds_read_b128 v[92:95], v118 offset:40960\n"
    "ds_read_b128 v[44:47], v116 offset:0\n"
    "ds_read_b128 v[48:51], v117 offset:0\n"
    "ds_read_b128 v[52:55], v116 offset:2048\n"
    "ds_read_b128 v[56:59], v117 offset:2048\n"
    "ds_read_b128 v[60:63], v116 offset:4096\n"
    "ds_read_b128 v[64:67], v117 offset:4096\n"
    "ds_read_b128 v[68:71], v116 offset:6144\n"
    "ds_read_b128 v[72:75], v117 offset:6144\n"
    "ds_read_b128 v[76:79], v116 offset:8192\n"
    "ds_read_b128 v[80:83], v117 offset:8192\n"
    "s_waitcnt lgkmcnt(8)\n"
    "s_barrier\n"
    "v_mfma_f32_16x16x32_bf16 v[4:7], v[44:47], v[84:87], 0\n"
    "ds_read_b128 v[100:103], v118 offset:49152\n"
    "v_mfma_f32_16x16x32_bf16 v[4:7], v[48:51], v[88:91], v[4:7]\n"
    "ds_read_b128 v[104:107], v119 offset:49152\n"
    "v_mfma_f32_16x16x32_bf16 v[8:11], v[48:51], v[96:99], 0\n"
    "ds_read_b128 v[112:115], v119 offset:57344\n"
    "v_mfma_f32_16x16x32_bf16 v[8:11], v[44:47], v[92:95], v[8:11]\n"
    "ds_read_b128 v[108:111], v118 offset:57344\n"
    "s_waitcnt lgkmcnt(10)\n"
    "v_mfma_f32_16x16x32_bf16 v[16:19], v[52:55], v[92:95], 0\n"
    "ds_read_b128 v[48:51], v117 offset:10240\n"
    "v_mfma_f32_16x16x32_bf16 v[16:19], v[56:59], v[96:99], v[16:19]\n"
    "ds_read_b128 v[44:47], v116 offset:10240\n"
    "v_mfma_f32_16x16x32_bf16 v[12:15], v[56:59], v[88:91], 0\n"
    "ds_read_b128 v[56:59], v117 offset:12288\n"
    "v_mfma_f32_16x16x32_bf16 v[12:15], v[52:55], v[84:87], v[12:15]\n"
    "ds_read_b128 v[52:55], v116 offset:12288\n"
    "s_waitcnt lgkmcnt(12)\n"
    "v_mfma_f32_16x16x32_bf16 v[20:23], v[60:63], v[84:87], 0\n"
    "v_mfma_f32_16x16x32_bf16 v[20:23], v[64:67], v[88:91], v[20:23]\n"
    "v_mfma_f32_16x16x32_bf16 v[24:27], v[64:67], v[96:99], 0\n"
    "ds_read_b128 v[64:67], v117 offset:14336\n"
    "v_mfma_f32_16x16x32_bf16 v[24:27], v[60:63], v[92:95], v[24:27]\n"
    "ds_read_b128 v[60:63], v116 offset:14336\n"
    "s_waitcnt lgkmcnt(12)\n"
    "v_mfma_f32_16x16x32_bf16 v[32:35], v[68:71], v[92:95], 0\n"
    "v_mfma_f32_16x16x32_bf16 v[32:35], v[72:75], v[96:99], v[32:35]\n"
    "v_mfma_f32_16x16x32_bf16 v[28:31], v[72:75], v[88:91], 0\n"
    "ds_read_b128 v[72:75], v117 offset:16384\n"
    "v_mfma_f32_16x16x32_bf16 v[28:31], v[68:71], v[84:87], v[28:31]\n"
    "ds_read_b128 v[68:71], v116 offset:16384\n"
    "s_waitcnt lgkmcnt(12)\n"
    "v_mfma_f32_16x16x32_bf16 v[36:39], v[76:79], v[84:87], 0\n"
    "v_mfma_f32_16x16x32_bf16 v[36:39], v[80:83], v[88:91], v[36:39]\n"
    "v_mfma_f32_16x16x32_bf16 v[40:43], v[80:83], v[96:99], 0\n"
    "ds_read_b128 v[80:83], v117 offset:18432\n"
    "s_waitcnt lgkmcnt(7)\n"
    "v_mfma_f32_16x16x32_bf16 v[40:43], v[76:79], v[92:95], v[40:43]\n"
    "s_barrier\n"
    "v_mfma_f32_16x16x32_bf16 v[4:7], v[44:47], v[100:103], v[4:7]\n"
    "ds_read_b128 v[84:87], v118 offset:0\n"
    "v_mfma_f32_16x16x32_bf16 v[4:7], v[48:51], v[104:107], v[4:7]\n"
    "ds_read_b128 v[88:91], v119 offset:0\n"
    "v_mfma_f32_16x16x32_bf16 v[8:11], v[48:51], v[112:115], v[8:11]\n"
    "ds_read_b128 v[76:79], v116 offset:18432\n"
    "v_mfma_f32_16x16x32_bf16 v[8:11], v[44:47], v[108:111], v[8:11]\n"
    "ds_read_b128 v[96:99], v119 offset:8192\n"
    "s_waitcnt lgkmcnt(9)\n"
    "v_mfma_f32_16x16x32_bf16 v[16:19], v[52:55], v[108:111], v[16:19]\n"
    "ds_read_b128 v[92:95], v118 offset:8192\n"
    "v_mfma_f32_16x16x32_bf16 v[16:19], v[56:59], v[112:115], v[16:19]\n"
    "ds_read_b128 v[48:51], v117 offset:20480\n"
    "v_mfma_f32_16x16x32_bf16 v[12:15], v[56:59], v[104:107], v[12:15]\n"
    "ds_read_b128 v[44:47], v116 offset:20480\n"
    "v_mfma_f32_16x16x32_bf16 v[12:15], v[52:55], v[100:103], v[12:15]\n"
    "ds_read_b128 v[56:59], v117 offset:22528\n"
    "s_waitcnt lgkmcnt(11)\n"
    "v_mfma_f32_16x16x32_bf16 v[20:23], v[60:63], v[100:103], v[20:23]\n"
    "ds_read_b128 v[52:55], v116 offset:22528\n"
    "v_mfma_f32_16x16x32_bf16 v[20:23], v[64:67], v[104:107], v[20:23]\n"
    "v_mfma_f32_16x16x32_bf16 v[24:27], v[64:67], v[112:115], v[24:27]\n"
    "ds_read_b128 v[64:67], v117 offset:24576\n"
    "v_mfma_f32_16x16x32_bf16 v[24:27], v[60:63], v[108:111], v[24:27]\n"
    "ds_read_b128 v[60:63], v116 offset:24576\n"
    "s_waitcnt lgkmcnt(12)\n"
    "v_mfma_f32_16x16x32_bf16 v[32:35], v[68:71], v[108:111], v[32:35]\n"
    "v_mfma_f32_16x16x32_bf16 v[32:35], v[72:75], v[112:115], v[32:35]\n"
    "v_mfma_f32_16x16x32_bf16 v[28:31], v[72:75], v[104:107], v[28:31]\n"
    "ds_read_b128 v[72:75], v117 offset:26624\n"
    "v_mfma_f32_16x16x32_bf16 v[28:31], v[68:71], v[100:103], v[28:31]\n"
    "ds_read_b128 v[68:71], v116 offset:26624\n"
    "s_waitcnt lgkmcnt(10)\n"
    "v_mfma_f32_16x16x32_bf16 v[36:39], v[76:79], v[100:103], v[36:39]\n"
    "v_mfma_f32_16x16x32_bf16 v[36:39], v[80:83], v[104:107], v[36:39]\n"
    "v_mfma_f32_16x16x32_bf16 v[40:43], v[80:83], v[112:115], v[40:43]\n"
    "ds_read_b128 v[80:83], v117 offset:28672\n"
    "s_waitcnt lgkmcnt(7)\n"
    "v_mfma_f32_16x16x32_bf16 v[40:43], v[76:79], v[108:111], v[40:43]\n"
    "s_barrier\n"
    "v_mfma_f32_16x16x32_bf16 v[4:7], v[44:47], v[84:87], v[4:7]\n"
    "ds_read_b128 v[100:103], v118 offset:16384\n"
    "v_mfma_f32_16x16x32_bf16 v[4:7], v[48:51], v[88:91], v[4:7]\n"
    "ds_read_b128 v[104:107], v119 offset:16384\n"
    "v_mfma_f32_16x16x32_bf16 v[8:11], v[48:51], v[96:99], v[8:11]\n"
    "ds_read_b128 v[76:79], v116 offset:28672\n"
    "v_mfma_f32_16x16x32_bf16 v[8:11], v[44:47], v[92:95], v[8:11]\n"
    "ds_read_b128 v[112:115], v119 offset:24576\n"
    "s_waitcnt lgkmcnt(9)\n"
    "v_mfma_f32_16x16x32_bf16 v[16:19], v[52:55], v[92:95], v[16:19]\n"
    "ds_read_b128 v[108:111], v118 offset:24576\n"
    "v_mfma_f32_16x16x32_bf16 v[16:19], v[56:59], v[96:99], v[16:19]\n"
    "ds_read_b128 v[48:51], v117 offset:30720\n"
    "v_mfma_f32_16x16x32_bf16 v[12:15], v[56:59], v[88:91], v[12:15]\n"
    "ds_read_b128 v[44:47], v116 offset:30720\n"
    "v_mfma_f32_16x16x32_bf16 v[12:15], v[52:55], v[84:87], v[12:15]\n"
    "ds_read_b128 v[56:59], v117 offset:32768\n"
    "s_waitcnt lgkmcnt(11)\n"
    "v_mfma_f32_16x16x32_bf16 v[20:23], v[60:63], v[84:87], v[20:23]\n"
    "ds_read_b128 v[52:55], v116 offset:32768\n"
    "v_mfma_f32_16x16x32_bf16 v[20:23], v[64:67], v[88:91], v[20:23]\n"
    "v_mfma_f32_16x16x32_bf16 v[24:27], v[64:67], v[96:99], v[24:27]\n"
    "ds_read_b128 v[64:67], v117 offset:34816\n"
    "v_mfma_f32_16x16x32_bf16 v[24:27], v[60:63], v[92:95], v[24:27]\n"
    "ds_read_b128 v[60:63], v116 offset:34816\n"
    "s_waitcnt lgkmcnt(12)\n"
    "v_mfma_f32_16x16x32_bf16 v[32:35], v[68:71], v[92:95], v[32:35]\n"
    "v_mfma_f32_16x16x32_bf16 v[32:35], v[72:75], v[96:99], v[32:35]\n"
    "v_mfma_f32_16x16x32_bf16 v[28:31], v[72:75], v[88:91], v[28:31]\n"
    "ds_read_b128 v[72:75], v117 offset:36864\n"
    "v_mfma_f32_16x16x32_bf16 v[28:31], v[68:71], v[84:87], v[28:31]\n"
    "ds_read_b128 v[68:71], v116 offset:36864\n"
    "s_waitcnt lgkmcnt(10)\n"
    "v_mfma_f32_16x16x32_bf16 v[36:39], v[76:79], v[84:87], v[36:39]\n"
    "v_mfma_f32_16x16x32_bf16 v[36:39], v[80:83], v[88:91], v[36:39]\n"
    "v_mfma_f32_16x16x32_bf16 v[40:43], v[80:83], v[96:99], v[40:43]\n"
    "ds_read_b128 v[80:83], v117 offset:38912\n"
    "s_waitcnt lgkmcnt(7)\n"
    "v_mfma_f32_16x16x32_bf16 v[40:43], v[76:79], v[92:95], v[40:43]\n"
    "s_barrier\n"
    "v_mfma_f32_16x16x32_bf16 v[4:7], v[44:47], v[100:103], v[4:7]\n"
    "ds_read_b128 v[84:87], v118 offset:32768\n"
    "v_mfma_f32_16x16x32_bf16 v[4:7], v[48:51], v[104:107], v[4:7]\n"
    "ds_read_b128 v[88:91], v119 offset:32768\n"
    "v_mfma_f32_16x16x32_bf16 v[8:11], v[48:51], v[112:115], v[8:11]\n"
    "ds_read_b128 v[76:79], v116 offset:38912\n"
    "v_mfma_f32_16x16x32_bf16 v[8:11], v[44:47], v[108:111], v[8:11]\n"
    "ds_read_b128 v[96:99], v119 offset:40960\n"
    "s_waitcnt lgkmcnt(9)\n"
    "v_mfma_f32_16x16x32_bf16 v[16:19], v[52:55], v[108:111], v[16:19]\n"
    "ds_read_b128 v[92:95], v118 offset:40960\n"
    "v_mfma_f32_16x16x32_bf16 v[16:19], v[56:59], v[112:115], v[16:19]\n"
    "ds_read_b128 v[48:51], v117 offset:40960\n"
    "v_mfma_f32_16x16x32_bf16 v[12:15], v[56:59], v[104:107], v[12:15]\n"
    "ds_read_b128 v[44:47], v116 offset:40960\n"
    "v_mfma_f32_16x16x32_bf16 v[12:15], v[52:55], v[100:103], v[12:15]\n"
    "ds_read_b128 v[56:59], v117 offset:43008\n"
    "s_waitcnt lgkmcnt(11)\n"
    "v_mfma_f32_16x16x32_bf16 v[20:23], v[60:63], v[100:103], v[20:23]\n"
    "ds_read_b128 v[52:55], v116 offset:43008\n"
    "v_mfma_f32_16x16x32_bf16 v[20:23], v[64:67], v[104:107], v[20:23]\n"
    "v_mfma_f32_16x16x32_bf16 v[24:27], v[64:67], v[112:115], v[24:27]\n"
    "ds_read_b128 v[64:67], v117 offset:45056\n"
    "v_mfma_f32_16x16x32_bf16 v[24:27], v[60:63], v[108:111], v[24:27]\n"
    "ds_read_b128 v[60:63], v116 offset:45056\n"
    "s_waitcnt lgkmcnt(12)\n"
    "v_mfma_f32_16x16x32_bf16 v[32:35], v[68:71], v[108:111], v[32:35]\n"
    "v_mfma_f32_16x16x32_bf16 v[32:35], v[72:75], v[112:115], v[32:35]\n"
    "v_mfma_f32_16x16x32_bf16 v[28:31], v[72:75], v[104:107], v[28:31]\n"
    "ds_read_b128 v[72:75], v117 offset:47104\n"
    "v_mfma_f32_16x16x32_bf16 v[28:31], v[68:71], v[100:103], v[28:31]\n"
    "ds_read_b128 v[68:71], v116 offset:47104\n"
    "s_waitcnt lgkmcnt(10)\n"
    "v_mfma_f32_16x16x32_bf16 v[36:39], v[76:79], v[100:103], v[36:39]\n"
    "v_mfma_f32_16x16x32_bf16 v[36:39], v[80:83], v[104:107], v[36:39]\n"
    "v_mfma_f32_16x16x32_bf16 v[40:43], v[80:83], v[112:115], v[40:43]\n"
    "ds_read_b128 v[80:83], v117 offset:49152\n"
    "s_waitcnt lgkmcnt(7)\n"
    "v_mfma_f32_16x16x32_bf16 v[40:43], v[76:79], v[108:111], v[40:43]\n"
    "s_barrier\n"
    "v_mfma_f32_16x16x32_bf16 v[4:7], v[44:47], v[84:87], v[4:7]\n"
    "ds_read_b128 v[76:79], v116 offset:49152\n"
    "v_mfma_f32_16x16x32_bf16 v[4:7], v[48:51], v[88:91], v[4:7]\n"
    "v_add_u32 v116, 51200, v116\n"
    "v_add_u32 v117, 51200, v117\n"
    "v_mfma_f32_16x16x32_bf16 v[8:11], v[48:51], v[96:99], v[8:11]\n"
    "ds_read_b128 v[100:103], v118 offset:49152\n"
    "v_mfma_f32_16x16x32_bf16 v[8:11], v[44:47], v[92:95], v[8:11]\n"
    "ds_read_b128 v[104:107], v119 offset:49152\n"
    "s_waitcnt lgkmcnt(8)\n"
    "v_mfma_f32_16x16x32_bf16 v[16:19], v[52:55], v[92:95], v[16:19]\n"
    "ds_read_b128 v[112:115], v119 offset:57344\n"
    "v_mfma_f32_16x16x32_bf16 v[16:19], v[56:59], v[96:99], v[16:19]\n"
    "ds_read_b128 v[108:111], v118 offset:57344\n"
    "v_mfma_f32_16x16x32_bf16 v[12:15], v[56:59], v[88:91], v[12:15]\n"
    "ds_read_b128 v[48:51], v117 offset:0\n"
    "v_mfma_f32_16x16x32_bf16 v[12:15], v[52:55], v[84:87], v[12:15]\n"
    "ds_read_b128 v[44:47], v116 offset:0\n"
    "s_waitcnt lgkmcnt(10)\n"
    "v_mfma_f32_16x16x32_bf16 v[20:23], v[60:63], v[84:87], v[20:23]\n"
    "ds_read_b128 v[56:59], v117 offset:2048\n"
    "v_mfma_f32_16x16x32_bf16 v[20:23], v[64:67], v[88:91], v[20:23]\n"
    "ds_read_b128 v[52:55], v116 offset:2048\n"
    "v_mfma_f32_16x16x32_bf16 v[24:27], v[64:67], v[96:99], v[24:27]\n"
    "ds_read_b128 v[64:67], v117 offset:4096\n"
    "v_mfma_f32_16x16x32_bf16 v[24:27], v[60:63], v[92:95], v[24:27]\n"
    "ds_read_b128 v[60:63], v116 offset:4096\n"
    "s_waitcnt lgkmcnt(12)\n"
    "v_mfma_f32_16x16x32_bf16 v[32:35], v[68:71], v[92:95], v[32:35]\n"
    "v_mfma_f32_16x16x32_bf16 v[32:35], v[72:75], v[96:99], v[32:35]\n"
    "v_mfma_f32_16x16x32_bf16 v[28:31], v[72:75], v[88:91], v[28:31]\n"
    "ds_read_b128 v[72:75], v117 offset:6144\n"
    "v_mfma_f32_16x16x32_bf16 v[28:31], v[68:71], v[84:87], v[28:31]\n"
    "ds_read_b128 v[68:71], v116 offset:6144\n"
    "s_waitcnt lgkmcnt(12)\n"
    "v_mfma_f32_16x16x32_bf16 v[36:39], v[76:79], v[84:87], v[36:39]\n"
    "v_mfma_f32_16x16x32_bf16 v[36:39], v[80:83], v[88:91], v[36:39]\n"
    "v_mfma_f32_16x16x32_bf16 v[40:43], v[80:83], v[96:99], v[40:43]\n"
    "ds_read_b128 v[80:83], v117 offset:8192\n"
    "s_waitcnt lgkmcnt(7)\n"
    "v_mfma_f32_16x16x32_bf16 v[40:43], v[76:79], v[92:95], v[40:43]\n"
    "s_barrier\n"
    "v_mfma_f32_16x16x32_bf16 v[4:7], v[44:47], v[100:103], v[4:7]\n"
    "ds_read_b128 v[84:87], v118 offset:0\n"
    "v_mfma_f32_16x16x32_bf16 v[4:7], v[48:51], v[104:107], v[4:7]\n"
    "ds_read_b128 v[88:91], v119 offset:0\n"
    "v_mfma_f32_16x16x32_bf16 v[8:11], v[48:51], v[112:115], v[8:11]\n"
    "ds_read_b128 v[76:79], v116 offset:8192\n"
    "v_mfma_f32_16x16x32_bf16 v[8:11], v[44:47], v[108:111], v[8:11]\n"
    "ds_read_b128 v[96:99], v119 offset:8192\n"
    "s_waitcnt lgkmcnt(9)\n"
    "v_mfma_f32_16x16x32_bf16 v[16:19], v[52:55], v[108:111], v[16:19]\n"
    "ds_read_b128 v[92:95], v118 offset:8192\n"
    "v_mfma_f32_16x16x32_bf16 v[16:19], v[56:59], v[112:115], v[16:19]\n"
    "ds_read_b128 v[48:51], v117 offset:10240\n"
    "v_mfma_f32_16x16x32_bf16 v[12:15], v[56:59], v[104:107], v[12:15]\n"
    "ds_read_b128 v[44:47], v116 offset:10240\n"
    "v_mfma_f32_16x16x32_bf16 v[12:15], v[52:55], v[100:103], v[12:15]\n"
    "ds_read_b128 v[56:59], v117 offset:12288\n"
    "s_waitcnt lgkmcnt(11)\n"
    "v_mfma_f32_16x16x32_bf16 v[20:23], v[60:63], v[100:103], v[20:23]\n"
    "ds_read_b128 v[52:55], v116 offset:12288\n"
    "v_mfma_f32_16x16x32_bf16 v[20:23], v[64:67], v[104:107], v[20:23]\n"
    "v_mfma_f32_16x16x32_bf16 v[24:27], v[64:67], v[112:115], v[24:27]\n"
    "ds_read_b128 v[64:67], v117 offset:14336\n"
    "v_mfma_f32_16x16x32_bf16 v[24:27], v[60:63], v[108:111], v[24:27]\n"
    "ds_read_b128 v[60:63], v116 offset:14336\n"
    "s_waitcnt lgkmcnt(12)\n"
    "v_mfma_f32_16x16x32_bf16 v[32:35], v[68:71], v[108:111], v[32:35]\n"
    "v_mfma_f32_16x16x32_bf16 v[32:35], v[72:75], v[112:115], v[32:35]\n"
    "v_mfma_f32_16x16x32_bf16 v[28:31], v[72:75], v[104:107], v[28:31]\n"
    "ds_read_b128 v[72:75], v117 offset:16384\n"
    "v_mfma_f32_16x16x32_bf16 v[28:31], v[68:71], v[100:103], v[28:31]\n"
    "ds_read_b128 v[68:71], v116 offset:16384\n"
    "s_waitcnt lgkmcnt(10)\n"
    "v_mfma_f32_16x16x32_bf16 v[36:39], v[76:79], v[100:103], v[36:39]\n"
    "v_mfma_f32_16x16x32_bf16 v[36:39], v[80:83], v[104:107], v[36:39]\n"
    "v_mfma_f32_16x16x32_bf16 v[40:43], v[80:83], v[112:115], v[40:43]\n"
    "ds_read_b128 v[80:83], v117 offset:18432\n"
    "s_waitcnt lgkmcnt(7)\n"
    "v_mfma_f32_16x16x32_bf16 v[40:43], v[76:79], v[108:111], v[40:43]\n"
    "s_barrier\n"
    "v_mfma_f32_16x16x32_bf16 v[4:7], v[44:47], v[84:87], v[4:7]\n"
    "ds_read_b128 v[100:103], v118 offset:16384\n"
    "v_mfma_f32_16x16x32_bf16 v[4:7], v[48:51], v[88:91], v[4:7]\n"
    "ds_read_b128 v[104:107], v119 offset:16384\n"
    "v_mfma_f32_16x16x32_bf16 v[8:11], v[48:51], v[96:99], v[8:11]\n"
    "ds_read_b128 v[76:79], v116 offset:18432\n"
    "v_mfma_f32_16x16x32_bf16 v[8:11], v[44:47], v[92:95], v[8:11]\n"
    "ds_read_b128 v[112:115], v119 offset:24576\n"
    "s_waitcnt lgkmcnt(9)\n"
    "v_mfma_f32_16x16x32_bf16 v[16:19], v[52:55], v[92:95], v[16:19]\n"
    "ds_read_b128 v[108:111], v118 offset:24576\n"
    "v_mfma_f32_16x16x32_bf16 v[16:19], v[56:59], v[96:99], v[16:19]\n"
    "ds_read_b128 v[48:51], v117 offset:20480\n"
    "v_mfma_f32_16x16x32_bf16 v[12:15], v[56:59], v[88:91], v[12:15]\n"
    "ds_read_b128 v[44:47], v116 offset:20480\n"
    "v_mfma_f32_16x16x32_bf16 v[12:15], v[52:55], v[84:87], v[12:15]\n"
    "ds_read_b128 v[56:59], v117 offset:22528\n"
    "s_waitcnt lgkmcnt(11)\n"
    "v_mfma_f32_16x16x32_bf16 v[20:23], v[60:63], v[84:87], v[20:23]\n"
    "ds_read_b128 v[52:55], v116 offset:22528\n"
    "v_mfma_f32_16x16x32_bf16 v[20:23], v[64:67], v[88:91], v[20:23]\n"
    "v_mfma_f32_16x16x32_bf16 v[24:27], v[64:67], v[96:99], v[24:27]\n"
    "ds_read_b128 v[64:67], v117 offset:24576\n"
    "v_mfma_f32_16x16x32_bf16 v[24:27], v[60:63], v[92:95], v[24:27]\n"
    "ds_read_b128 v[60:63], v116 offset:24576\n"
    "s_waitcnt lgkmcnt(12)\n"
    "v_mfma_f32_16x16x32_bf16 v[32:35], v[68:71], v[92:95], v[32:35]\n"
    "v_mfma_f32_16x16x32_bf16 v[32:35], v[72:75], v[96:99], v[32:35]\n"
    "v_mfma_f32_16x16x32_bf16 v[28:31], v[72:75], v[88:91], v[28:31]\n"
    "ds_read_b128 v[72:75], v117 offset:26624\n"
    "v_mfma_f32_16x16x32_bf16 v[28:31], v[68:71], v[84:87], v[28:31]\n"
    "ds_read_b128 v[68:71], v116 offset:26624\n"
    "s_waitcnt lgkmcnt(10)\n"
    "v_mfma_f32_16x16x32_bf16 v[36:39], v[76:79], v[84:87], v[36:39]\n"
    "v_mfma_f32_16x16x32_bf16 v[36:39], v[80:83], v[88:91], v[36:39]\n"
    "v_mfma_f32_16x16x32_bf16 v[40:43], v[80:83], v[96:99], v[40:43]\n"
    "ds_read_b128 v[80:83], v117 offset:28672\n"
    "s_waitcnt lgkmcnt(7)\n"
    "v_mfma_f32_16x16x32_bf16 v[40:43], v[76:79], v[92:95], v[40:43]\n"
    "s_barrier\n"
    "v_mfma_f32_16x16x32_bf16 v[4:7], v[44:47], v[100:103], v[4:7]\n"
    "ds_read_b128 v[84:87], v118 offset:32768\n"
    "v_mfma_f32_16x16x32_bf16 v[4:7], v[48:51], v[104:107], v[4:7]\n"
    "ds_read_b128 v[88:91], v119 offset:32768\n"
    "v_mfma_f32_16x16x32_bf16 v[8:11], v[48:51], v[112:115], v[8:11]\n"
    "ds_read_b128 v[76:79], v116 offset:28672\n"
    "v_mfma_f32_16x16x32_bf16 v[8:11], v[44:47], v[108:111], v[8:11]\n"
    "ds_read_b128 v[96:99], v119 offset:40960\n"
    "s_waitcnt lgkmcnt(9)\n"
    "v_mfma_f32_16x16x32_bf16 v[16:19], v[52:55], v[108:111], v[16:19]\n"
    "ds_read_b128 v[92:95], v118 offset:40960\n"
    "v_mfma_f32_16x16x32_bf16 v[16:19], v[56:59], v[112:115], v[16:19]\n"
    "ds_read_b128 v[48:51], v117 offset:30720\n"
    "v_mfma_f32_16x16x32_bf16 v[12:15], v[56:59], v[104:107], v[12:15]\n"
    "ds_read_b128 v[44:47], v116 offset:30720\n"
    "v_mfma_f32_16x16x32_bf16 v[12:15], v[52:55], v[100:103], v[12:15]\n"
    "ds_read_b128 v[56:59], v117 offset:32768\n"
    "s_waitcnt lgkmcnt(11)\n"
    "v_mfma_f32_16x16x32_bf16 v[20:23], v[60:63], v[100:103], v[20:23]\n"
    "ds_read_b128 v[52:55], v116 offset:32768\n"
    "v_mfma_f32_16x16x32_bf16 v[20:23], v[64:67], v[104:107], v[20:23]\n"
    "v_mfma_f32_16x16x32_bf16 v[24:27], v[64:67], v[112:115], v[24:27]\n"
    "ds_read_b128 v[64:67], v117 offset:34816\n"
    "v_mfma_f32_16x16x32_bf16 v[24:27], v[60:63], v[108:111], v[24:27]\n"
    "ds_read_b128 v[60:63], v116 offset:34816\n"
    "s_waitcnt lgkmcnt(12)\n"
    "v_mfma_f32_16x16x32_bf16 v[32:35], v[68:71], v[108:111], v[32:35]\n"
    "v_mfma_f32_16x16x32_bf16 v[32:35], v[72:75], v[112:115], v[32:35]\n"
    "v_mfma_f32_16x16x32_bf16 v[28:31], v[72:75], v[104:107], v[28:31]\n"
    "ds_read_b128 v[72:75], v117 offset:36864\n"
    "v_mfma_f32_16x16x32_bf16 v[28:31], v[68:71], v[100:103], v[28:31]\n"
    "ds_read_b128 v[68:71], v116 offset:36864\n"
    "s_waitcnt lgkmcnt(10)\n"
    "v_mfma_f32_16x16x32_bf16 v[36:39], v[76:79], v[100:103], v[36:39]\n"
    "v_mfma_f32_16x16x32_bf16 v[36:39], v[80:83], v[104:107], v[36:39]\n"
    "v_mfma_f32_16x16x32_bf16 v[40:43], v[80:83], v[112:115], v[40:43]\n"
    "ds_read_b128 v[80:83], v117 offset:38912\n"
    ".Lpage2:\n"
    "s_cbranch_execz .Ltouch_next\n"
    "s_waitcnt lgkmcnt(7)\n"
    "v_mfma_f32_16x16x32_bf16 v[40:43], v[76:79], v[108:111], v[40:43]\n"
    "s_barrier\n"
    "v_mfma_f32_16x16x32_bf16 v[4:7], v[44:47], v[84:87], v[4:7]\n"
    "ds_read_b128 v[76:79], v116 offset:38912\n"
    "v_mfma_f32_16x16x32_bf16 v[4:7], v[48:51], v[88:91], v[4:7]\n"
    "v_mfma_f32_16x16x32_bf16 v[8:11], v[48:51], v[96:99], v[8:11]\n"
    "v_mfma_f32_16x16x32_bf16 v[8:11], v[44:47], v[92:95], v[8:11]\n"
    "ds_read_b128 v[44:47], v163 offset:0\n"
    "s_waitcnt lgkmcnt(7)\n"
    "v_mfma_f32_16x16x32_bf16 v[16:19], v[52:55], v[92:95], v[16:19]\n"
    "ds_read_b128 v[48:51], v163 offset:320\n"
    "v_mfma_f32_16x16x32_bf16 v[16:19], v[56:59], v[96:99], v[16:19]\n"
    "v_mfma_f32_16x16x32_bf16 v[12:15], v[56:59], v[88:91], v[12:15]\n"
    "v_mfma_f32_16x16x32_bf16 v[12:15], v[52:55], v[84:87], v[12:15]\n"
    "ds_read_b128 v[52:55], v163 offset:64\n"
    "s_waitcnt lgkmcnt(7)\n"
    "v_mfma_f32_16x16x32_bf16 v[20:23], v[60:63], v[84:87], v[20:23]\n"
    "ds_read_b128 v[56:59], v163 offset:384\n"
    "v_mfma_f32_16x16x32_bf16 v[20:23], v[64:67], v[88:91], v[20:23]\n"
    "v_mfma_f32_16x16x32_bf16 v[24:27], v[64:67], v[96:99], v[24:27]\n"
    "v_mfma_f32_16x16x32_bf16 v[24:27], v[60:63], v[92:95], v[24:27]\n"
    "ds_read_b128 v[60:63], v163 offset:128\n"
    "s_waitcnt lgkmcnt(7)\n"
    "v_mfma_f32_16x16x32_bf16 v[32:35], v[68:71], v[92:95], v[32:35]\n"
    "ds_read_b128 v[64:67], v163 offset:448\n"
    "v_mfma_f32_16x16x32_bf16 v[32:35], v[72:75], v[96:99], v[32:35]\n"
    "v_mfma_f32_16x16x32_bf16 v[28:31], v[72:75], v[88:91], v[28:31]\n"
    "v_mfma_f32_16x16x32_bf16 v[28:31], v[68:71], v[84:87], v[28:31]\n"
    "ds_read_b128 v[68:71], v163 offset:192\n"
    "s_waitcnt lgkmcnt(7)\n"
    "v_mfma_f32_16x16x32_bf16 v[36:39], v[76:79], v[84:87], v[36:39]\n"
    "ds_read_b128 v[72:75], v163 offset:512\n"
    "v_mfma_f32_16x16x32_bf16 v[36:39], v[80:83], v[88:91], v[36:39]\n"
    "v_mfma_f32_16x16x32_bf16 v[40:43], v[80:83], v[96:99], v[40:43]\n"
    "v_mfma_f32_16x16x32_bf16 v[40:43], v[76:79], v[92:95], v[40:43]\n"
    "s_setprio 0\n"
    "s_setprio 1\n"
    "s_barrier\n"
    "s_nop 2\n"
    "v_mov_b32 v102, v39\n"
    "v_mov_b32 v84, v39\n"
    "v_mov_b32 v103, v43\n"
    "v_mov_b32 v85, v43\n"
    "v_permlane16_swap_b32 v102, v84\n"
    "s_nop 0\n"
    "v_permlane16_swap_b32 v103, v85\n"
    "v_mov_b32 v102, v84\n"
    "v_mov_b32 v103, v85\n"
    "s_nop 0\n"
    "v_permlane32_swap_b32 v102, v84\n"
    "v_permlane32_swap_b32 v103, v85\n"
    "s_waitcnt lgkmcnt(6)\n"
    "v_mul_f32 v104, v48, v84\n"
    "v_mul_f32 v105, v48, v85\n"
    "v_fma_f32 v4, v44, v4, -v104\n"
    "v_fma_f32 v8, v44, v8, -v105\n"
    "v_mul_f32 v104, v49, v84\n"
    "v_mul_f32 v105, v49, v85\n"
    "v_fma_f32 v5, v45, v5, -v104\n"
    "v_fma_f32 v9, v45, v9, -v105\n"
    "v_fma_f32 v88, v136, v4, v5\n"
    "v_fma_f32 v89, v136, v8, v9\n"
    "v_fma_f32 v88, -v137, v8, v88\n"
    "v_fmac_f32 v89, v137, v4\n"
    "v_mul_f32 v104, v50, v84\n"
    "ds_read_b128 v[76:79], v163 offset:256\n"
    "ds_read_b128 v[80:83], v163 offset:576\n"
    "v_mul_f32 v105, v50, v85\n"
    "v_fma_f32 v6, v46, v6, -v104\n"
    "v_fma_f32 v10, v46, v10, -v105\n"
    "v_fma_f32 v86, v136, v88, v6\n"
    "v_fma_f32 v87, v136, v89, v10\n"
    "v_fma_f32 v86, -v137, v89, v86\n"
    "v_fmac_f32 v87, v137, v88\n"
    "v_mul_f32 v104, v51, v84\n"
    "v_mul_f32 v105, v51, v85\n"
    "v_fma_f32 v7, v47, v7, -v104\n"
    "v_fma_f32 v11, v47, v11, -v105\n"
    "v_fma_f32 v88, v136, v86, v7\n"
    "v_fma_f32 v89, v136, v87, v11\n"
    "v_fma_f32 v88, -v137, v87, v88\n"
    "v_fmac_f32 v89, v137, v86\n"
    "s_waitcnt lgkmcnt(6)\n"
    "v_mul_f32 v104, v56, v84\n"
    "v_mul_f32 v105, v56, v85\n"
    "s_barrier\n"
    "v_fma_f32 v12, v52, v12, -v104\n"
    "v_fma_f32 v16, v52, v16, -v105\n"
    "v_fma_f32 v86, v136, v88, v12\n"
    "v_fma_f32 v87, v136, v89, v16\n"
    "v_fma_f32 v86, -v137, v89, v86\n"
    "v_fmac_f32 v87, v137, v88\n"
    "v_mul_f32 v104, v57, v84\n"
    "v_mul_f32 v105, v57, v85\n"
    "v_fma_f32 v13, v53, v13, -v104\n"
    "v_fma_f32 v17, v53, v17, -v105\n"
    "v_fma_f32 v88, v136, v86, v13\n"
    "v_fma_f32 v89, v136, v87, v17\n"
    "v_fma_f32 v88, -v137, v87, v88\n"
    "v_fmac_f32 v89, v137, v86\n"
    "v_mul_f32 v104, v58, v84\n"
    "v_mul_f32 v105, v58, v85\n"
    "v_fma_f32 v14, v54, v14, -v104\n"
    "v_fma_f32 v18, v54, v18, -v105\n"
    "v_fma_f32 v86, v136, v88, v14\n"
    "v_fma_f32 v87, v136, v89, v18\n"
    "v_fma_f32 v86, -v137, v89, v86\n"
    "v_fmac_f32 v87, v137, v88\n"
    "v_mul_f32 v104, v59, v84\n"
    "v_mul_f32 v105, v59, v85\n"
    "v_fma_f32 v15, v55, v15, -v104\n"
    "v_fma_f32 v19, v55, v19, -v105\n"
    "v_fma_f32 v88, v136, v86, v15\n"
    "v_fma_f32 v89, v136, v87, v19\n"
    "v_fma_f32 v88, -v137, v87, v88\n"
    "v_fmac_f32 v89, v137, v86\n"
    "s_waitcnt lgkmcnt(4)\n"
    "v_mul_f32 v104, v64, v84\n"
    "v_mul_f32 v105, v64, v85\n"
    "v_fma_f32 v20, v60, v20, -v104\n"
    "v_fma_f32 v24, v60, v24, -v105\n"
    "v_fma_f32 v86, v136, v88, v20\n"
    "v_fma_f32 v87, v136, v89, v24\n"
    "v_fma_f32 v86, -v137, v89, v86\n"
    "v_fmac_f32 v87, v137, v88\n"
    "v_mul_f32 v104, v65, v84\n"
    "v_mul_f32 v105, v65, v85\n"
    "v_fma_f32 v21, v61, v21, -v104\n"
    "v_fma_f32 v25, v61, v25, -v105\n"
    "v_fma_f32 v88, v136, v86, v21\n"
    "v_fma_f32 v89, v136, v87, v25\n"
    "s_barrier\n"
    "v_fma_f32 v88, -v137, v87, v88\n"
    "v_fmac_f32 v89, v137, v86\n"
    "v_mul_f32 v104, v66, v84\n"
    "v_mul_f32 v105, v66, v85\n"
    "v_fma_f32 v22, v62, v22, -v104\n"
    "v_fma_f32 v26, v62, v26, -v105\n"
    "v_fma_f32 v86, v136, v88, v22\n"
    "v_fma_f32 v87, v136, v89, v26\n"
    "v_fma_f32 v86, -v137, v89, v86\n"
    "v_fmac_f32 v87, v137, v88\n"
    "v_mul_f32 v104, v67, v84\n"
    "v_mul_f32 v105, v67, v85\n"
    "v_fma_f32 v23, v63, v23, -v104\n"
    "v_fma_f32 v27, v63, v27, -v105\n"
    "v_fma_f32 v88, v136, v86, v23\n"
    "v_fma_f32 v89, v136, v87, v27\n"
    "v_fma_f32 v88, -v137, v87, v88\n"
    "v_fmac_f32 v89, v137, v86\n"
    "s_waitcnt lgkmcnt(2)\n"
    "v_mul_f32 v104, v72, v84\n"
    "v_mul_f32 v105, v72, v85\n"
    "v_fma_f32 v28, v68, v28, -v104\n"
    "v_fma_f32 v32, v68, v32, -v105\n"
    "v_fma_f32 v86, v136, v88, v28\n"
    "v_fma_f32 v87, v136, v89, v32\n"
    "v_fma_f32 v86, -v137, v89, v86\n"
    "v_fmac_f32 v87, v137, v88\n"
    "v_mul_f32 v104, v73, v84\n"
    "v_mul_f32 v105, v73, v85\n"
    "v_fma_f32 v29, v69, v29, -v104\n"
    "v_fma_f32 v33, v69, v33, -v105\n"
    "v_fma_f32 v88, v136, v86, v29\n"
    "v_fma_f32 v89, v136, v87, v33\n"
    "v_fma_f32 v88, -v137, v87, v88\n"
    "v_fmac_f32 v89, v137, v86\n"
    "v_mul_f32 v104, v74, v84\n"
    "v_mul_f32 v105, v74, v85\n"
    "v_fma_f32 v30, v70, v30, -v104\n"
    "v_fma_f32 v34, v70, v34, -v105\n"
    "v_fma_f32 v86, v136, v88, v30\n"
    "v_fma_f32 v87, v136, v89, v34\n"
    "v_fma_f32 v86, -v137, v89, v86\n"
    "v_fmac_f32 v87, v137, v88\n"
    "v_mul_f32 v104, v75, v84\n"
    "v_mul_f32 v105, v75, v85\n"
    "s_barrier\n"
    "v_fma_f32 v31, v71, v31, -v104\n"
    "v_fma_f32 v35, v71, v35, -v105\n"
    "v_fma_f32 v88, v136, v86, v31\n"
    "v_fma_f32 v89, v136, v87, v35\n"
    "v_fma_f32 v88, -v137, v87, v88\n"
    "v_fmac_f32 v89, v137, v86\n"
    "s_waitcnt lgkmcnt(0)\n"
    "v_mul_f32 v104, v80, v84\n"
    "v_mul_f32 v105, v80, v85\n"
    "v_fma_f32 v36, v76, v36, -v104\n"
    "v_fma_f32 v40, v76, v40, -v105\n"
    "v_fma_f32 v86, v136, v88, v36\n"
    "v_fma_f32 v87, v136, v89, v40\n"
    "v_fma_f32 v86, -v137, v89, v86\n"
    "v_fmac_f32 v87, v137, v88\n"
    "v_mul_f32 v104, v81, v84\n"
    "v_mul_f32 v105, v81, v85\n"
    "v_fma_f32 v37, v77, v37, -v104\n"
    "v_fma_f32 v41, v77, v41, -v105\n"
    "v_fma_f32 v88, v136, v86, v37\n"
    "v_fma_f32 v89, v136, v87, v41\n"
    "v_fma_f32 v88, -v137, v87, v88\n"
    "v_fmac_f32 v89, v137, v86\n"
    "v_mul_f32 v104, v82, v84\n"
    "v_mul_f32 v105, v82, v85\n"
    "v_fma_f32 v38, v78, v38, -v104\n"
    "v_fma_f32 v42, v78, v42, -v105\n"
    "v_fma_f32 v86, v136, v88, v38\n"
    "v_fma_f32 v87, v136, v89, v42\n"
    "v_fma_f32 v86, -v137, v89, v86\n"
    "v_fmac_f32 v87, v137, v88\n"
    "v_mul_f32 v104, v83, v84\n"
    "v_mul_f32 v105, v83, v85\n"
    "v_fma_f32 v39, v79, v39, -v104\n"
    "v_fma_f32 v43, v79, v43, -v105\n"
    "v_fma_f32 v88, v136, v86, v39\n"
    "v_fma_f32 v89, v136, v87, v43\n"
    "v_fma_f32 v88, -v137, v87, v88\n"
    "v_fmac_f32 v89, v137, v86\n"
    "s_mov_b32 exec_lo, 0\n"
    "s_mov_b32 exec_hi, 0xffff0000\n"
    "v_mov_b32 v88, v86\n"
    "v_mov_b32 v89, v87\n"
    "s_mov_b64 exec, -1\n"
    "v_mov_b32 v92, v88\n"
    "v_mov_b32 v94, v88\n"
    "s_nop 1\n"
    "v_permlane16_swap_b32 v92, v94\n"
    "s_barrier\n"
    "v_mov_b32 v96, v92\n"
    "v_mov_b32 v98, v94\n"
    "s_nop 0\n"
    "v_permlane32_swap_b32 v92, v96\n"
    "v_permlane32_swap_b32 v94, v98\n"
    "v_mov_b32 v93, v89\n"
    "v_mov_b32 v95, v89\n"
    "s_nop 1\n"
    "v_permlane16_swap_b32 v93, v95\n"
    "v_mov_b32 v97, v93\n"
    "v_mov_b32 v99, v95\n"
    "s_nop 0\n"
    "v_permlane32_swap_b32 v93, v97\n"
    "v_permlane32_swap_b32 v95, v99\n"
    "v_mov_b32 v111, s17\n"
    "v_mov_b32 v113, s18\n"
    "v_fma_f32 v106, v140, v92, v94\n"
    "v_fma_f32 v107, v140, v93, v95\n"
    "v_fma_f32 v106, -v141, v93, v106\n"
    "v_fmac_f32 v107, v141, v92\n"
    "v_fma_f32 v108, v140, v106, v96\n"
    "v_fma_f32 v109, v140, v107, v97\n"
    "v_fma_f32 v108, -v141, v107, v108\n"
    "v_fmac_f32 v109, v141, v106\n"
    "v_fma_f32 v110, v138, v108, v98\n"
    "v_fma_f32 v112, v138, v109, v99\n"
    "v_fma_f32 v110, -v139, v109, v110\n"
    "v_fmac_f32 v112, v139, v108\n"
    "s_sub_u32 s28, 25, s10\n"
    "s_lshl_b32 s28, s28, 10\n"
    "s_barrier\n"
    "s_add_u32 s25, s16, 4\n"
    "s_sub_u32 s19, s25, 9\n"
    "s_cmp_ge_u32 s25, 9\n"
    "s_cselect_b32 s25, s19, s25\n"
    "s_mul_i32 s25, s25, 589824\n"
    "s_add_u32 s25, s25, s26\n"
    "s_add_u32 m0, s27, 0\n"
    "s_add_u32 s19, s25, 768\n"
    "buffer_load_dwordx4 v154, s[72:75], s19 offen offset:0 lds\n"
    "buffer_load_dwordx4 v155, s[72:75], s19 offen offset:1024 lds\n"
    "s_add_u32 m0, s27, 8192\n"
    "s_add_u32 s19, s25, 37632\n"
    "buffer_load_dwordx4 v154, s[72:75], s19 offen offset:0 lds\n"
    "buffer_load_dwordx4 v155, s[72:75], s19 offen offset:1024 lds\n"
    "s_mov_b64 exec, 0xffff\n"
    "buffer_store_dwordx4 v[110:113], v133, s[88:91], s28 offen sc1\n"
    "s_mov_b32 exec_lo, 0x0000ffff\n"
    "v_mov_b32 v108, 0\n"
    "v_mov_b32 v109, 0\n"
    "s_mov_b32 exec_lo, 0xffff0000\n"
    "v_mov_b32 v108, v92\n"
    "v_mov_b32 v109, v93\n"
    "s_mov_b32 exec_lo, 0\n"
    "s_mov_b32 exec_hi, 0xffff\n"
    "v_mov_b32 v108, v106\n"
    "v_mov_b32 v109, v107\n"
    "s_mov_b64 exec, -1\n"
    "s_barrier\n"
    "s_add_u32 s25, s16, 5\n"
    "s_sub_u32 s19, s25, 9\n"
    "s_cmp_ge_u32 s25, 9\n"
    "s_cselect_b32 s25, s19, s25\n"
    "s_mul_i32 s25, s25, 589824\n"
    "s_add_u32 s25, s25, s26\n"
    "s_add_u32 m0, s27, 16384\n"
    "s_add_u32 s19, s25, 512\n"
    "buffer_load_dwordx4 v154, s[72:75], s19 offen offset:0 lds\n"
    "buffer_load_dwordx4 v155, s[72:75], s19 offen offset:1024 lds\n"
    "s_add_u32 s20, s11, s14\n"
    "s_add_u32 s20, s20, 3\n"
    "s_sub_u32 s19, s20, 9\n"
    "s_cmp_ge_u32 s20, 9\n"
    "s_cselect_b32 s20, s19, s20\n"
    "s_lshl_b32 s19, s11, 4\n"
    "s_add_u32 s19, s19, s15\n"
    "s_mul_i32 s19, s19, 36864\n"
    "s_lshl_b32 s21, s20, 7\n"
    "s_add_u32 s97, s19, s21\n"
    "s_lshl_b32 s19, s20, 1\n"
    "s_add_u32 s19, s19, s15\n"
    "s_mul_i32 s19, s19, 36864\n"
    "s_lshl_b32 s21, s11, 7\n"
    "s_add_u32 s99, s19, s21\n"
    "s_add_u32 m0, s27, 24576\n"
    "s_add_u32 s19, s25, 37376\n"
    "buffer_load_dwordx4 v154, s[72:75], s19 offen offset:0 lds\n"
    "buffer_load_dwordx4 v155, s[72:75], s19 offen offset:1024 lds\n"
    "s_barrier\n"
    "s_add_u32 m0, s27, 32768\n"
    "s_add_u32 s19, s25, 768\n"
    "buffer_load_dwordx4 v154, s[72:75], s19 offen offset:0 lds\n"
    "buffer_load_dwordx4 v155, s[72:75], s19 offen offset:1024 lds\n"
    "s_add_u32 s20, s11, s14\n"
    "s_add_u32 s20, s20, 1\n"
    "s_sub_u32 s19, s20, 9\n"
    "s_cmp_ge_u32 s20, 9\n"
    "s_cselect_b32 s20, s19, s20\n"
    "s_lshl_b32 s19, s11, 4\n"
    "s_add_u32 s19, s19, s15\n"
    "s_mul_i32 s19, s19, 36864\n"
    "s_lshl_b32 s21, s20, 7\n"
    "s_add_u32 s96, s19, s21\n"
    "s_lshl_b32 s19, s20, 1\n"
    "s_add_u32 s19, s19, s15\n"
    "s_mul_i32 s19, s19, 36864\n"
    "s_lshl_b32 s21, s11, 7\n"
    "s_add_u32 s98, s19, s21\n"
    "s_add_u32 m0, s27, 40960\n"
    "s_add_u32 s19, s25, 37632\n"
    "buffer_load_dwordx4 v154, s[72:75], s19 offen offset:0 lds\n"
    "buffer_load_dwordx4 v155, s[72:75], s19 offen offset:1024 lds\n"
    "s_waitcnt vmcnt(9)\n"
    "s_barrier\n"
    "s_add_u32 s25, s16, 6\n"
    "s_sub_u32 s19, s25, 9\n"
    "s_cmp_ge_u32 s25, 9\n"
    "s_cselect_b32 s25, s19, s25\n"
    "s_mul_i32 s25, s25, 589824\n"
    "s_add_u32 s25, s25, s26\n"
    "s_add_u32 m0, s27, 49152\n"
    "s_add_u32 s19, s25, 512\n"
    "buffer_load_dwordx4 v154, s[72:75], s19 offen offset:0 lds\n"
    "buffer_load_dwordx4 v155, s[72:75], s19 offen offset:1024 lds\n"
    "s_add_u32 m0, s27, 57344\n"
    "s_add_u32 s19, s25, 37376\n"
    "buffer_load_dwordx4 v154, s[72:75], s19 offen offset:0 lds\n"
    "buffer_load_dwordx4 v155, s[72:75], s19 offen offset:1024 lds\n"
    "s_add_u32 s31, s28, 1024\n"
    "buffer_load_dwordx4 v[44:47], v133, s[88:91], s31 offen sc1\n"
    "s_add_u32 s31, s28, 5120\n"
    "buffer_load_dwordx4 v[48:51], v133, s[88:91], s31 offen sc1\n"
    "s_add_u32 s31, s28, 9216\n"
    "buffer_load_dwordx4 v[52:55], v133, s[88:91], s31 offen sc1\n"
    "s_add_u32 s31, s28, 13312\n"
    "buffer_load_dwordx4 v[56:59], v133, s[88:91], s31 offen sc1\n"
    "s_waitcnt vmcnt(12)\n"
    "s_barrier\n"
    "s_add_u32 m0, s27, 0\n"
    "s_add_u32 s19, s25, 768\n"
    "buffer_load_dwordx4 v154, s[72:75], s19 offen offset:0 lds\n"
    "buffer_load_dwordx4 v155, s[72:75], s19 offen offset:1024 lds\n"
    "s_add_u32 m0, s27, 8192\n"
    "s_add_u32 s19, s25, 37632\n"
    "buffer_load_dwordx4 v154, s[72:75], s19 offen offset:0 lds\n"
    "buffer_load_dwordx4 v155, s[72:75], s19 offen offset:1024 lds\n"
    "s_add_u32 s31, s28, 17408\n"
    "buffer_load_dwordx4 v[60:63], v133, s[88:91], s31 offen sc1\n"
    "s_add_u32 s31, s28, 21504\n"
    "buffer_load_dwordx4 v[64:67], v133, s[88:91], s31 offen sc1\n"
    "s_add_u32 s31, s28, 25600\n"
    "buffer_load_dwordx4 v[68:71], v133, s[88:91], s31 offen sc1\n"
    "s_waitcnt vmcnt(15)\n"
    "s_barrier\n"
    "s_add_u32 s25, s16, 7\n"
    "s_sub_u32 s19, s25, 9\n"
    "s_cmp_ge_u32 s25, 9\n"
    "s_cselect_b32 s25, s19, s25\n"
    "s_mul_i32 s25, s25, 589824\n"
    "s_add_u32 s25, s25, s26\n"
    "s_add_u32 m0, s27, 16384\n"
    "s_add_u32 s19, s25, 512\n"
    "buffer_load_dwordx4 v154, s[72:75], s19 offen offset:0 lds\n"
    "buffer_load_dwordx4 v155, s[72:75], s19 offen offset:1024 lds\n"
    "s_add_u32 m0, s27, 24576\n"
    "s_add_u32 s19, s25, 37376\n"
    "buffer_load_dwordx4 v154, s[72:75], s19 offen offset:0 lds\n"
    "buffer_load_dwordx4 v155, s[72:75], s19 offen offset:1024 lds\n"
    "s_mov_b32 s20, s11\n"
    "s_lshl_b32 s19, s11, 4\n"
    "s_add_u32 s19, s19, s12\n"
    "s_mul_i32 s19, s19, 36864\n"
    "s_lshl_b32 s21, s20, 7\n"
    "s_add_u32 s25, s19, s21\n"
    "s_lshl_b32 s19, s20, 1\n"
    "s_add_u32 s19, s19, s15\n"
    "s_mul_i32 s19, s19, 36864\n"
    "s_lshl_b32 s21, s11, 7\n"
    "s_add_u32 s26, s19, s21\n"
    "s_waitcnt vmcnt(15)\n"
    "s_barrier\n"
    "s_waitcnt vmcnt(4)\n"
    "s_mov_b32 s24, 1048576\n"
    ".LT_cpoll_1:\n"
    "v_xor_b32 v45, s17, v45\n"
    "v_xor_b32 v49, s17, v49\n"
    "v_xor_b32 v53, s17, v53\n"
    "v_xor_b32 v57, s17, v57\n"
    "v_xor_b32 v61, s17, v61\n"
    "v_xor_b32 v65, s17, v65\n"
    "v_xor_b32 v69, s17, v69\n"
    "v_bitop3_b32 v45, v45, v47, s18 bitop3:0xf6\n"
    "v_bitop3_b32 v49, v49, v51, s18 bitop3:0xf6\n"
    "v_bitop3_b32 v53, v53, v55, s18 bitop3:0xf6\n"
    "v_bitop3_b32 v57, v57, v59, s18 bitop3:0xf6\n"
    "v_bitop3_b32 v61, v61, v63, s18 bitop3:0xf6\n"
    "v_bitop3_b32 v65, v65, v67, s18 bitop3:0xf6\n"
    "v_bitop3_b32 v69, v69, v71, s18 bitop3:0xf6\n"
    "v_min_u32 v45, 1, v45\n"
    "v_min_u32 v49, 1, v49\n"
    "v_min_u32 v53, 1, v53\n"
    "v_min_u32 v57, 1, v57\n"
    "v_min_u32 v61, 1, v61\n"
    "v_min_u32 v65, 1, v65\n"
    "v_min_u32 v69, 1, v69\n"
    "v_add3_u32 v45, v45, v49, v53\n"
    "v_add3_u32 v57, v57, v61, v65\n"
    "v_add3_u32 v45, v45, v57, v69\n"
    "v_cmp_ne_u32 vcc, v45, v135\n"
    "s_nop 0\n"
    "s_cmp_eq_u64 vcc, 0\n"
    "s_cbranch_scc1 .LT_cok_2\n"
    "s_add_u32 s31, s28, 1024\n"
    "buffer_load_dwordx4 v[44:47], v133, s[88:91], s31 offen sc1\n"
    "s_add_u32 s31, s28, 5120\n"
    "buffer_load_dwordx4 v[48:51], v133, s[88:91], s31 offen sc1\n"
    "s_add_u32 s31, s28, 9216\n"
    "buffer_load_dwordx4 v[52:55], v133, s[88:91], s31 offen sc1\n"
    "s_add_u32 s31, s28, 13312\n"
    "buffer_load_dwordx4 v[56:59], v133, s[88:91], s31 offen sc1\n"
    "s_add_u32 s31, s28, 17408\n"
    "buffer_load_dwordx4 v[60:63], v133, s[88:91], s31 offen sc1\n"
    "s_add_u32 s31, s28, 21504\n"
    "buffer_load_dwordx4 v[64:67], v133, s[88:91], s31 offen sc1\n"
    "s_add_u32 s31, s28, 25600\n"
    "buffer_load_dwordx4 v[68:71], v133, s[88:91], s31 offen sc1\n"
    "s_waitcnt vmcnt(0)\n"
    "s_sub_u32 s24, s24, 1\n"
    "s_cmp_eq_u32 s24, 0\n"
    "s_cbranch_scc0 .LT_cpoll_1\n"
    ".LT_cok_2:\n"
    "v_fma_f32 v88, v146, v68, v64\n"
    "v_fma_f32 v89, v146, v70, v66\n"
    "v_fma_f32 v88, -v147, v70, v88\n"
    "v_fmac_f32 v89, v147, v68\n"
    "v_fma_f32 v86, v146, v88, v60\n"
    "v_fma_f32 v87, v146, v89, v62\n"
    "v_fma_f32 v86, -v147, v89, v86\n"
    "v_fmac_f32 v87, v147, v88\n"
    "v_fma_f32 v88, v146, v86, v56\n"
    "v_fma_f32 v89, v146, v87, v58\n"
    "v_fma_f32 v88, -v147, v87, v88\n"
    "v_fmac_f32 v89, v147, v86\n"
    "v_fma_f32 v86, v146, v88, v52\n"
    "s_barrier\n"
    "v_fma_f32 v87, v146, v89, v54\n"
    "v_fma_f32 v86, -v147, v89, v86\n"
    "v_fmac_f32 v87, v147, v88\n"
    "v_fma_f32 v88, v146, v86, v48\n"
    "v_fma_f32 v89, v146, v87, v50\n"
    "v_fma_f32 v88, -v147, v87, v88\n"
    "v_fmac_f32 v89, v147, v86\n"
    "v_fma_f32 v86, v146, v88, v44\n"
    "v_fma_f32 v87, v146, v89, v46\n"
    "v_fma_f32 v86, -v147, v89, v86\n"
    "v_fmac_f32 v87, v147, v88\n"
    "v_mul_f32 v106, v144, v86\n"
    "v_mul_f32 v107, v144, v87\n"
    "v_fma_f32 v106, -v145, v87, v106\n"
    "v_fmac_f32 v107, v145, v86\n"
    "v_mov_b32 v92, v106\n"
    "v_mov_b32 v93, v107\n"
    "s_nop 0\n"
    "v_permlane16_swap_b32 v92, v106\n"
    "v_permlane16_swap_b32 v93, v107\n"
    "v_add_f32 v106, v106, v92\n"
    "v_add_f32 v107, v107, v93\n"
    "v_mov_b32 v92, v106\n"
    "v_mov_b32 v93, v107\n"
    "s_nop 0\n"
    "v_permlane32_swap_b32 v92, v106\n"
    "v_permlane32_swap_b32 v93, v107\n"
    "v_add_f32 v106, v106, v92\n"
    "v_add_f32 v107, v107, v93\n"
    "v_fma_f32 v88, v142, v106, v108\n"
    "v_fma_f32 v89, v142, v107, v109\n"
    "v_fma_f32 v88, -v143, v107, v88\n"
    "v_fmac_f32 v89, v143, v106\n"
    "v_fma_f32 v86, v136, v88, v4\n"
    "v_fma_f32 v87, v136, v89, v8\n"
    "v_fma_f32 v86, -v137, v89, v86\n"
    "s_waitcnt vmcnt(0)\n"
    "s_barrier\n"
    "v_fmac_f32 v87, v137, v88\n"
    "v_mul_f32 v44, v148, v86\n"
    "v_mul_f32 v48, v148, v87\n"
    "v_fma_f32 v44, -v149, v87, v44\n"
    "v_fma_f32 v48, -v149, v86, -v48\n"
    "v_fma_f32 v88, v136, v86, v5\n"
    "v_fma_f32 v89, v136, v87, v9\n"
    "v_fma_f32 v88, -v137, v87, v88\n"
    "v_fmac_f32 v89, v137, v86\n"
    "v_mul_f32 v45, v148, v88\n"
    "v_mul_f32 v49, v148, v89\n"
    "v_fma_f32 v45, -v149, v89, v45\n"
    "v_fma_f32 v49, -v149, v88, -v49\n"
    "v_fma_f32 v86, v136, v88, v6\n"
    "v_fma_f32 v87, v136, v89, v10\n"
    "v_fma_f32 v86, -v137, v89, v86\n"
    "v_fmac_f32 v87, v137, v88\n"
    "v_mul_f32 v46, v148, v86\n"
    "v_mul_f32 v50, v148, v87\n"
    "v_fma_f32 v46, -v149, v87, v46\n"
    "v_fma_f32 v50, -v149, v86, -v50\n"
    "v_fma_f32 v88, v136, v86, v7\n"
    "v_fma_f32 v89, v136, v87, v11\n"
    "v_fma_f32 v88, -v137, v87, v88\n"
    "v_fmac_f32 v89, v137, v86\n"
    "v_mul_f32 v47, v148, v88\n"
    "v_mul_f32 v51, v148, v89\n"
    "v_fma_f32 v47, -v149, v89, v47\n"
    "v_fma_f32 v51, -v149, v88, -v51\n"
    "v_cvt_pk_bf16_f32 v52, v44, v45\n"
    "v_cvt_pk_bf16_f32 v53, v46, v47\n"
    "v_cvt_pk_bf16_f32 v54, v48, v49\n"
    "v_cvt_pk_bf16_f32 v55, v50, v51\n"
    "v_fma_f32 v86, v136, v88, v12\n"
    "v_fma_f32 v87, v136, v89, v16\n"
    "v_fma_f32 v86, -v137, v89, v86\n"
    "v_fmac_f32 v87, v137, v88\n"
    "v_mul_f32 v44, v148, v86\n"
    "s_barrier\n"
    "v_mul_f32 v48, v148, v87\n"
    "v_fma_f32 v44, -v149, v87, v44\n"
    "v_fma_f32 v48, -v149, v86, -v48\n"
    "v_fma_f32 v88, v136, v86, v13\n"
    "v_fma_f32 v89, v136, v87, v17\n"
    "v_fma_f32 v88, -v137, v87, v88\n"
    "v_fmac_f32 v89, v137, v86\n"
    "v_mul_f32 v45, v148, v88\n"
    "v_mul_f32 v49, v148, v89\n"
    "v_fma_f32 v45, -v149, v89, v45\n"
    "v_fma_f32 v49, -v149, v88, -v49\n"
    "v_fma_f32 v86, v136, v88, v14\n"
    "v_fma_f32 v87, v136, v89, v18\n"
    "v_fma_f32 v86, -v137, v89, v86\n"
    "v_fmac_f32 v87, v137, v88\n"
    "v_mul_f32 v46, v148, v86\n"
    "v_mul_f32 v50, v148, v87\n"
    "v_fma_f32 v46, -v149, v87, v46\n"
    "v_fma_f32 v50, -v149, v86, -v50\n"
    "v_fma_f32 v88, v136, v86, v15\n"
    "v_fma_f32 v89, v136, v87, v19\n"
    "v_fma_f32 v88, -v137, v87, v88\n"
    "v_fmac_f32 v89, v137, v86\n"
    "v_mul_f32 v47, v148, v88\n"
    "v_mul_f32 v51, v148, v89\n"
    "v_fma_f32 v47, -v149, v89, v47\n"
    "v_fma_f32 v51, -v149, v88, -v51\n"
    "v_cvt_pk_bf16_f32 v56, v44, v45\n"
    "v_cvt_pk_bf16_f32 v57, v46, v47\n"
    "v_cvt_pk_bf16_f32 v58, v48, v49\n"
    "v_cvt_pk_bf16_f32 v59, v50, v51\n"
    "v_fma_f32 v86, v136, v88, v20\n"
    "v_fma_f32 v87, v136, v89, v24\n"
    "v_fma_f32 v86, -v137, v89, v86\n"
    "v_fmac_f32 v87, v137, v88\n"
    "v_mul_f32 v44, v148, v86\n"
    "v_mul_f32 v48, v148, v87\n"
    "v_fma_f32 v44, -v149, v87, v44\n"
    "s_barrier\n"
    "v_fma_f32 v48, -v149, v86, -v48\n"
    "v_fma_f32 v88, v136, v86, v21\n"
    "v_fma_f32 v89, v136, v87, v25\n"
    "v_fma_f32 v88, -v137, v87, v88\n"
    "v_fmac_f32 v89, v137, v86\n"
    "v_mul_f32 v45, v148, v88\n"
    "v_mul_f32 v49, v148, v89\n"
    "v_fma_f32 v45, -v149, v89, v45\n"
    "v_fma_f32 v49, -v149, v88, -v49\n"
    "v_fma_f32 v86, v136, v88, v22\n"
    "v_fma_f32 v87, v136, v89, v26\n"
    "v_fma_f32 v86, -v137, v89, v86\n"
    "v_fmac_f32 v87, v137, v88\n"
    "v_mul_f32 v46, v148, v86\n"
    "v_mul_f32 v50, v148, v87\n"
    "v_fma_f32 v46, -v149, v87, v46\n"
    "v_fma_f32 v50, -v149, v86, -v50\n"
    "v_fma_f32 v88, v136, v86, v23\n"
    "v_fma_f32 v89, v136, v87, v27\n"
    "v_fma_f32 v88, -v137, v87, v88\n"
    "v_fmac_f32 v89, v137, v86\n"
    "v_mul_f32 v47, v148, v88\n"
    "v_mul_f32 v51, v148, v89\n"
    "v_fma_f32 v47, -v149, v89, v47\n"
    "v_fma_f32 v51, -v149, v88, -v51\n"
    "v_cvt_pk_bf16_f32 v60, v44, v45\n"
    "v_cvt_pk_bf16_f32 v61, v46, v47\n"
    "v_cvt_pk_bf16_f32 v62, v48, v49\n"
    "v_cvt_pk_bf16_f32 v63, v50, v51\n"
    "v_fma_f32 v86, v136, v88, v28\n"
    "v_fma_f32 v87, v136, v89, v32\n"
    "v_fma_f32 v86, -v137, v89, v86\n"
    "v_fmac_f32 v87, v137, v88\n"
    "v_mul_f32 v44, v148, v86\n"
    "v_mul_f32 v48, v148, v87\n"
    "v_fma_f32 v44, -v149, v87, v44\n"
    "v_fma_f32 v48, -v149, v86, -v48\n"
    "v_fma_f32 v88, v136, v86, v29\n"
    "s_barrier\n"
    "v_fma_f32 v89, v136, v87, v33\n"
    "v_fma_f32 v88, -v137, v87, v88\n"
    "v_fmac_f32 v89, v137, v86\n"
    "v_mul_f32 v45, v148, v88\n"
    "v_mul_f32 v49, v148, v89\n"
    "v_fma_f32 v45, -v149, v89, v45\n"
    "v_fma_f32 v49, -v149, v88, -v49\n"
    "v_fma_f32 v86, v136, v88, v30\n"
    "v_fma_f32 v87, v136, v89, v34\n"
    "v_fma_f32 v86, -v137, v89, v86\n"
    "v_fmac_f32 v87, v137, v88\n"
    "v_mul_f32 v46, v148, v86\n"
    "v_mul_f32 v50, v148, v87\n"
    "v_fma_f32 v46, -v149, v87, v46\n"
    "v_fma_f32 v50, -v149, v86, -v50\n"
    "v_fma_f32 v88, v136, v86, v31\n"
    "v_fma_f32 v89, v136, v87, v35\n"
    "v_fma_f32 v88, -v137, v87, v88\n"
    "v_fmac_f32 v89, v137, v86\n"
    "v_mul_f32 v47, v148, v88\n"
    "v_mul_f32 v51, v148, v89\n"
    "v_fma_f32 v47, -v149, v89, v47\n"
    "v_fma_f32 v51, -v149, v88, -v51\n"
    "v_cvt_pk_bf16_f32 v64, v44, v45\n"
    "v_cvt_pk_bf16_f32 v65, v46, v47\n"
    "v_cvt_pk_bf16_f32 v66, v48, v49\n"
    "v_cvt_pk_bf16_f32 v67, v50, v51\n"
    "v_fma_f32 v86, v136, v88, v36\n"
    "v_fma_f32 v87, v136, v89, v40\n"
    "v_fma_f32 v86, -v137, v89, v86\n"
    "v_fmac_f32 v87, v137, v88\n"
    "v_mul_f32 v44, v148, v86\n"
    "v_mul_f32 v48, v148, v87\n"
    "v_fma_f32 v44, -v149, v87, v44\n"
    "v_fma_f32 v48, -v149, v86, -v48\n"
    ".Lpage3:\n"
    "s_cbranch_execz .Ltouch_next\n"
    "v_fma_f32 v88, v136, v86, v37\n"
    "v_fma_f32 v89, v136, v87, v41\n"
    "v_fma_f32 v88, -v137, v87, v88\n"
    "s_barrier\n"
    "s_add_u32 m0, s27, 8192\n"
    "s_add_u32 s19, s97, 73728\n"
    "buffer_load_dwordx4 v122, s[76:79], s19 offen offset:0 lds\n"
    "v_fmac_f32 v89, v137, v86\n"
    "v_mul_f32 v45, v148, v88\n"
    "v_mul_f32 v49, v148, v89\n"
    "v_fma_f32 v45, -v149, v89, v45\n"
    "v_fma_f32 v49, -v149, v88, -v49\n"
    "v_fma_f32 v86, v136, v88, v38\n"
    "v_fma_f32 v87, v136, v89, v42\n"
    "v_fma_f32 v86, -v137, v89, v86\n"
    "v_fmac_f32 v87, v137, v88\n"
    "v_mul_f32 v46, v148, v86\n"
    "buffer_load_dwordx4 v123, s[76:79], s19 offen offset:1024 lds\n"
    "v_mul_f32 v50, v148, v87\n"
    "v_fma_f32 v46, -v149, v87, v46\n"
    "v_fma_f32 v50, -v149, v86, -v50\n"
    "v_fma_f32 v88, v136, v86, v39\n"
    "v_fma_f32 v89, v136, v87, v43\n"
    "v_fma_f32 v88, -v137, v87, v88\n"
    "v_fmac_f32 v89, v137, v86\n"
    "v_mul_f32 v47, v148, v88\n"
    "v_mul_f32 v51, v148, v89\n"
    "v_fma_f32 v47, -v149, v89, v47\n"
    "s_add_u32 m0, s27, 4294928384\n"
    "s_add_u32 s19, s25, 165888\n"
    "buffer_load_dwordx4 v122, s[76:79], s19 offen offset:0 lds\n"
    "v_fma_f32 v51, -v149, v88, -v51\n"
    "v_cvt_pk_bf16_f32 v68, v44, v45\n"
    "v_cvt_pk_bf16_f32 v69, v46, v47\n"
    "v_cvt_pk_bf16_f32 v70, v48, v49\n"
    "v_cvt_pk_bf16_f32 v71, v50, v51\n"
    "v_mov_b32 v72, v150\n"
    "v_mov_b32 v73, v151\n"
    "v_mov_b32 v74, v152\n"
    "v_mov_b32 v75, v153\n"
    "v_mov_b32 v76, v156\n"
    "buffer_load_dwordx4 v123, s[76:79], s19 offen offset:1024 lds\n"
    "v_mov_b32 v77, v157\n"
    "v_mov_b32 v78, v158\n"
    "v_mov_b32 v79, v159\n"
    "v_mov_b32 v80, v160\n"
    "v_mov_b32 v81, v161\n"
    "s_barrier\n"
    "s_add_u32 m0, s27, 16384\n"
    "s_add_u32 s19, s97, 92160\n"
    "buffer_load_dwordx4 v122, s[76:79], s19 offen offset:0 lds\n"
    "buffer_load_dwordx4 v123, s[76:79], s19 offen offset:1024 lds\n"
    "ds_read_b64_tr_b16 v[100:101], v120 offset:0\n"
    "ds_read_b64_tr_b16 v[104:105], v120 offset:2048\n"
    "ds_read_b64_tr_b16 v[108:109], v121 offset:0\n"
    "ds_read_b64_tr_b16 v[112:113], v121 offset:2048\n"
    "s_add_u32 m0, s27, 24576\n"
    "s_add_u32 s19, s96, 147456\n"
    "buffer_load_dwordx4 v122, s[76:79], s19 offen offset:0 lds\n"
    "ds_write2st64_b64 v127, v[52:53], v[54:55] offset0:0 offset1:20\n"
    "buffer_load_dwordx4 v123, s[76:79], s19 offen offset:1024 lds\n"
    "s_waitcnt lgkmcnt(1)\n"
    "s_barrier\n"
    "s_add_u32 m0, s27, 32768\n"
    "s_add_u32 s19, s96, 165888\n"
    "buffer_load_dwordx4 v122, s[76:79], s19 offen offset:0 lds\n"
    "buffer_load_dwordx4 v123, s[76:79], s19 offen offset:1024 lds\n"
    "ds_read_b64_tr_b16 v[102:103], v120 offset:8192\n"
    "ds_read_b64_tr_b16 v[106:107], v120 offset:10240\n"
    "ds_read_b64_tr_b16 v[110:111], v121 offset:8192\n"
    "ds_read_b64_tr_b16 v[114:115], v121 offset:10240\n"
    "s_add_u32 m0, s27, 40960\n"
    "s_add_u32 s19, s97, 147456\n"
    "buffer_load_dwordx4 v122, s[76:79], s19 offen offset:0 lds\n"
    "ds_write2st64_b64 v127, v[56:57], v[58:59] offset0:2 offset1:22\n"
    "buffer_load_dwordx4 v123, s[76:79], s19 offen offset:1024 lds\n"
    "s_add_u32 m0, s27, 4294912000\n"
    "s_add_u32 s19, s25, 294912\n"
    "buffer_load_dwordx4 v122, s[76:79], s19 offen offset:0 lds\n"
    "buffer_load_dwordx4 v123, s[76:79], s19 offen offset:1024 lds\n"
    "s_waitcnt lgkmcnt(1)\n"
    "s_waitcnt vmcnt(8)\n"
    "s_barrier\n"
    "s_add_u32 m0, s27, 49152\n"
    "s_add_u32 s19, s97, 165888\n"
    "buffer_load_dwordx4 v122, s[76:79], s19 offen offset:0 lds\n"
    "buffer_load_dwordx4 v123, s[76:79], s19 offen offset:1024 lds\n"
    "ds_read_b64_tr_b16 v[84:85], v120 offset:24576\n"
    "ds_read_b64_tr_b16 v[88:89], v120 offset:26624\n"
    "ds_read_b64_tr_b16 v[92:93], v121 offset:24576\n"
    "ds_read_b64_tr_b16 v[96:97], v121 offset:26624\n"
    "s_add_u32 m0, s27, 57344\n"
    "s_add_u32 s19, s96, 221184\n"
    "buffer_load_dwordx4 v122, s[76:79], s19 offen offset:0 lds\n"
    "ds_write2st64_b64 v127, v[60:61], v[62:63] offset0:4 offset1:24\n"
    "buffer_load_dwordx4 v123, s[76:79], s19 offen offset:1024 lds\n"
    "s_add_u32 m0, s27, 4294944768\n"
    "s_add_u32 s19, s25, 313344\n"
    "buffer_load_dwordx4 v122, s[76:79], s19 offen offset:0 lds\n"
    "buffer_load_dwordx4 v123, s[76:79], s19 offen offset:1024 lds\n"
    "s_waitcnt lgkmcnt(1)\n"
    "s_waitcnt vmcnt(10)\n"
    "s_barrier\n"
    "s_add_u32 m0, s27, 8192\n"
    "s_add_u32 s19, s96, 239616\n"
    "buffer_load_dwordx4 v122, s[76:79], s19 offen offset:0 lds\n"
    "buffer_load_dwordx4 v123, s[76:79], s19 offen offset:1024 lds\n"
    "ds_read_b64_tr_b16 v[86:87], v120 offset:16384\n"
    "ds_read_b64_tr_b16 v[90:91], v120 offset:18432\n"
    "ds_read_b64_tr_b16 v[94:95], v121 offset:16384\n"
    "ds_read_b64_tr_b16 v[98:99], v121 offset:18432\n"
    "s_add_u32 m0, s27, 16384\n"
    "s_add_u32 s19, s97, 221184\n"
    "buffer_load_dwordx4 v122, s[76:79], s19 offen offset:0 lds\n"
    "ds_write2st64_b64 v127, v[64:65], v[66:67] offset0:6 offset1:26\n"
    "buffer_load_dwordx4 v123, s[76:79], s19 offen offset:1024 lds\n"
    "s_add_u32 m0, s27, 4294936576\n"
    "s_add_u32 s19, s25, 442368\n"
    "buffer_load_dwordx4 v122, s[76:79], s19 offen offset:0 lds\n"
    "buffer_load_dwordx4 v123, s[76:79], s19 offen offset:1024 lds\n"
    "s_waitcnt lgkmcnt(1)\n"
    "s_waitcnt vmcnt(10)\n"
    "s_barrier\n"
    "s_add_u32 m0, s27, 24576\n"
    "s_add_u32 s19, s97, 239616\n"
    "buffer_load_dwordx4 v122, s[76:79], s19 offen offset:0 lds\n"
    "buffer_load_dwordx4 v123, s[76:79], s19 offen offset:1024 lds\n"
    "ds_read_b64_tr_b16 v[132:133], v120 offset:0\n"
    "ds_read_b64_tr_b16 v[136:137], v120 offset:2048\n"
    "ds_read_b64_tr_b16 v[140:141], v121 offset:0\n"
    "ds_read_b64_tr_b16 v[144:145], v121 offset:2048\n"
    "s_add_u32 m0, s27, 32768\n"
    "s_add_u32 s19, s98, 0\n"
    "buffer_load_dwordx4 v122, s[80:83], s19 offen offset:0 lds\n"
    "ds_write2st64_b64 v127, v[68:69], v[70:71] offset0:8 offset1:28\n"
    "buffer_load_dwordx4 v123, s[80:83], s19 offen offset:1024 lds\n"
    "s_waitcnt lgkmcnt(1)\n"
    "s_waitcnt vmcnt(8)\n"
    "s_barrier\n"
    "s_add_u32 m0, s27, 40960\n"
    "s_add_u32 s19, s98, 18432\n"
    "buffer_load_dwordx4 v122, s[80:83], s19 offen offset:0 lds\n"
    "buffer_load_dwordx4 v123, s[80:83], s19 offen offset:1024 lds\n"
    "ds_read_b64_tr_b16 v[134:135], v120 offset:32768\n"
    "ds_read_b64_tr_b16 v[138:139], v120 offset:34816\n"
    "ds_read_b64_tr_b16 v[142:143], v121 offset:32768\n"
    "ds_read_b64_tr_b16 v[146:147], v121 offset:34816\n"
    "s_add_u32 m0, s27, 49152\n"
    "s_add_u32 s19, s99, 0\n"
    "buffer_load_dwordx4 v122, s[80:83], s19 offen offset:0 lds\n"
    "buffer_load_dwordx4 v123, s[80:83], s19 offen offset:1024 lds\n"
    "s_waitcnt lgkmcnt(0)\n"
    "s_waitcnt vmcnt(6)\n"
    "s_barrier\n"
    "s_add_u32 m0, s27, 57344\n"
    "s_add_u32 s19, s99, 18432\n"
    "buffer_load_dwordx4 v122, s[80:83], s19 offen offset:0 lds\n"
    "buffer_load_dwordx4 v123, s[80:83], s19 offen offset:1024 lds\n"
    "ds_read_b64_tr_b16 v[148:149], v120 offset:24576\n"
    "ds_read_b64_tr_b16 v[152:153], v120 offset:26624\n"
    "ds_read_b64_tr_b16 v[156:157], v121 offset:24576\n"
    "ds_read_b64_tr_b16 v[160:161], v121 offset:26624\n"
    "s_add_u32 m0, s27, 8192\n"
    "s_add_u32 s19, s98, 0\n"
    "buffer_load_dwordx4 v122, s[84:87], s19 offen offset:0 lds\n"
    "buffer_load_dwordx4 v123, s[84:87], s19 offen offset:1024 lds\n"
    "s_add_u32 m0, s27, 4294944768\n"
    "s_add_u32 s19, s25, 460800\n"
    "buffer_load_dwordx4 v122, s[76:79], s19 offen offset:0 lds\n"
    "buffer_load_dwordx4 v123, s[76:79], s19 offen offset:1024 lds\n"
    "s_waitcnt lgkmcnt(0)\n"
    "s_setprio 3\n"
    "s_waitcnt vmcnt(8)\n"
    "s_barrier\n"
    "s_add_u32 m0, s27, 16384\n"
    "s_add_u32 s19, s98, 18432\n"
    "buffer_load_dwordx4 v122, s[84:87], s19 offen offset:0 lds\n"
    "buffer_load_dwordx4 v123, s[84:87], s19 offen offset:1024 lds\n"
    "s_add_u32 m0, s27, 24576\n"
    "s_add_u32 s19, s99, 0\n"
    "buffer_load_dwordx4 v122, s[84:87], s19 offen offset:0 lds\n"
    "buffer_load_dwordx4 v123, s[84:87], s19 offen offset:1024 lds\n"
    "s_waitcnt vmcnt(4)\n"
    "s_barrier\n"
    "s_add_u32 m0, s27, 32768\n"
    "s_add_u32 s19, s99, 18432\n"
    "buffer_load_dwordx4 v122, s[84:87], s19 offen offset:0 lds\n"
    "buffer_load_dwordx4 v123, s[84:87], s19 offen offset:1024 lds\n"
    "ds_read_b64_tr_b16 v[150:151], v120 offset:32768\n"
    "ds_read_b64_tr_b16 v[154:155], v120 offset:34816\n"
    "ds_read_b64_tr_b16 v[158:159], v121 offset:32768\n"
    "ds_read_b64_tr_b16 v[162:163], v121 offset:34816\n"
    "s_add_u32 m0, s27, 40960\n"
    "s_add_u32 s19, s96, 294912\n"
    "buffer_load_dwordx4 v122, s[76:79], s19 offen offset:0 lds\n"
    "buffer_load_dwordx4 v123, s[76:79], s19 offen offset:1024 lds\n"
    "s_waitcnt lgkmcnt(0)\n"
    "s_waitcnt vmcnt(6)\n"
    "s_barrier\n"
    "s_add_u32 m0, s27, 49152\n"
    "s_add_u32 s19, s96, 313344\n"
    "buffer_load_dwordx4 v122, s[76:79], s19 offen offset:0 lds\n"
    "buffer_load_dwordx4 v123, s[76:79], s19 offen offset:1024 lds\n"
    "s_add_u32 m0, s27, 57344\n"
    "s_add_u32 s19, s97, 294912\n"
    "buffer_load_dwordx4 v122, s[76:79], s19 offen offset:0 lds\n"
    "buffer_load_dwordx4 v123, s[76:79], s19 offen offset:1024 lds\n"
    "s_waitcnt vmcnt(6)\n"
    "s_barrier\n"
    "s_add_u32 m0, s27, 8192\n"
    "s_add_u32 s19, s97, 313344\n"
    "buffer_load_dwordx4 v122, s[76:79], s19 offen offset:0 lds\n"
    "buffer_load_dwordx4 v123, s[76:79], s19 offen offset:1024 lds\n"
    "s_add_u32 m0, s27, 16384\n"
    "s_add_u32 s19, s96, 368640\n"
    "buffer_load_dwordx4 v122, s[76:79], s19 offen offset:0 lds\n"
    "buffer_load_dwordx4 v123, s[76:79], s19 offen offset:1024 lds\n"
    "s_waitcnt vmcnt(6)\n"
    "s_barrier\n"
    "s_add_u32 m0, s27, 24576\n"
    "s_add_u32 s19, s96, 387072\n"
    "buffer_load_dwordx4 v122, s[76:79], s19 offen offset:0 lds\n"
    "buffer_load_dwordx4 v123, s[76:79], s19 offen offset:1024 lds\n"
    "s_add_u32 m0, s27, 32768\n"
    "s_add_u32 s19, s97, 368640\n"
    "buffer_load_dwordx4 v122, s[76:79], s19 offen offset:0 lds\n"
    "buffer_load_dwordx4 v123, s[76:79], s19 offen offset:1024 lds\n"
    "s_waitcnt vmcnt(6)\n"
    "s_barrier\n"
    "s_add_u32 m0, s27, 40960\n"
    "s_add_u32 s19, s97, 387072\n"
    "buffer_load_dwordx4 v122, s[76:79], s19 offen offset:0 lds\n"
    "buffer_load_dwordx4 v123, s[76:79], s19 offen offset:1024 lds\n"
    "s_add_u32 m0, s27, 49152\n"
    "s_add_u32 s19, s96, 442368\n"
    "buffer_load_dwordx4 v122, s[76:79], s19 offen offset:0 lds\n"
    "buffer_load_dwordx4 v123, s[76:79], s19 offen offset:1024 lds\n"
    "s_waitcnt vmcnt(6)\n"
    "s_barrier\n"
    "s_add_u32 m0, s27, 57344\n"
    "s_add_u32 s19, s96, 460800\n"
    "buffer_load_dwordx4 v122, s[76:79], s19 offen offset:0 lds\n"
    "buffer_load_dwordx4 v123, s[76:79], s19 offen offset:1024 lds\n"
    "s_add_u32 m0, s27, 8192\n"
    "s_add_u32 s19, s97, 442368\n"
    "buffer_load_dwordx4 v122, s[76:79], s19 offen offset:0 lds\n"
    "buffer_load_dwordx4 v123, s[76:79], s19 offen offset:1024 lds\n"
    "s_waitcnt vmcnt(6)\n"
    "s_barrier\n"
    "s_add_u32 m0, s27, 16384\n"
    "s_add_u32 s19, s97, 460800\n"
    "buffer_load_dwordx4 v122, s[76:79], s19 offen offset:0 lds\n"
    "buffer_load_dwordx4 v123, s[76:79], s19 offen offset:1024 lds\n"
    "s_add_u32 m0, s27, 24576\n"
    "s_add_u32 s19, s96, 516096\n"
    "buffer_load_dwordx4 v122, s[76:79], s19 offen offset:0 lds\n"
    "buffer_load_dwordx4 v123, s[76:79], s19 offen offset:1024 lds\n"
    "s_waitcnt vmcnt(6)\n"
    "s_barrier\n"
    "s_mov_b32 s30, 162432\n"
    "s_add_u32 m0, s27, 32768\n"
    "s_add_u32 s19, s96, 534528\n"
    "buffer_load_dwordx4 v122, s[76:79], s19 offen offset:0 lds\n"
    "v_add_u32 v2, s30, v128\n"
    "s_mul_i32 s30, s14, 10240\n"
    "buffer_load_dwordx4 v123, s[76:79], s19 offen offset:1024 lds\n"
    "v_add_u32 v116, s30, v126\n"
    "s_add_u32 m0, s27, 40960\n"
    "s_add_u32 s19, s97, 516096\n"
    "buffer_load_dwordx4 v122, s[76:79], s19 offen offset:0 lds\n"
    "v_add_u32 v117, 51200, v116\n"
    "s_mul_i32 s30, s12, 10240\n"
    "s_add_u32 s30, s30, 112640\n"
    "buffer_load_dwordx4 v123, s[76:79], s19 offen offset:1024 lds\n"
    "v_add_u32 v127, s30, v126\n"
    "s_cmp_eq_u32 s14, 0\n"
    "s_cselect_b64 s[34:35], -1, 0\n"
    "s_mul_i32 s28, s10, 9\n"
    "s_add_u32 s28, s28, s11\n"
    "s_mul_i32 s29, s28, 81920\n"
    "s_mul_i32 s30, s14, 10240\n"
    "s_add_u32 s29, s29, s30\n"
    "s_lshl_b32 s30, s15, 10\n"
    "s_add_u32 s29, s29, s30\n"
    "s_add_u32 s29, s29, 1048576\n"
    "s_lshl_b32 s28, s28, 11\n"
    "s_lshl_b32 s30, s12, 7\n"
    "s_add_u32 s28, s28, s30\n"
    "s_add_u32 s28, s28, 524296\n"
    "ds_read2_b32 v[44:45], v2 offset0:0 offset1:80\n"
    "ds_read2_b32 v[46:47], v2 offset0:16 offset1:96\n"
    "ds_read2_b32 v[48:49], v2 offset0:32 offset1:112\n"
    "ds_read2_b32 v[50:51], v2 offset0:48 offset1:128\n"
    "ds_read2_b32 v[52:53], v2 offset0:64 offset1:144\n"
    "s_mov_b32 s22, s17\n"
    "s_mov_b32 s23, s18\n"
    "s_waitcnt vmcnt(6)\n"
    "s_barrier\n"
    "s_add_u32 m0, s27, 49152\n"
    "s_add_u32 s19, s97, 534528\n"
    "buffer_load_dwordx4 v122, s[76:79], s19 offen offset:0 lds\n"
    "v_lshlrev_b32 v54, 16, v130\n"
    "v_and_b32 v55, s5, v130\n"
    "v_lshlrev_b32 v56, 16, v131\n"
    "v_and_b32 v57, s5, v131\n"
    "buffer_load_dwordx4 v123, s[76:79], s19 offen offset:1024 lds\n"
    "s_add_u32 s20, s11, s14\n"
    "s_add_u32 s20, s20, 5\n"
    "s_sub_u32 s19, s20, 9\n"
    "s_cmp_ge_u32 s20, 9\n"
    "s_cselect_b32 s20, s19, s20\n"
    "s_lshl_b32 s19, s11, 4\n"
    "s_add_u32 s19, s19, s15\n"
    "s_mul_i32 s19, s19, 36864\n"
    "s_lshl_b32 s21, s20, 7\n"
    "s_add_u32 s96, s19, s21\n"
    "s_lshl_b32 s19, s20, 1\n"
    "s_add_u32 s19, s19, s15\n"
    "s_mul_i32 s19, s19, 36864\n"
    "s_lshl_b32 s21, s11, 7\n"
    "s_add_u32 s98, s19, s21\n"
    "v_lshlrev_b32 v58, 16, v164\n"
    "v_and_b32 v59, s5, v164\n"
    "v_lshlrev_b32 v60, 16, v165\n"
    "s_add_u32 m0, s27, 57344\n"
    "s_add_u32 s19, s96, 0\n"
    "buffer_load_dwordx4 v122, s[76:79], s19 offen offset:0 lds\n"
    "v_and_b32 v61, s5, v165\n"
    "s_waitcnt lgkmcnt(0)\n"
    "v_lshlrev_b32 v62, 16, v72\n"
    "v_and_b32 v63, s5, v72\n"
    "v_fma_f32 v64, v62, v44, -v45\n"
    "buffer_load_dwordx4 v123, s[76:79], s19 offen offset:1024 lds\n"
    "v_add_f32 v66, v62, v58\n"
    "v_fma_f32 v65, v63, v44, -v45\n"
    "v_add_f32 v67, v63, v59\n"
    "s_waitcnt vmcnt(6)\n"
    "s_barrier\n"
    "s_add_u32 m0, s27, 8192\n"
    "s_add_u32 s19, s96, 18432\n"
    "buffer_load_dwordx4 v122, s[76:79], s19 offen offset:0 lds\n"
    "v_fmac_f32 v66, v64, v54\n"
    "v_fmac_f32 v67, v65, v55\n"
    "v_cndmask_b32_e64 v4, 0, v66, s[34:35]\n"
    "v_cndmask_b32_e64 v8, v66, 0, s[34:35]\n"
    "buffer_load_dwordx4 v123, s[76:79], s19 offen offset:1024 lds\n"
    "v_cndmask_b32_e64 v5, 0, v67, s[34:35]\n"
    "v_cndmask_b32_e64 v9, v67, 0, s[34:35]\n"
    "v_lshlrev_b32 v62, 16, v73\n"
    "s_add_u32 m0, s27, 16384\n"
    "s_add_u32 s19, s96, 73728\n"
    "buffer_load_dwordx4 v122, s[76:79], s19 offen offset:0 lds\n"
    "v_and_b32 v63, s5, v73\n"
    "v_fma_f32 v64, v62, v44, -v45\n"
    "v_add_f32 v66, v62, v60\n"
    "v_fma_f32 v65, v63, v44, -v45\n"
    "buffer_load_dwordx4 v123, s[76:79], s19 offen offset:1024 lds\n"
    "v_add_f32 v67, v63, v61\n"
    "v_fmac_f32 v66, v64, v56\n"
    "v_fmac_f32 v67, v65, v57\n"
    "s_waitcnt vmcnt(6)\n"
    "s_barrier\n"
    "s_add_u32 m0, s27, 24576\n"
    "s_add_u32 s19, s96, 92160\n"
    "buffer_load_dwordx4 v122, s[76:79], s19 offen offset:0 lds\n"
    "v_cndmask_b32_e64 v6, 0, v66, s[34:35]\n"
    "v_cndmask_b32_e64 v10, v66, 0, s[34:35]\n"
    "v_cndmask_b32_e64 v7, 0, v67, s[34:35]\n"
    "v_cndmask_b32_e64 v11, v67, 0, s[34:35]\n"
    "buffer_load_dwordx4 v123, s[76:79], s19 offen offset:1024 lds\n"
    "v_lshlrev_b32 v62, 16, v74\n"
    "v_and_b32 v63, s5, v74\n"
    "v_fma_f32 v64, v62, v46, -v47\n"
    "s_add_u32 m0, s27, 32768\n"
    "s_add_u32 s19, s96, 147456\n"
    "buffer_load_dwordx4 v122, s[76:79], s19 offen offset:0 lds\n"
    "v_add_f32 v66, v62, v58\n"
    "v_fma_f32 v65, v63, v46, -v47\n"
    "v_add_f32 v67, v63, v59\n"
    "v_fmac_f32 v66, v64, v54\n"
    "buffer_load_dwordx4 v123, s[76:79], s19 offen offset:1024 lds\n"
    "v_fmac_f32 v67, v65, v55\n"
    "v_cndmask_b32_e64 v12, 0, v66, s[34:35]\n"
    "v_cndmask_b32_e64 v16, v66, 0, s[34:35]\n"
    "s_waitcnt vmcnt(6)\n"
    "s_barrier\n"
    "s_add_u32 m0, s27, 40960\n"
    "s_add_u32 s19, s96, 165888\n"
    "buffer_load_dwordx4 v122, s[76:79], s19 offen offset:0 lds\n"
    "v_cndmask_b32_e64 v13, 0, v67, s[34:35]\n"
    "v_cndmask_b32_e64 v17, v67, 0, s[34:35]\n"
    "v_lshlrev_b32 v62, 16, v75\n"
    "v_and_b32 v63, s5, v75\n"
    "buffer_load_dwordx4 v123, s[76:79], s19 offen offset:1024 lds\n"
    "v_fma_f32 v64, v62, v46, -v47\n"
    "v_add_f32 v66, v62, v60\n"
    "v_fma_f32 v65, v63, v46, -v47\n"
    "s_add_u32 m0, s27, 49152\n"
    "s_add_u32 s19, s96, 221184\n"
    "buffer_load_dwordx4 v122, s[76:79], s19 offen offset:0 lds\n"
    "v_add_f32 v67, v63, v61\n"
    "v_fmac_f32 v66, v64, v56\n"
    "v_fmac_f32 v67, v65, v57\n"
    "v_cndmask_b32_e64 v14, 0, v66, s[34:35]\n"
    "buffer_load_dwordx4 v123, s[76:79], s19 offen offset:1024 lds\n"
    "v_cndmask_b32_e64 v18, v66, 0, s[34:35]\n"
    "v_cndmask_b32_e64 v15, 0, v67, s[34:35]\n"
    "v_cndmask_b32_e64 v19, v67, 0, s[34:35]\n"
    "s_waitcnt vmcnt(6)\n"
    "s_barrier\n"
    "s_add_u32 m0, s27, 57344\n"
    "s_add_u32 s19, s96, 239616\n"
    "buffer_load_dwordx4 v122, s[76:79], s19 offen offset:0 lds\n"
    "v_lshlrev_b32 v62, 16, v76\n"
    "v_and_b32 v63, s5, v76\n"
    "v_fma_f32 v64, v62, v48, -v49\n"
    "v_add_f32 v66, v62, v58\n"
    "buffer_load_dwordx4 v123, s[76:79], s19 offen offset:1024 lds\n"
    "v_fma_f32 v65, v63, v48, -v49\n"
    "v_add_f32 v67, v63, v59\n"
    "v_fmac_f32 v66, v64, v54\n"
    "s_add_u32 m0, s27, 8192\n"
    "s_add_u32 s19, s98, 0\n"
    "buffer_load_dwordx4 v122, s[80:83], s19 offen offset:0 lds\n"
    "v_fmac_f32 v67, v65, v55\n"
    "v_cndmask_b32_e64 v20, 0, v66, s[34:35]\n"
    "v_cndmask_b32_e64 v24, v66, 0, s[34:35]\n"
    "v_cndmask_b32_e64 v21, 0, v67, s[34:35]\n"
    "buffer_load_dwordx4 v123, s[80:83], s19 offen offset:1024 lds\n"
    "v_cndmask_b32_e64 v25, v67, 0, s[34:35]\n"
    "v_lshlrev_b32 v62, 16, v77\n"
    "v_and_b32 v63, s5, v77\n"
    "s_waitcnt vmcnt(6)\n"
    "s_barrier\n"
    "s_add_u32 m0, s27, 16384\n"
    "s_add_u32 s19, s98, 18432\n"
    "buffer_load_dwordx4 v122, s[80:83], s19 offen offset:0 lds\n"
    "v_fma_f32 v64, v62, v48, -v49\n"
    "v_add_f32 v66, v62, v60\n"
    "v_fma_f32 v65, v63, v48, -v49\n"
    "v_add_f32 v67, v63, v61\n"
    "buffer_load_dwordx4 v123, s[80:83], s19 offen offset:1024 lds\n"
    "v_fmac_f32 v66, v64, v56\n"
    "v_fmac_f32 v67, v65, v57\n"
    "v_cndmask_b32_e64 v22, 0, v66, s[34:35]\n"
    "s_add_u32 m0, s27, 24576\n"
    "s_add_u32 s19, s98, 0\n"
    "buffer_load_dwordx4 v122, s[84:87], s19 offen offset:0 lds\n"
    "v_cndmask_b32_e64 v26, v66, 0, s[34:35]\n"
    "v_cndmask_b32_e64 v23, 0, v67, s[34:35]\n"
    "v_cndmask_b32_e64 v27, v67, 0, s[34:35]\n"
    "v_lshlrev_b32 v62, 16, v78\n"
    "buffer_load_dwordx4 v123, s[84:87], s19 offen offset:1024 lds\n"
    "v_and_b32 v63, s5, v78\n"
    "v_fma_f32 v64, v62, v50, -v51\n"
    "v_add_f32 v66, v62, v58\n"
    "s_waitcnt vmcnt(6)\n"
    "s_barrier\n"
    "s_add_u32 m0, s27, 32768\n"
    "s_add_u32 s19, s98, 18432\n"
    "buffer_load_dwordx4 v122, s[84:87], s19 offen offset:0 lds\n"
    "v_fma_f32 v65, v63, v50, -v51\n"
    "v_add_f32 v67, v63, v59\n"
    "v_fmac_f32 v66, v64, v54\n"
    "v_fmac_f32 v67, v65, v55\n"
    "buffer_load_dwordx4 v123, s[84:87], s19 offen offset:1024 lds\n"
    "v_cndmask_b32_e64 v28, 0, v66, s[34:35]\n"
    "v_cndmask_b32_e64 v32, v66, 0, s[34:35]\n"
    "v_cndmask_b32_e64 v29, 0, v67, s[34:35]\n"
    "s_add_u32 m0, s27, 40960\n"
    "s_add_u32 s19, s96, 294912\n"
    "buffer_load_dwordx4 v122, s[76:79], s19 offen offset:0 lds\n"
    "v_cndmask_b32_e64 v33, v67, 0, s[34:35]\n"
    "v_lshlrev_b32 v62, 16, v79\n"
    "v_and_b32 v63, s5, v79\n"
    "v_fma_f32 v64, v62, v50, -v51\n"
    "buffer_load_dwordx4 v123, s[76:79], s19 offen offset:1024 lds\n"
    "v_add_f32 v66, v62, v60\n"
    "v_fma_f32 v65, v63, v50, -v51\n"
    "v_add_f32 v67, v63, v61\n"
    "s_waitcnt vmcnt(6)\n"
    "s_barrier\n"
    "s_add_u32 m0, s27, 49152\n"
    "s_add_u32 s19, s96, 313344\n"
    "buffer_load_dwordx4 v122, s[76:79], s19 offen offset:0 lds\n"
    "v_fmac_f32 v66, v64, v56\n"
    "v_fmac_f32 v67, v65, v57\n"
    "v_cndmask_b32_e64 v30, 0, v66, s[34:35]\n"
    "v_cndmask_b32_e64 v34, v66, 0, s[34:35]\n"
    "buffer_load_dwordx4 v123, s[76:79], s19 offen offset:1024 lds\n"
    "v_cndmask_b32_e64 v31, 0, v67, s[34:35]\n"
    "v_cndmask_b32_e64 v35, v67, 0, s[34:35]\n"
    "v_lshlrev_b32 v62, 16, v80\n"
    "s_add_u32 m0, s27, 57344\n"
    "s_add_u32 s19, s96, 368640\n"
    "buffer_load_dwordx4 v122, s[76:79], s19 offen offset:0 lds\n"
    "v_and_b32 v63, s5, v80\n"
    "v_fma_f32 v64, v62, v52, -v53\n"
    "v_add_f32 v66, v62, v58\n"
    "v_fma_f32 v65, v63, v52, -v53\n"
    "buffer_load_dwordx4 v123, s[76:79], s19 offen offset:1024 lds\n"
    "v_add_f32 v67, v63, v59\n"
    "v_fmac_f32 v66, v64, v54\n"
    "v_fmac_f32 v67, v65, v55\n"
    "s_waitcnt vmcnt(6)\n"
    "s_barrier\n"
    "s_add_u32 m0, s27, 8192\n"
    "s_add_u32 s19, s96, 387072\n"
    "buffer_load_dwordx4 v122, s[76:79], s19 offen offset:0 lds\n"
    "v_cndmask_b32_e64 v36, 0, v66, s[34:35]\n"
    "v_cndmask_b32_e64 v40, v66, 0, s[34:35]\n"
    "v_cndmask_b32_e64 v37, 0, v67, s[34:35]\n"
    "v_cndmask_b32_e64 v41, v67, 0, s[34:35]\n"
    "buffer_load_dwordx4 v123, s[76:79], s19 offen offset:1024 lds\n"
    "v_lshlrev_b32 v62, 16, v81\n"
    "v_and_b32 v63, s5, v81\n"
    "v_fma_f32 v64, v62, v52, -v53\n"
    "s_add_u32 m0, s27, 16384\n"
    "s_add_u32 s19, s96, 442368\n"
    "buffer_load_dwordx4 v122, s[76:79], s19 offen offset:0 lds\n"
    "v_add_f32 v66, v62, v60\n"
    "v_fma_f32 v65, v63, v52, -v53\n"
    "v_add_f32 v67, v63, v61\n"
    "v_fmac_f32 v66, v64, v56\n"
    "buffer_load_dwordx4 v123, s[76:79], s19 offen offset:1024 lds\n"
    "v_fmac_f32 v67, v65, v57\n"
    "v_cndmask_b32_e64 v38, 0, v66, s[34:35]\n"
    "v_cndmask_b32_e64 v42, v66, 0, s[34:35]\n"
    "s_waitcnt vmcnt(6)\n"
    "s_barrier\n"
    "s_add_u32 m0, s27, 24576\n"
    "s_add_u32 s19, s96, 460800\n"
    "buffer_load_dwordx4 v122, s[76:79], s19 offen offset:0 lds\n"
    "v_cndmask_b32_e64 v39, 0, v67, s[34:35]\n"
    "v_cndmask_b32_e64 v43, v67, 0, s[34:35]\n"
    "buffer_load_dwordx4 v123, s[76:79], s19 offen offset:1024 lds\n"
    "s_add_u32 m0, s27, 32768\n"
    "s_add_u32 s19, s96, 516096\n"
    "buffer_load_dwordx4 v122, s[76:79], s19 offen offset:0 lds\n"
    "buffer_load_dwordx4 v123, s[76:79], s19 offen offset:1024 lds\n"
    "s_waitcnt vmcnt(6)\n"
    "s_barrier\n"
    "s_add_u32 m0, s27, 40960\n"
    "s_add_u32 s19, s96, 534528\n"
    "buffer_load_dwordx4 v122, s[76:79], s19 offen offset:0 lds\n"
    "buffer_load_dwordx4 v123, s[76:79], s19 offen offset:1024 lds\n"
    "s_add_u32 s20, s11, s14\n"
    "s_add_u32 s20, s20, 7\n"
    "s_sub_u32 s19, s20, 9\n"
    "s_cmp_ge_u32 s20, 9\n"
    "s_cselect_b32 s20, s19, s20\n"
    "s_lshl_b32 s19, s11, 4\n"
    "s_add_u32 s19, s19, s15\n"
    "s_mul_i32 s19, s19, 36864\n"
    "s_lshl_b32 s21, s20, 7\n"
    "s_add_u32 s96, s19, s21\n"
    "s_lshl_b32 s19, s20, 1\n"
    "s_add_u32 s19, s19, s15\n"
    "s_mul_i32 s19, s19, 36864\n"
    "s_lshl_b32 s21, s11, 7\n"
    "s_add_u32 s98, s19, s21\n"
    "s_add_u32 m0, s27, 49152\n"
    "s_add_u32 s19, s96, 0\n"
    "buffer_load_dwordx4 v122, s[76:79], s19 offen offset:0 lds\n"
    "buffer_load_dwordx4 v123, s[76:79], s19 offen offset:1024 lds\n"
    "s_waitcnt vmcnt(6)\n"
    "s_barrier\n"
    "s_add_u32 m0, s27, 57344\n"
    "s_add_u32 s19, s96, 18432\n"
    "buffer_load_dwordx4 v122, s[76:79], s19 offen offset:0 lds\n"
    "buffer_load_dwordx4 v123, s[76:79], s19 offen offset:1024 lds\n"
    "s_add_u32 m0, s27, 8192\n"
    "s_add_u32 s19, s96, 73728\n"
    "buffer_load_dwordx4 v122, s[76:79], s19 offen offset:0 lds\n"
    "buffer_load_dwordx4 v123, s[76:79], s19 offen offset:1024 lds\n"
    "s_waitcnt vmcnt(6)\n"
    "s_barrier\n"
    "s_add_u32 m0, s27, 16384\n"
    "s_add_u32 s19, s96, 92160\n"
    "buffer_load_dwordx4 v122, s[76:79], s19 offen offset:0 lds\n"
    "buffer_load_dwordx4 v123, s[76:79], s19 offen offset:1024 lds\n"
    "s_add_u32 m0, s27, 24576\n"
    "s_add_u32 s19, s96, 147456\n"
    "buffer_load_dwordx4 v122, s[76:79], s19 offen offset:0 lds\n"
    "buffer_load_dwordx4 v123, s[76:79], s19 offen offset:1024 lds\n"
    "s_waitcnt vmcnt(6)\n"
    "s_barrier\n"
    "s_add_u32 m0, s27, 32768\n"
    "s_add_u32 s19, s96, 165888\n"
    "buffer_load_dwordx4 v122, s[76:79], s19 offen offset:0 lds\n"
    "buffer_load_dwordx4 v123, s[76:79], s19 offen offset:1024 lds\n"
    "s_add_u32 m0, s27, 40960\n"
    "s_add_u32 s19, s96, 221184\n"
    "buffer_load_dwordx4 v122, s[76:79], s19 offen offset:0 lds\n"
    "buffer_load_dwordx4 v123, s[76:79], s19 offen offset:1024 lds\n"
    "s_waitcnt vmcnt(6)\n"
    "s_barrier\n"
    "s_add_u32 m0, s27, 49152\n"
    "s_add_u32 s19, s96, 239616\n"
    "buffer_load_dwordx4 v122, s[76:79], s19 offen offset:0 lds\n"
    "buffer_load_dwordx4 v123, s[76:79], s19 offen offset:1024 lds\n"
    "s_add_u32 m0, s27, 57344\n"
    "s_add_u32 s19, s98, 0\n"
    "buffer_load_dwordx4 v122, s[80:83], s19 offen offset:0 lds\n"
    "buffer_load_dwordx4 v123, s[80:83], s19 offen offset:1024 lds\n"
    "s_waitcnt vmcnt(6)\n"
    "s_barrier\n"
    "s_add_u32 m0, s27, 8192\n"
    "s_add_u32 s19, s98, 18432\n"
    "buffer_load_dwordx4 v122, s[80:83], s19 offen offset:0 lds\n"
    "buffer_load_dwordx4 v123, s[80:83], s19 offen offset:1024 lds\n"
    "s_add_u32 m0, s27, 16384\n"
    "s_add_u32 s19, s98, 0\n"
    "buffer_load_dwordx4 v122, s[84:87], s19 offen offset:0 lds\n"
    "buffer_load_dwordx4 v123, s[84:87], s19 offen offset:1024 lds\n"
    "s_waitcnt vmcnt(6)\n"
    "s_barrier\n"
    "s_add_u32 m0, s27, 24576\n"
    ".Lpage4:\n"
    "s_cbranch_execz .Ltouch_next\n"
    "s_add_u32 s19, s98, 18432\n"
    "buffer_load_dwordx4 v122, s[84:87], s19 offen offset:0 lds\n"
    "buffer_load_dwordx4 v123, s[84:87], s19 offen offset:1024 lds\n"
    "s_add_u32 m0, s27, 32768\n"
    "s_add_u32 s19, s96, 294912\n"
    "buffer_load_dwordx4 v122, s[76:79], s19 offen offset:0 lds\n"
    "buffer_load_dwordx4 v123, s[76:79], s19 offen offset:1024 lds\n"
    "s_mov_b64 exec, 1\n"
    "buffer_load_dwordx2 v[164:165], off, s[68:71], s28 offset:0 sc1\n"
    "s_mov_b64 exec, -1\n"
    "s_waitcnt vmcnt(7)\n"
    "s_barrier\n"
    "s_add_u32 m0, s27, 40960\n"
    "s_add_u32 s19, s96, 313344\n"
    "buffer_load_dwordx4 v122, s[76:79], s19 offen offset:0 lds\n"
    "buffer_load_dwordx4 v123, s[76:79], s19 offen offset:1024 lds\n"
    "s_add_u32 m0, s27, 49152\n"
    "s_add_u32 s19, s96, 368640\n"
    "buffer_load_dwordx4 v122, s[76:79], s19 offen offset:0 lds\n"
    "buffer_load_dwordx4 v123, s[76:79], s19 offen offset:1024 lds\n"
    "s_waitcnt vmcnt(7)\n"
    "s_barrier\n"
    "s_add_u32 m0, s27, 57344\n"
    "s_add_u32 s19, s96, 387072\n"
    "buffer_load_dwordx4 v122, s[76:79], s19 offen offset:0 lds\n"
    "buffer_load_dwordx4 v123, s[76:79], s19 offen offset:1024 lds\n"
    "s_add_u32 m0, s27, 8192\n"
    "s_add_u32 s19, s96, 442368\n"
    "buffer_load_dwordx4 v122, s[76:79], s19 offen offset:0 lds\n"
    "buffer_load_dwordx4 v123, s[76:79], s19 offen offset:1024 lds\n"
    "s_waitcnt vmcnt(8)\n"
    "s_mov_b32 s24, 1048576\n"
    ".LT_poll_3:\n"
    "v_cmp_eq_u64 vcc, s[22:23], v[164:165]\n"
    "s_nop 0\n"
    "s_bitcmp1_b32 vcc_lo, 0\n"
    "s_cbranch_scc1 .LT_ok_4\n"
    "s_mov_b64 exec, 1\n"
    "buffer_load_dwordx2 v[164:165], off, s[68:71], s28 offset:0 sc1\n"
    "s_mov_b64 exec, -1\n"
    "s_waitcnt vmcnt(0)\n"
    "s_sub_u32 s24, s24, 1\n"
    "s_cmp_eq_u32 s24, 0\n"
    "s_cbranch_scc0 .LT_poll_3\n"
    ".LT_ok_4:\n"
    "s_waitcnt vmcnt(6)\n"
    "s_barrier\n"
    "s_add_u32 m0, s27, 16384\n"
    "s_add_u32 s19, s96, 460800\n"
    "buffer_load_dwordx4 v122, s[76:79], s19 offen offset:0 lds\n"
    "buffer_load_dwordx4 v123, s[76:79], s19 offen offset:1024 lds\n"
    "s_add_u32 m0, s27, 24576\n"
    "s_add_u32 s19, s96, 516096\n"
    "buffer_load_dwordx4 v122, s[76:79], s19 offen offset:0 lds\n"
    "buffer_load_dwordx4 v123, s[76:79], s19 offen offset:1024 lds\n"
    "s_mov_b64 exec, 1\n"
    "buffer_load_dwordx2 v[164:165], off, s[68:71], s28 offset:512 sc1\n"
    "s_mov_b64 exec, -1\n"
    "s_waitcnt vmcnt(7)\n"
    "s_barrier\n"
    "s_add_u32 m0, s27, 32768\n"
    "s_add_u32 s19, s96, 534528\n"
    "buffer_load_dwordx4 v122, s[76:79], s19 offen offset:0 lds\n"
    "buffer_load_dwordx4 v123, s[76:79], s19 offen offset:1024 lds\n"
    "s_waitcnt vmcnt(5)\n"
    "s_barrier\n"
    "s_add_u32 m0, s27, 57344\n"
    "s_add_u32 s19, s26, 18432\n"
    "buffer_load_dwordx4 v122, s[92:95], s19 offen offset:0 lds\n"
    "buffer_load_dwordx4 v123, s[92:95], s19 offen offset:1024 lds\n"
    "s_waitcnt vmcnt(4)\n"
    "s_mov_b32 s24, 1048576\n"
    ".LT_poll_5:\n"
    "v_cmp_eq_u64 vcc, s[22:23], v[164:165]\n"
    "s_nop 0\n"
    "s_bitcmp1_b32 vcc_lo, 0\n"
    "s_cbranch_scc1 .LT_ok_6\n"
    "s_mov_b64 exec, 1\n"
    "buffer_load_dwordx2 v[164:165], off, s[68:71], s28 offset:512 sc1\n"
    "s_mov_b64 exec, -1\n"
    "s_waitcnt vmcnt(0)\n"
    "s_sub_u32 s24, s24, 1\n"
    "s_cmp_eq_u32 s24, 0\n"
    "s_cbranch_scc0 .LT_poll_5\n"
    ".LT_ok_6:\n"
    "s_waitcnt vmcnt(2)\n"
    "s_barrier\n"
    "s_add_u32 m0, s27, 8192\n"
    "s_add_u32 s19, s26, 0\n"
    "buffer_load_dwordx4 v122, s[92:95], s19 offen offset:0 lds\n"
    "buffer_load_dwordx4 v123, s[92:95], s19 offen offset:1024 lds\n"
    "s_barrier\n"
    "s_setprio 2\n"
    "ds_read_b128 v[44:47], v116 offset:0\n"
    "ds_read_b128 v[48:51], v116 offset:5120\n"
    "ds_read_b128 v[52:55], v116 offset:1024\n"
    "ds_read_b128 v[56:59], v116 offset:6144\n"
    "ds_read_b128 v[60:63], v116 offset:2048\n"
    "ds_read_b128 v[64:67], v116 offset:7168\n"
    "ds_read_b128 v[68:71], v116 offset:3072\n"
    "ds_read_b128 v[72:75], v116 offset:8192\n"
    "s_waitcnt lgkmcnt(6)\n"
    "s_barrier\n"
    "v_mfma_f32_16x16x32_bf16 v[4:7], v[100:103], v[44:47], v[4:7]\n"
    "ds_read_b128 v[76:79], v116 offset:4096\n"
    "v_mfma_f32_16x16x32_bf16 v[4:7], v[104:107], v[48:51], v[4:7]\n"
    "ds_read_b128 v[80:83], v116 offset:9216\n"
    "v_mfma_f32_16x16x32_bf16 v[8:11], v[112:115], v[48:51], v[8:11]\n"
    "ds_read_b128 v[48:51], v116 offset:25600\n"
    "v_mfma_f32_16x16x32_bf16 v[8:11], v[108:111], v[44:47], v[8:11]\n"
    "ds_read_b128 v[44:47], v116 offset:20480\n"
    "s_waitcnt lgkmcnt(8)\n"
    "v_mfma_f32_16x16x32_bf16 v[16:19], v[108:111], v[52:55], v[16:19]\n"
    "v_mfma_f32_16x16x32_bf16 v[16:19], v[112:115], v[56:59], v[16:19]\n"
    "v_mfma_f32_16x16x32_bf16 v[12:15], v[104:107], v[56:59], v[12:15]\n"
    "ds_read_b128 v[56:59], v116 offset:26624\n"
    "v_mfma_f32_16x16x32_bf16 v[12:15], v[100:103], v[52:55], v[12:15]\n"
    "ds_read_b128 v[52:55], v116 offset:21504\n"
    "s_waitcnt lgkmcnt(8)\n"
    "v_mfma_f32_16x16x32_bf16 v[20:23], v[100:103], v[60:63], v[20:23]\n"
    "v_mfma_f32_16x16x32_bf16 v[20:23], v[104:107], v[64:67], v[20:23]\n"
    "v_mfma_f32_16x16x32_bf16 v[24:27], v[112:115], v[64:67], v[24:27]\n"
    "ds_read_b128 v[64:67], v116 offset:27648\n"
    "v_mfma_f32_16x16x32_bf16 v[24:27], v[108:111], v[60:63], v[24:27]\n"
    "ds_read_b128 v[60:63], v116 offset:22528\n"
    "s_waitcnt lgkmcnt(8)\n"
    "v_mfma_f32_16x16x32_bf16 v[32:35], v[108:111], v[68:71], v[32:35]\n"
    "v_mfma_f32_16x16x32_bf16 v[32:35], v[112:115], v[72:75], v[32:35]\n"
    "v_mfma_f32_16x16x32_bf16 v[28:31], v[104:107], v[72:75], v[28:31]\n"
    "ds_read_b128 v[72:75], v116 offset:28672\n"
    "v_mfma_f32_16x16x32_bf16 v[28:31], v[100:103], v[68:71], v[28:31]\n"
    "ds_read_b128 v[68:71], v116 offset:23552\n"
    "s_waitcnt lgkmcnt(8)\n"
    "v_mfma_f32_16x16x32_bf16 v[36:39], v[100:103], v[76:79], v[36:39]\n"
    "v_mfma_f32_16x16x32_bf16 v[36:39], v[104:107], v[80:83], v[36:39]\n"
    "v_mfma_f32_16x16x32_bf16 v[40:43], v[112:115], v[80:83], v[40:43]\n"
    "ds_read_b128 v[80:83], v116 offset:29696\n"
    "s_waitcnt lgkmcnt(7)\n"
    "s_waitcnt vmcnt(0)\n"
    "v_mfma_f32_16x16x32_bf16 v[40:43], v[108:111], v[76:79], v[40:43]\n"
    "v_mfma_f32_16x16x32_bf16 v[4:7], v[84:87], v[44:47], v[4:7]\n"
    "ds_read_b128 v[100:103], v118 offset:8192\n"
    "v_mfma_f32_16x16x32_bf16 v[4:7], v[88:91], v[48:51], v[4:7]\n"
    "ds_read_b128 v[104:107], v119 offset:8192\n"
    "v_mfma_f32_16x16x32_bf16 v[8:11], v[96:99], v[48:51], v[8:11]\n"
    "ds_read_b128 v[76:79], v116 offset:24576\n"
    "v_mfma_f32_16x16x32_bf16 v[8:11], v[92:95], v[44:47], v[8:11]\n"
    "ds_read_b128 v[112:115], v119 offset:57344\n"
    "s_waitcnt lgkmcnt(9)\n"
    "v_mfma_f32_16x16x32_bf16 v[16:19], v[92:95], v[52:55], v[16:19]\n"
    "ds_read_b128 v[108:111], v118 offset:57344\n"
    "v_mfma_f32_16x16x32_bf16 v[16:19], v[96:99], v[56:59], v[16:19]\n"
    "ds_read_b64_tr_b16 v[48:49], v124 offset:5120\n"
    "v_mfma_f32_16x16x32_bf16 v[12:15], v[88:91], v[56:59], v[12:15]\n"
    "ds_read_b64_tr_b16 v[50:51], v125 offset:5120\n"
    "v_mfma_f32_16x16x32_bf16 v[12:15], v[84:87], v[52:55], v[12:15]\n"
    "ds_read_b64_tr_b16 v[44:45], v124 offset:0\n"
    "s_waitcnt lgkmcnt(11)\n"
    "v_mfma_f32_16x16x32_bf16 v[20:23], v[84:87], v[60:63], v[20:23]\n"
    "ds_read_b64_tr_b16 v[46:47], v125 offset:0\n"
    "v_mfma_f32_16x16x32_bf16 v[20:23], v[88:91], v[64:67], v[20:23]\n"
    "ds_read_b64_tr_b16 v[56:57], v124 offset:6144\n"
    "v_mfma_f32_16x16x32_bf16 v[24:27], v[96:99], v[64:67], v[24:27]\n"
    "ds_read_b64_tr_b16 v[58:59], v125 offset:6144\n"
    "v_mfma_f32_16x16x32_bf16 v[24:27], v[92:95], v[60:63], v[24:27]\n"
    "ds_read_b64_tr_b16 v[52:53], v124 offset:1024\n"
    "s_waitcnt lgkmcnt(13)\n"
    "v_mfma_f32_16x16x32_bf16 v[32:35], v[92:95], v[68:71], v[32:35]\n"
    "ds_read_b64_tr_b16 v[54:55], v125 offset:1024\n"
    "v_mfma_f32_16x16x32_bf16 v[32:35], v[96:99], v[72:75], v[32:35]\n"
    "ds_read_b64_tr_b16 v[64:65], v124 offset:7168\n"
    "v_mfma_f32_16x16x32_bf16 v[28:31], v[88:91], v[72:75], v[28:31]\n"
    "ds_read_b64_tr_b16 v[66:67], v125 offset:7168\n"
    "v_mfma_f32_16x16x32_bf16 v[28:31], v[84:87], v[68:71], v[28:31]\n"
    "ds_read_b64_tr_b16 v[60:61], v124 offset:2048\n"
    "s_waitcnt lgkmcnt(13)\n"
    "v_mfma_f32_16x16x32_bf16 v[36:39], v[84:87], v[76:79], v[36:39]\n"
    "ds_read_b64_tr_b16 v[62:63], v125 offset:2048\n"
    "v_mfma_f32_16x16x32_bf16 v[36:39], v[88:91], v[80:83], v[36:39]\n"
    "ds_read_b64_tr_b16 v[72:73], v124 offset:8192\n"
    "v_mfma_f32_16x16x32_bf16 v[40:43], v[96:99], v[80:83], v[40:43]\n"
    "ds_read_b64_tr_b16 v[74:75], v125 offset:8192\n"
    "s_waitcnt lgkmcnt(10)\n"
    "v_mfma_f32_16x16x32_bf16 v[40:43], v[92:95], v[76:79], v[40:43]\n"
    "v_mfma_f32_16x16x32_bf16 v[4:7], v[100:103], v[44:47], v[4:7]\n"
    "ds_read_b64_tr_b16 v[68:69], v124 offset:3072\n"
    "v_mfma_f32_16x16x32_bf16 v[4:7], v[104:107], v[48:51], v[4:7]\n"
    "ds_read_b64_tr_b16 v[70:71], v125 offset:3072\n"
    "v_mfma_f32_16x16x32_bf16 v[8:11], v[112:115], v[48:51], v[8:11]\n"
    "ds_read_b64_tr_b16 v[80:81], v124 offset:9216\n"
    "v_mfma_f32_16x16x32_bf16 v[8:11], v[108:111], v[44:47], v[8:11]\n"
    "ds_read_b64_tr_b16 v[82:83], v125 offset:9216\n"
    "s_waitcnt lgkmcnt(10)\n"
    "v_mfma_f32_16x16x32_bf16 v[16:19], v[108:111], v[52:55], v[16:19]\n"
    "ds_read_b64_tr_b16 v[76:77], v124 offset:4096\n"
    "v_mfma_f32_16x16x32_bf16 v[16:19], v[112:115], v[56:59], v[16:19]\n"
    "ds_read_b64_tr_b16 v[78:79], v125 offset:4096\n"
    "v_mfma_f32_16x16x32_bf16 v[12:15], v[104:107], v[56:59], v[12:15]\n"
    "ds_read_b128 v[48:51], v116 offset:46080\n"
    "v_mfma_f32_16x16x32_bf16 v[12:15], v[100:103], v[52:55], v[12:15]\n"
    "ds_read_b128 v[44:47], v116 offset:40960\n"
    "s_waitcnt lgkmcnt(10)\n"
    "v_mfma_f32_16x16x32_bf16 v[20:23], v[100:103], v[60:63], v[20:23]\n"
    "ds_read_b128 v[56:59], v116 offset:47104\n"
    "v_mfma_f32_16x16x32_bf16 v[20:23], v[104:107], v[64:67], v[20:23]\n"
    "ds_read_b128 v[52:55], v116 offset:41984\n"
    "v_mfma_f32_16x16x32_bf16 v[24:27], v[112:115], v[64:67], v[24:27]\n"
    "ds_read_b128 v[64:67], v116 offset:48128\n"
    "v_mfma_f32_16x16x32_bf16 v[24:27], v[108:111], v[60:63], v[24:27]\n"
    "ds_read_b128 v[60:63], v116 offset:43008\n"
    "s_waitcnt lgkmcnt(10)\n"
    "v_mfma_f32_16x16x32_bf16 v[32:35], v[108:111], v[68:71], v[32:35]\n"
    "v_mfma_f32_16x16x32_bf16 v[32:35], v[112:115], v[72:75], v[32:35]\n"
    "v_mfma_f32_16x16x32_bf16 v[28:31], v[104:107], v[72:75], v[28:31]\n"
    "ds_read_b128 v[72:75], v116 offset:49152\n"
    "v_mfma_f32_16x16x32_bf16 v[28:31], v[100:103], v[68:71], v[28:31]\n"
    "ds_read_b128 v[68:71], v116 offset:44032\n"
    "s_waitcnt lgkmcnt(8)\n"
    "v_mfma_f32_16x16x32_bf16 v[36:39], v[100:103], v[76:79], v[36:39]\n"
    "v_mfma_f32_16x16x32_bf16 v[36:39], v[104:107], v[80:83], v[36:39]\n"
    "v_mfma_f32_16x16x32_bf16 v[40:43], v[112:115], v[80:83], v[40:43]\n"
    "ds_read_b128 v[80:83], v116 offset:50176\n"
    "s_waitcnt lgkmcnt(7)\n"
    "v_mfma_f32_16x16x32_bf16 v[40:43], v[108:111], v[76:79], v[40:43]\n"
    "v_mfma_f32_16x16x32_bf16 v[4:7], v[132:135], v[44:47], v[4:7]\n"
    "ds_read_b128 v[76:79], v116 offset:45056\n"
    "v_mfma_f32_16x16x32_bf16 v[4:7], v[136:139], v[48:51], v[4:7]\n"
    "v_mfma_f32_16x16x32_bf16 v[8:11], v[144:147], v[48:51], v[8:11]\n"
    "ds_read_b128 v[48:51], v117 offset:15360\n"
    "v_mfma_f32_16x16x32_bf16 v[8:11], v[140:143], v[44:47], v[8:11]\n"
    "ds_read_b128 v[44:47], v117 offset:10240\n"
    "s_waitcnt lgkmcnt(8)\n"
    "v_mfma_f32_16x16x32_bf16 v[16:19], v[140:143], v[52:55], v[16:19]\n"
    "v_mfma_f32_16x16x32_bf16 v[16:19], v[144:147], v[56:59], v[16:19]\n"
    "v_mfma_f32_16x16x32_bf16 v[12:15], v[136:139], v[56:59], v[12:15]\n"
    "ds_read_b128 v[56:59], v117 offset:16384\n"
    "v_mfma_f32_16x16x32_bf16 v[12:15], v[132:135], v[52:55], v[12:15]\n"
    "ds_read_b128 v[52:55], v117 offset:11264\n"
    "s_waitcnt lgkmcnt(8)\n"
    "v_mfma_f32_16x16x32_bf16 v[20:23], v[132:135], v[60:63], v[20:23]\n"
    "v_mfma_f32_16x16x32_bf16 v[20:23], v[136:139], v[64:67], v[20:23]\n"
    "v_mfma_f32_16x16x32_bf16 v[24:27], v[144:147], v[64:67], v[24:27]\n"
    "ds_read_b128 v[64:67], v117 offset:17408\n"
    "v_mfma_f32_16x16x32_bf16 v[24:27], v[140:143], v[60:63], v[24:27]\n"
    "ds_read_b128 v[60:63], v117 offset:12288\n"
    "s_waitcnt lgkmcnt(8)\n"
    "v_mfma_f32_16x16x32_bf16 v[32:35], v[140:143], v[68:71], v[32:35]\n"
    "v_mfma_f32_16x16x32_bf16 v[32:35], v[144:147], v[72:75], v[32:35]\n"
    "v_mfma_f32_16x16x32_bf16 v[28:31], v[136:139], v[72:75], v[28:31]\n"
    "ds_read_b128 v[72:75], v117 offset:18432\n"
    "v_mfma_f32_16x16x32_bf16 v[28:31], v[132:135], v[68:71], v[28:31]\n"
    "ds_read_b128 v[68:71], v117 offset:13312\n"
    "s_waitcnt lgkmcnt(8)\n"
    "v_mfma_f32_16x16x32_bf16 v[36:39], v[132:135], v[76:79], v[36:39]\n"
    "v_mfma_f32_16x16x32_bf16 v[36:39], v[136:139], v[80:83], v[36:39]\n"
    "v_mfma_f32_16x16x32_bf16 v[40:43], v[144:147], v[80:83], v[40:43]\n"
    "ds_read_b128 v[80:83], v117 offset:19456\n"
    "s_waitcnt lgkmcnt(7)\n"
    "v_mfma_f32_16x16x32_bf16 v[40:43], v[140:143], v[76:79], v[40:43]\n"
    "v_mfma_f32_16x16x32_bf16 v[4:7], v[148:151], v[44:47], v[4:7]\n"
    "ds_read_b128 v[76:79], v117 offset:14336\n"
    "v_mfma_f32_16x16x32_bf16 v[4:7], v[152:155], v[48:51], v[4:7]\n"
    "v_mfma_f32_16x16x32_bf16 v[8:11], v[160:163], v[48:51], v[8:11]\n"
    "v_mfma_f32_16x16x32_bf16 v[8:11], v[156:159], v[44:47], v[8:11]\n"
    "s_waitcnt lgkmcnt(6)\n"
    "v_mfma_f32_16x16x32_bf16 v[16:19], v[156:159], v[52:55], v[16:19]\n"
    "v_mfma_f32_16x16x32_bf16 v[16:19], v[160:163], v[56:59], v[16:19]\n"
    "v_mfma_f32_16x16x32_bf16 v[12:15], v[152:155], v[56:59], v[12:15]\n"
    "v_mfma_f32_16x16x32_bf16 v[12:15], v[148:151], v[52:55], v[12:15]\n"
    "s_nop 0\n"
    "ds_write_b128 v127, v[4:7] offset:0\n"
    "s_waitcnt lgkmcnt(5)\n"
    "v_mfma_f32_16x16x32_bf16 v[20:23], v[148:151], v[60:63], v[20:23]\n"
    "v_mfma_f32_16x16x32_bf16 v[20:23], v[152:155], v[64:67], v[20:23]\n"
    "ds_write_b128 v127, v[8:11] offset:1024\n"
    "v_mfma_f32_16x16x32_bf16 v[24:27], v[160:163], v[64:67], v[24:27]\n"
    "v_mfma_f32_16x16x32_bf16 v[24:27], v[156:159], v[60:63], v[24:27]\n"
    "ds_write_b128 v127, v[16:19] offset:3072\n"
    "s_waitcnt lgkmcnt(5)\n"
    "v_mfma_f32_16x16x32_bf16 v[32:35], v[156:159], v[68:71], v[32:35]\n"
    "v_mfma_f32_16x16x32_bf16 v[32:35], v[160:163], v[72:75], v[32:35]\n"
    "ds_write_b128 v127, v[12:15] offset:2048\n"
    "v_mfma_f32_16x16x32_bf16 v[28:31], v[152:155], v[72:75], v[28:31]\n"
    "v_mfma_f32_16x16x32_bf16 v[28:31], v[148:151], v[68:71], v[28:31]\n"
    "ds_write_b128 v127, v[20:23] offset:4096\n"
    "s_waitcnt lgkmcnt(5)\n"
    "v_mfma_f32_16x16x32_bf16 v[36:39], v[148:151], v[76:79], v[36:39]\n"
    "v_mfma_f32_16x16x32_bf16 v[36:39], v[152:155], v[80:83], v[36:39]\n"
    "ds_write_b128 v127, v[24:27] offset:5120\n"
    "v_mfma_f32_16x16x32_bf16 v[40:43], v[160:163], v[80:83], v[40:43]\n"
    "v_mfma_f32_16x16x32_bf16 v[40:43], v[156:159], v[76:79], v[40:43]\n"
    "ds_write_b128 v127, v[32:35] offset:7168\n"
    "ds_write_b128 v127, v[28:31] offset:6144\n"
    "s_nop 2\n"
    "ds_write_b128 v127, v[36:39] offset:8192\n"
    "s_nop 1\n"
    "ds_write_b128 v127, v[40:43] offset:9216\n"
    "s_setprio 0\n"
    "s_waitcnt lgkmcnt(0)\n"
    "s_barrier\n"
    "s_endpgm\n"
    ".LroleS:\n"
    "s_load_dword s17, s[64:65], 0x0\n"
    "s_mov_b32 s68, s64\n"
    "s_and_b32 s69, s65, 0xffff\n"
    "s_mov_b32 s70, 50331648\n"
    "s_mov_b32 s71, 0x20000\n"
    "s_mov_b32 s76, s42\n"
    "s_and_b32 s77, s43, 0xffff\n"
    "s_mov_b32 s78, 663552\n"
    "s_mov_b32 s79, 0x20000\n"
    "s_mov_b32 s80, s44\n"
    "s_and_b32 s81, s45, 0xffff\n"
    "s_mov_b32 s82, 663552\n"
    "s_mov_b32 s83, 0x20000\n"
    "v_bfe_u32 v4, v0, 1, 2\n"
    "v_lshrrev_b32 v5, 4, v0\n"
    "v_xor_b32 v4, v4, v5\n"
    "v_and_b32 v5, 1, v0\n"
    "v_lshlrev_b32 v5, 4, v5\n"
    "v_lshl_add_u32 v80, v4, 5, v5\n"
    "v_lshrrev_b32 v4, 3, v0\n"
    "v_mul_u32_u24 v4, 1152, v4\n"
    "v_add_u32 v80, v80, v4\n"
    "v_add_u32 v81, 8192, v80\n"
    "v_and_b32 v4, 15, v0\n"
    "v_lshrrev_b32 v5, 4, v0\n"
    "v_and_b32 v157, 6, v0\n"
    "v_xor_b32 v5, v5, v157\n"
    "v_lshlrev_b32 v5, 4, v5\n"
    "v_lshl_add_u32 v156, v4, 7, v5\n"
    "v_xor_b32 v157, 64, v156\n"
    "s_lshl_b32 s30, s14, 12\n"
    "s_add_u32 s30, s30, 96256\n"
    "v_lshrrev_b32 v4, 5, v0\n"
    "v_lshlrev_b32 v4, 10, v4\n"
    "v_bfe_u32 v5, v0, 2, 3\n"
    "v_lshl_add_u32 v4, v5, 7, v4\n"
    "v_and_b32 v160, 3, v0\n"
    "v_lshl_add_u32 v4, v160, 3, v4\n"
    "v_add_u32 v4, s30, v4\n"
    "v_lshrrev_b32 v5, 1, v5\n"
    "s_lshl_b32 s30, s15, 1\n"
    "v_xor_b32 v5, s30, v5\n"
    "v_lshl_add_u32 v160, v5, 5, v4\n"
    "v_xor_b32 v161, 32, v160\n"
    "s_mov_b32 s30, 161792\n"
    "v_and_b32 v158, 15, v0\n"
    "v_lshl_add_u32 v158, v158, 3, s30\n"
    "s_lshl_b32 s30, s12, 7\n"
    "s_add_u32 s30, s30, 93184\n"
    "v_and_b32 v159, 48, v0\n"
    "v_add_u32 v159, s30, v159\n"
    "s_mul_i32 s30, s12, 5120\n"
    "s_add_u32 s30, s30, 40960\n"
    "v_lshl_add_u32 v1, v0, 4, s30\n"
    "s_mov_b32 s30, 81920\n"
    "v_and_b32 v4, 15, v0\n"
    "v_lshl_add_u32 v162, v4, 3, s30\n"
    "v_lshrrev_b32 v4, 4, v0\n"
    "v_lshl_add_u32 v162, v4, 8, v162\n"
    "v_and_b32 v4, 1, v4\n"
    "v_mul_u32_u24 v4, 144, v4\n"
    "v_xor_b32 v162, v162, v4\n"
    "v_xor_b32 v163, 136, v162\n"
    "s_lshl_b32 s30, s11, 2\n"
    "s_add_u32 s30, s30, s12\n"
    "s_mul_i32 s93, s30, 18432\n"
    "s_lshl_b32 s30, s16, 7\n"
    "s_add_u32 s92, s93, s30\n"
    "s_add_u32 s93, s93, 1152\n"
    "s_mov_b32 s20, 0x3eeff558\n"
    "s_mov_b32 s21, 0x409d30fc\n"
    "s_waitcnt lgkmcnt(0)\n"
    "s_add_u32 s17, s17, 1\n"
    "s_xor_b32 s18, s17, 1540483477\n"
    "v_mov_b32 v164, s17\n"
    "v_mov_b32 v165, s18\n"
    "s_add_u32 m0, s22, 10240\n"
    "s_add_u32 s19, s23, 0\n"
    "buffer_load_dwordx4 v82, s[72:75], s19 offen lds\n"
    "s_add_u32 m0, s22, 12288\n"
    "s_add_u32 s19, s23, 4608\n"
    "buffer_load_dwordx4 v82, s[72:75], s19 offen lds\n"
    "s_add_u32 m0, s22, 14336\n"
    "s_add_u32 s19, s23, 9216\n"
    "buffer_load_dwordx4 v82, s[72:75], s19 offen lds\n"
    "s_add_u32 m0, s22, 16384\n"
    "s_add_u32 s19, s23, 13824\n"
    "buffer_load_dwordx4 v82, s[72:75], s19 offen lds\n"
    "s_add_u32 m0, s22, 18432\n"
    "s_add_u32 s19, s23, 18432\n"
    "s_mov_b32 exec_hi, s34\n"
    "buffer_load_dwordx4 v82, s[72:75], s19 offen lds\n"
    "s_mov_b32 exec_hi, -1\n"
    "s_add_u32 s23, s23, 256\n"
    "s_sub_u32 s19, s23, 1152\n"
    "s_cmp_ge_u32 s23, s24\n"
    "s_cselect_b32 s23, s19, s23\n"
    "s_setprio 3\n"
    "s_waitcnt vmcnt(13)\n"
    "s_waitcnt vmcnt(9)\n"
    "s_barrier\n"
    "s_add_u32 s25, s16, 1\n"
    "s_sub_u32 s19, s25, 9\n"
    "s_cmp_ge_u32 s25, 9\n"
    "s_cselect_b32 s25, s19, s25\n"
    "s_mul_i32 s25, s25, 589824\n"
    "s_add_u32 s25, s25, s26\n"
    "s_add_u32 m0, s27, 32768\n"
    "s_add_u32 s19, s25, 0\n"
    "buffer_load_dwordx4 v2, s[84:87], s19 offen offset:0 lds\n"
    "buffer_load_dwordx4 v3, s[84:87], s19 offen offset:1024 lds\n"
    "s_add_u32 m0, s27, 40960\n"
    "s_add_u32 s19, s25, 36864\n"
    "buffer_load_dwordx4 v2, s[84:87], s19 offen offset:0 lds\n"
    "buffer_load_dwordx4 v3, s[84:87], s19 offen offset:1024 lds\n"
    "s_waitcnt vmcnt(9)\n"
    "s_barrier\n"
    "s_add_u32 m0, s27, 49152\n"
    "s_add_u32 s19, s25, 256\n"
    "buffer_load_dwordx4 v2, s[84:87], s19 offen offset:0 lds\n"
    "buffer_load_dwordx4 v3, s[84:87], s19 offen offset:1024 lds\n"
    "s_add_u32 m0, s27, 57344\n"
    "s_add_u32 s19, s25, 37120\n"
    "buffer_load_dwordx4 v2, s[84:87], s19 offen offset:0 lds\n"
    "buffer_load_dwordx4 v3, s[84:87], s19 offen offset:1024 lds\n"
    "s_add_u32 s25, s16, 2\n"
    "s_sub_u32 s19, s25, 9\n"
    "s_cmp_ge_u32 s25, 9\n"
    "s_cselect_b32 s25, s19, s25\n"
    "s_mul_i32 s25, s25, 589824\n"
    "s_add_u32 s25, s25, s26\n"
    "s_add_u32 m0, s27, 0\n"
    "s_add_u32 s19, s25, 0\n"
    "buffer_load_dwordx4 v2, s[84:87], s19 offen offset:0 lds\n"
    "buffer_load_dwordx4 v3, s[84:87], s19 offen offset:1024 lds\n"
    "s_add_u32 m0, s22, 30720\n"
    "s_add_u32 s19, s23, 0\n"
    "buffer_load_dwordx4 v82, s[72:75], s19 offen lds\n"
    "s_waitcnt vmcnt(11)\n"
    "s_waitcnt vmcnt(7)\n"
    "s_barrier\n"
    "s_add_u32 m0, s27, 8192\n"
    "s_add_u32 s19, s25, 36864\n"
    "buffer_load_dwordx4 v2, s[84:87], s19 offen offset:0 lds\n"
    "buffer_load_dwordx4 v3, s[84:87], s19 offen offset:1024 lds\n"
    "s_add_u32 m0, s27, 16384\n"
    "s_add_u32 s19, s25, 256\n"
    "buffer_load_dwordx4 v2, s[84:87], s19 offen offset:0 lds\n"
    "buffer_load_dwordx4 v3, s[84:87], s19 offen offset:1024 lds\n"
    "s_add_u32 m0, s27, 24576\n"
    "s_add_u32 s19, s25, 37120\n"
    "buffer_load_dwordx4 v2, s[84:87], s19 offen offset:0 lds\n"
    "buffer_load_dwordx4 v3, s[84:87], s19 offen offset:1024 lds\n"
    "s_add_u32 m0, s22, 32768\n"
    "s_add_u32 s19, s23, 4608\n"
    "buffer_load_dwordx4 v82, s[72:75], s19 offen lds\n"
    "s_add_u32 m0, s22, 34816\n"
    "s_add_u32 s19, s23, 9216\n"
    "buffer_load_dwordx4 v82, s[72:75], s19 offen lds\n"
    "s_waitcnt vmcnt(11)\n"
    "s_barrier\n"
    "s_add_u32 s25, s16, 3\n"
    "s_sub_u32 s19, s25, 9\n"
    "s_cmp_ge_u32 s25, 9\n"
    "s_cselect_b32 s25, s19, s25\n"
    "s_mul_i32 s25, s25, 589824\n"
    "s_add_u32 s25, s25, s26\n"
    "s_add_u32 m0, s27, 32768\n"
    "s_add_u32 s19, s25, 0\n"
    "buffer_load_dwordx4 v2, s[84:87], s19 offen offset:0 lds\n"
    "buffer_load_dwordx4 v3, s[84:87], s19 offen offset:1024 lds\n"
    "s_add_u32 m0, s27, 40960\n"
    "s_add_u32 s19, s25, 36864\n"
    "buffer_load_dwordx4 v2, s[84:87], s19 offen offset:0 lds\n"
    "buffer_load_dwordx4 v3, s[84:87], s19 offen offset:1024 lds\n"
    "s_add_u32 m0, s22, 36864\n"
    "s_add_u32 s19, s23, 13824\n"
    "buffer_load_dwordx4 v82, s[72:75], s19 offen lds\n"
    "s_add_u32 m0, s22, 38912\n"
    "s_add_u32 s19, s23, 18432\n"
    "s_mov_b32 exec_hi, s34\n"
    "buffer_load_dwordx4 v82, s[72:75], s19 offen lds\n"
    "s_mov_b32 exec_hi, -1\n"
    "s_add_u32 s23, s23, 256\n"
    "s_sub_u32 s19, s23, 1152\n"
    "s_cmp_ge_u32 s23, s24\n"
    "s_cselect_b32 s23, s19, s23\n"
    "s_waitcnt vmcnt(12)\n"
    "s_barrier\n"
    "s_add_u32 m0, s27, 49152\n"
    "s_add_u32 s19, s25, 256\n"
    "buffer_load_dwordx4 v2, s[84:87], s19 offen offset:0 lds\n"
    "buffer_load_dwordx4 v3, s[84:87], s19 offen offset:1024 lds\n"
    "s_add_u32 m0, s27, 57344\n"
    "s_add_u32 s19, s25, 37120\n"
    "buffer_load_dwordx4 v2, s[84:87], s19 offen offset:0 lds\n"
    "buffer_load_dwordx4 v3, s[84:87], s19 offen offset:1024 lds\n"
    "s_add_u32 m0, s22, 51200\n"
    "s_add_u32 s19, s23, 0\n"
    "buffer_load_dwordx4 v82, s[72:75], s19 offen lds\n"
    "s_waitcnt vmcnt(13)\n"
    "s_barrier\n"
    "s_add_u32 s25, s16, 4\n"
    "s_sub_u32 s19, s25, 9\n"
    "s_cmp_ge_u32 s25, 9\n"
    "s_cselect_b32 s25, s19, s25\n"
    "s_mul_i32 s25, s25, 589824\n"
    "s_add_u32 s25, s25, s26\n"
    "s_add_u32 m0, s27, 0\n"
    "s_add_u32 s19, s25, 0\n"
    "buffer_load_dwordx4 v2, s[84:87], s19 offen offset:0 lds\n"
    "buffer_load_dwordx4 v3, s[84:87], s19 offen offset:1024 lds\n"
    "s_add_u32 m0, s27, 8192\n"
    "s_add_u32 s19, s25, 36864\n"
    "buffer_load_dwordx4 v2, s[84:87], s19 offen offset:0 lds\n"
    "buffer_load_dwordx4 v3, s[84:87], s19 offen offset:1024 lds\n"
    "s_add_u32 m0, s22, 53248\n"
    "s_add_u32 s19, s23, 4608\n"
    "buffer_load_dwordx4 v82, s[72:75], s19 offen lds\n"
    "s_add_u32 m0, s22, 55296\n"
    "s_add_u32 s19, s23, 9216\n"
    "buffer_load_dwordx4 v82, s[72:75], s19 offen lds\n"
    "s_waitcnt vmcnt(11)\n"
    "s_barrier\n"
    "s_add_u32 m0, s27, 16384\n"
    "s_add_u32 s19, s25, 256\n"
    "buffer_load_dwordx4 v2, s[84:87], s19 offen offset:0 lds\n"
    "buffer_load_dwordx4 v3, s[84:87], s19 offen offset:1024 lds\n"
    "s_add_u32 m0, s27, 24576\n"
    "s_add_u32 s19, s25, 37120\n"
    "buffer_load_dwordx4 v2, s[84:87], s19 offen offset:0 lds\n"
    "buffer_load_dwordx4 v3, s[84:87], s19 offen offset:1024 lds\n"
    "s_add_u32 m0, s22, 57344\n"
    "s_add_u32 s19, s23, 13824\n"
    "buffer_load_dwordx4 v82, s[72:75], s19 offen lds\n"
    "s_add_u32 m0, s22, 59392\n"
    "s_add_u32 s19, s23, 18432\n"
    "s_mov_b32 exec_hi, s34\n"
    "buffer_load_dwordx4 v82, s[72:75], s19 offen lds\n"
    "s_mov_b32 exec_hi, -1\n"
    "s_add_u32 s23, s23, 256\n"
    "s_sub_u32 s19, s23, 1152\n"
    "s_cmp_ge_u32 s23, s24\n"
    "s_cselect_b32 s23, s19, s23\n"
    "s_waitcnt vmcnt(13)\n"
    "s_barrier\n"
    "s_add_u32 s25, s16, 5\n"
    "s_sub_u32 s19, s25, 9\n"
    "s_cmp_ge_u32 s25, 9\n"
    "s_cselect_b32 s25, s19, s25\n"
    "s_mul_i32 s25, s25, 589824\n"
    "s_add_u32 s25, s25, s26\n"
    "s_add_u32 m0, s27, 32768\n"
    "s_add_u32 s19, s25, 0\n"
    "buffer_load_dwordx4 v2, s[84:87], s19 offen offset:0 lds\n"
    "buffer_load_dwordx4 v3, s[84:87], s19 offen offset:1024 lds\n"
    "s_add_u32 m0, s27, 40960\n"
    "s_add_u32 s19, s25, 36864\n"
    "buffer_load_dwordx4 v2, s[84:87], s19 offen offset:0 lds\n"
    "buffer_load_dwordx4 v3, s[84:87], s19 offen offset:1024 lds\n"
    "s_add_u32 m0, s22, 71680\n"
    "s_add_u32 s19, s23, 0\n"
    "buffer_load_dwordx4 v82, s[72:75], s19 offen lds\n"
    "s_waitcnt vmcnt(13)\n"
    "s_barrier\n"
    "s_add_u32 m0, s27, 49152\n"
    "s_add_u32 s19, s25, 256\n"
    "buffer_load_dwordx4 v2, s[84:87], s19 offen offset:0 lds\n"
    "buffer_load_dwordx4 v3, s[84:87], s19 offen offset:1024 lds\n"
    "s_add_u32 m0, s27, 57344\n"
    "s_add_u32 s19, s25, 37120\n"
    "buffer_load_dwordx4 v2, s[84:87], s19 offen offset:0 lds\n"
    "buffer_load_dwordx4 v3, s[84:87], s19 offen offset:1024 lds\n"
    "s_add_u32 m0, s22, 73728\n"
    "s_add_u32 s19, s23, 4608\n"
    "buffer_load_dwordx4 v82, s[72:75], s19 offen lds\n"
    "s_add_u32 m0, s22, 75776\n"
    "s_add_u32 s19, s23, 9216\n"
    "buffer_load_dwordx4 v82, s[72:75], s19 offen lds\n"
    "s_waitcnt vmcnt(13)\n"
    "s_barrier\n"
    "s_add_u32 s25, s16, 6\n"
    "s_sub_u32 s19, s25, 9\n"
    "s_cmp_ge_u32 s25, 9\n"
    "s_cselect_b32 s25, s19, s25\n"
    "s_mul_i32 s25, s25, 589824\n"
    "s_add_u32 s25, s25, s26\n"
    "s_add_u32 m0, s27, 0\n"
    "s_add_u32 s19, s25, 0\n"
    "buffer_load_dwordx4 v2, s[84:87], s19 offen offset:0 lds\n"
    "buffer_load_dwordx4 v3, s[84:87], s19 offen offset:1024 lds\n"
    "s_add_u32 m0, s27, 8192\n"
    "s_add_u32 s19, s25, 36864\n"
    "buffer_load_dwordx4 v2, s[84:87], s19 offen offset:0 lds\n"
    "buffer_load_dwordx4 v3, s[84:87], s19 offen offset:1024 lds\n"
    "s_add_u32 m0, s22, 77824\n"
    "s_add_u32 s19, s23, 13824\n"
    "buffer_load_dwordx4 v82, s[72:75], s19 offen lds\n"
    "s_add_u32 m0, s22, 79872\n"
    "s_add_u32 s19, s23, 18432\n"
    "s_mov_b32 exec_hi, s34\n"
    "buffer_load_dwordx4 v82, s[72:75], s19 offen lds\n"
    "s_mov_b32 exec_hi, -1\n"
    "s_waitcnt vmcnt(17)\n"
    "s_waitcnt vmcnt(13)\n"
    "s_barrier\n"
    "s_add_u32 m0, s27, 16384\n"
    "s_add_u32 s19, s25, 256\n"
    "buffer_load_dwordx4 v2, s[84:87], s19 offen offset:0 lds\n"
    "buffer_load_dwordx4 v3, s[84:87], s19 offen offset:1024 lds\n"
    "s_add_u32 m0, s27, 24576\n"
    "s_add_u32 s19, s25, 37120\n"
    "buffer_load_dwordx4 v2, s[84:87], s19 offen offset:0 lds\n"
    ".Lpage5:\n"
    "s_cbranch_execz .Ltouch_next\n"
    "buffer_load_dwordx4 v3, s[84:87], s19 offen offset:1024 lds\n"
    "s_waitcnt vmcnt(12)\n"
    "s_barrier\n"
    "s_add_u32 s25, s16, 7\n"
    "s_sub_u32 s19, s25, 9\n"
    "s_cmp_ge_u32 s25, 9\n"
    "s_cselect_b32 s25, s19, s25\n"
    "s_mul_i32 s25, s25, 589824\n"
    "s_add_u32 s25, s25, s26\n"
    "s_add_u32 m0, s27, 32768\n"
    "s_add_u32 s19, s25, 0\n"
    "buffer_load_dwordx4 v2, s[84:87], s19 offen offset:0 lds\n"
    "buffer_load_dwordx4 v3, s[84:87], s19 offen offset:1024 lds\n"
    "s_add_u32 m0, s27, 40960\n"
    "s_add_u32 s19, s25, 36864\n"
    "buffer_load_dwordx4 v2, s[84:87], s19 offen offset:0 lds\n"
    "buffer_load_dwordx4 v3, s[84:87], s19 offen offset:1024 lds\n"
    "s_waitcnt vmcnt(10)\n"
    "s_barrier\n"
    "s_add_u32 m0, s27, 49152\n"
    "s_add_u32 s19, s25, 256\n"
    "buffer_load_dwordx4 v2, s[84:87], s19 offen offset:0 lds\n"
    "buffer_load_dwordx4 v3, s[84:87], s19 offen offset:1024 lds\n"
    "s_add_u32 m0, s27, 57344\n"
    "s_add_u32 s19, s25, 37120\n"
    "buffer_load_dwordx4 v2, s[84:87], s19 offen offset:0 lds\n"
    "buffer_load_dwordx4 v3, s[84:87], s19 offen offset:1024 lds\n"
    "s_waitcnt vmcnt(12)\n"
    "s_waitcnt vmcnt(8)\n"
    "s_barrier\n"
    "s_add_u32 s25, s16, 8\n"
    "s_sub_u32 s19, s25, 9\n"
    "s_cmp_ge_u32 s25, 9\n"
    "s_cselect_b32 s25, s19, s25\n"
    "s_mul_i32 s25, s25, 589824\n"
    "s_add_u32 s25, s25, s26\n"
    "s_add_u32 m0, s27, 0\n"
    "s_add_u32 s19, s25, 0\n"
    "buffer_load_dwordx4 v2, s[84:87], s19 offen offset:0 lds\n"
    "buffer_load_dwordx4 v3, s[84:87], s19 offen offset:1024 lds\n"
    "s_add_u32 m0, s27, 8192\n"
    "s_add_u32 s19, s25, 36864\n"
    "buffer_load_dwordx4 v2, s[84:87], s19 offen offset:0 lds\n"
    "buffer_load_dwordx4 v3, s[84:87], s19 offen offset:1024 lds\n"
    "s_waitcnt vmcnt(8)\n"
    "s_barrier\n"
    "s_add_u32 m0, s27, 16384\n"
    "s_add_u32 s19, s25, 256\n"
    "buffer_load_dwordx4 v2, s[84:87], s19 offen offset:0 lds\n"
    "buffer_load_dwordx4 v3, s[84:87], s19 offen offset:1024 lds\n"
    "s_add_u32 m0, s27, 24576\n"
    "s_add_u32 s19, s25, 37120\n"
    "buffer_load_dwordx4 v2, s[84:87], s19 offen offset:0 lds\n"
    "buffer_load_dwordx4 v3, s[84:87], s19 offen offset:1024 lds\n"
    "s_waitcnt vmcnt(8)\n"
    "s_barrier\n"
    "s_add_u32 m0, s27, 32768\n"
    "s_nop 0\n"
    "buffer_load_dwordx4 v80, s[76:79], s92 offen offset:0 lds\n"
    "buffer_load_dwordx4 v81, s[76:79], s92 offen offset:1024 lds\n"
    "s_add_u32 m0, s27, 40960\n"
    "s_nop 0\n"
    "buffer_load_dwordx4 v80, s[80:83], s92 offen offset:0 lds\n"
    "buffer_load_dwordx4 v81, s[80:83], s92 offen offset:1024 lds\n"
    "s_add_u32 s92, s92, 128\n"
    "s_sub_u32 s19, s92, 1152\n"
    "s_cmp_ge_u32 s92, s93\n"
    "s_cselect_b32 s92, s19, s92\n"
    "s_waitcnt vmcnt(8)\n"
    "s_barrier\n"
    "s_add_u32 m0, s27, 49152\n"
    "s_nop 0\n"
    "buffer_load_dwordx4 v80, s[76:79], s92 offen offset:0 lds\n"
    "buffer_load_dwordx4 v81, s[76:79], s92 offen offset:1024 lds\n"
    "s_add_u32 m0, s27, 57344\n"
    "s_nop 0\n"
    "buffer_load_dwordx4 v80, s[80:83], s92 offen offset:0 lds\n"
    "buffer_load_dwordx4 v81, s[80:83], s92 offen offset:1024 lds\n"
    "s_add_u32 s92, s92, 128\n"
    "s_sub_u32 s19, s92, 1152\n"
    "s_cmp_ge_u32 s92, s93\n"
    "s_cselect_b32 s92, s19, s92\n"
    "s_waitcnt vmcnt(8)\n"
    "s_barrier\n"
    "s_add_u32 m0, s27, 0\n"
    "s_nop 0\n"
    "buffer_load_dwordx4 v80, s[76:79], s92 offen offset:0 lds\n"
    "buffer_load_dwordx4 v81, s[76:79], s92 offen offset:1024 lds\n"
    "s_add_u32 m0, s27, 8192\n"
    "s_nop 0\n"
    "buffer_load_dwordx4 v80, s[80:83], s92 offen offset:0 lds\n"
    "buffer_load_dwordx4 v81, s[80:83], s92 offen offset:1024 lds\n"
    "s_add_u32 s92, s92, 128\n"
    "s_sub_u32 s19, s92, 1152\n"
    "s_cmp_ge_u32 s92, s93\n"
    "s_cselect_b32 s92, s19, s92\n"
    "s_waitcnt vmcnt(8)\n"
    "s_barrier\n"
    "s_add_u32 m0, s27, 16384\n"
    "s_nop 0\n"
    "buffer_load_dwordx4 v80, s[76:79], s92 offen offset:0 lds\n"
    "buffer_load_dwordx4 v81, s[76:79], s92 offen offset:1024 lds\n"
    "s_add_u32 m0, s27, 24576\n"
    "s_nop 0\n"
    "buffer_load_dwordx4 v80, s[80:83], s92 offen offset:0 lds\n"
    "buffer_load_dwordx4 v81, s[80:83], s92 offen offset:1024 lds\n"
    "s_add_u32 s92, s92, 128\n"
    "s_sub_u32 s19, s92, 1152\n"
    "s_cmp_ge_u32 s92, s93\n"
    "s_cselect_b32 s92, s19, s92\n"
    "s_waitcnt vmcnt(8)\n"
    "s_barrier\n"
    "s_add_u32 m0, s27, 32768\n"
    "s_nop 0\n"
    "buffer_load_dwordx4 v80, s[76:79], s92 offen offset:0 lds\n"
    "buffer_load_dwordx4 v81, s[76:79], s92 offen offset:1024 lds\n"
    "s_add_u32 m0, s27, 40960\n"
    "s_nop 0\n"
    "buffer_load_dwordx4 v80, s[80:83], s92 offen offset:0 lds\n"
    "buffer_load_dwordx4 v81, s[80:83], s92 offen offset:1024 lds\n"
    "s_add_u32 s92, s92, 128\n"
    "s_sub_u32 s19, s92, 1152\n"
    "s_cmp_ge_u32 s92, s93\n"
    "s_cselect_b32 s92, s19, s92\n"
    "s_waitcnt vmcnt(8)\n"
    "s_barrier\n"
    "s_add_u32 m0, s27, 49152\n"
    "s_nop 0\n"
    "buffer_load_dwordx4 v80, s[76:79], s92 offen offset:0 lds\n"
    "buffer_load_dwordx4 v81, s[76:79], s92 offen offset:1024 lds\n"
    "s_add_u32 m0, s27, 57344\n"
    "s_nop 0\n"
    "buffer_load_dwordx4 v80, s[80:83], s92 offen offset:0 lds\n"
    "buffer_load_dwordx4 v81, s[80:83], s92 offen offset:1024 lds\n"
    "s_add_u32 s92, s92, 128\n"
    "s_sub_u32 s19, s92, 1152\n"
    "s_cmp_ge_u32 s92, s93\n"
    "s_cselect_b32 s92, s19, s92\n"
    "s_waitcnt vmcnt(8)\n"
    "s_barrier\n"
    "s_add_u32 m0, s27, 0\n"
    "s_nop 0\n"
    "buffer_load_dwordx4 v80, s[76:79], s92 offen offset:0 lds\n"
    "buffer_load_dwordx4 v81, s[76:79], s92 offen offset:1024 lds\n"
    "s_add_u32 m0, s27, 8192\n"
    "s_nop 0\n"
    "buffer_load_dwordx4 v80, s[80:83], s92 offen offset:0 lds\n"
    "buffer_load_dwordx4 v81, s[80:83], s92 offen offset:1024 lds\n"
    "s_add_u32 s92, s92, 128\n"
    "s_sub_u32 s19, s92, 1152\n"
    "s_cmp_ge_u32 s92, s93\n"
    "s_cselect_b32 s92, s19, s92\n"
    "s_waitcnt vmcnt(8)\n"
    "s_barrier\n"
    "s_add_u32 m0, s27, 16384\n"
    "s_nop 0\n"
    "buffer_load_dwordx4 v80, s[76:79], s92 offen offset:0 lds\n"
    "buffer_load_dwordx4 v81, s[76:79], s92 offen offset:1024 lds\n"
    "s_add_u32 m0, s27, 24576\n"
    "s_nop 0\n"
    "buffer_load_dwordx4 v80, s[80:83], s92 offen offset:0 lds\n"
    "buffer_load_dwordx4 v81, s[80:83], s92 offen offset:1024 lds\n"
    "s_add_u32 s92, s92, 128\n"
    "s_sub_u32 s19, s92, 1152\n"
    "s_cmp_ge_u32 s92, s93\n"
    "s_cselect_b32 s92, s19, s92\n"
    "s_waitcnt vmcnt(8)\n"
    "s_barrier\n"
    "s_add_u32 m0, s27, 32768\n"
    "s_nop 0\n"
    "buffer_load_dwordx4 v80, s[76:79], s92 offen offset:0 lds\n"
    "buffer_load_dwordx4 v81, s[76:79], s92 offen offset:1024 lds\n"
    "s_add_u32 m0, s27, 40960\n"
    "s_nop 0\n"
    "buffer_load_dwordx4 v80, s[80:83], s92 offen offset:0 lds\n"
    "buffer_load_dwordx4 v81, s[80:83], s92 offen offset:1024 lds\n"
    "s_add_u32 s92, s92, 128\n"
    "s_sub_u32 s19, s92, 1152\n"
    "s_cmp_ge_u32 s92, s93\n"
    "s_cselect_b32 s92, s19, s92\n"
    "s_waitcnt vmcnt(8)\n"
    "s_barrier\n"
    "s_add_u32 s25, s16, 0\n"
    "s_sub_u32 s19, s25, 9\n"
    "s_cmp_ge_u32 s25, 9\n"
    "s_cselect_b32 s25, s19, s25\n"
    "s_mul_i32 s25, s25, 589824\n"
    "s_add_u32 s25, s25, s26\n"
    "s_add_u32 m0, s27, 49152\n"
    "s_add_u32 s19, s25, 512\n"
    "buffer_load_dwordx4 v2, s[84:87], s19 offen offset:0 lds\n"
    "buffer_load_dwordx4 v3, s[84:87], s19 offen offset:1024 lds\n"
    "s_add_u32 m0, s27, 57344\n"
    "s_add_u32 s19, s25, 37376\n"
    "buffer_load_dwordx4 v2, s[84:87], s19 offen offset:0 lds\n"
    "buffer_load_dwordx4 v3, s[84:87], s19 offen offset:1024 lds\n"
    "s_waitcnt vmcnt(8)\n"
    "s_barrier\n"
    "s_add_u32 m0, s27, 0\n"
    "s_add_u32 s19, s25, 768\n"
    "buffer_load_dwordx4 v2, s[84:87], s19 offen offset:0 lds\n"
    "buffer_load_dwordx4 v3, s[84:87], s19 offen offset:1024 lds\n"
    "s_add_u32 m0, s27, 8192\n"
    "s_add_u32 s19, s25, 37632\n"
    "buffer_load_dwordx4 v2, s[84:87], s19 offen offset:0 lds\n"
    "buffer_load_dwordx4 v3, s[84:87], s19 offen offset:1024 lds\n"
    "s_waitcnt vmcnt(8)\n"
    "s_barrier\n"
    "s_add_u32 s25, s16, 1\n"
    "s_sub_u32 s19, s25, 9\n"
    "s_cmp_ge_u32 s25, 9\n"
    "s_cselect_b32 s25, s19, s25\n"
    "s_mul_i32 s25, s25, 589824\n"
    "s_add_u32 s25, s25, s26\n"
    "s_add_u32 m0, s27, 16384\n"
    "s_add_u32 s19, s25, 512\n"
    "buffer_load_dwordx4 v2, s[84:87], s19 offen offset:0 lds\n"
    "buffer_load_dwordx4 v3, s[84:87], s19 offen offset:1024 lds\n"
    "s_add_u32 m0, s27, 24576\n"
    "s_add_u32 s19, s25, 37376\n"
    "buffer_load_dwordx4 v2, s[84:87], s19 offen offset:0 lds\n"
    "buffer_load_dwordx4 v3, s[84:87], s19 offen offset:1024 lds\n"
    "s_waitcnt vmcnt(8)\n"
    "s_barrier\n"
    "s_setprio 2\n"
    "s_add_u32 m0, s27, 32768\n"
    "s_add_u32 s19, s25, 768\n"
    "buffer_load_dwordx4 v2, s[84:87], s19 offen offset:0 lds\n"
    "buffer_load_dwordx4 v3, s[84:87], s19 offen offset:1024 lds\n"
    "s_add_u32 m0, s27, 40960\n"
    "s_add_u32 s19, s25, 37632\n"
    "buffer_load_dwordx4 v2, s[84:87], s19 offen offset:0 lds\n"
    "buffer_load_dwordx4 v3, s[84:87], s19 offen offset:1024 lds\n"
    "ds_read_b64_tr_b16 v[140:141], v160 offset:49152\n"
    "ds_read_b64_tr_b16 v[142:143], v160 offset:57344\n"
    "ds_read_b64_tr_b16 v[144:145], v160 offset:51200\n"
    "ds_read_b64_tr_b16 v[146:147], v160 offset:59392\n"
    "ds_read_b64_tr_b16 v[152:153], v161 offset:51200\n"
    "ds_read_b64_tr_b16 v[154:155], v161 offset:59392\n"
    "ds_read_b64_tr_b16 v[148:149], v161 offset:49152\n"
    "ds_read_b64_tr_b16 v[150:151], v161 offset:57344\n"
    "ds_read_b128 v[84:87], v156 offset:0\n"
    "ds_read_b128 v[88:91], v157 offset:0\n"
    "ds_read_b128 v[92:95], v156 offset:2048\n"
    "ds_read_b128 v[96:99], v157 offset:2048\n"
    "ds_read_b128 v[100:103], v156 offset:4096\n"
    "ds_read_b128 v[104:107], v157 offset:4096\n"
    "s_waitcnt lgkmcnt(4)\n"
    "s_waitcnt vmcnt(8)\n"
    "s_barrier\n"
    "v_mfma_f32_16x16x32_bf16 v[4:7], v[140:143], v[84:87], 0\n"
    "ds_read_b128 v[108:111], v156 offset:6144\n"
    "v_mfma_f32_16x16x32_bf16 v[4:7], v[144:147], v[88:91], v[4:7]\n"
    "ds_read_b128 v[112:115], v157 offset:6144\n"
    "v_mfma_f32_16x16x32_bf16 v[8:11], v[152:155], v[88:91], 0\n"
    "ds_read_b64_tr_b16 v[124:125], v160 offset:0\n"
    "v_mfma_f32_16x16x32_bf16 v[8:11], v[148:151], v[84:87], v[8:11]\n"
    "ds_read_b64_tr_b16 v[126:127], v160 offset:8192\n"
    "s_waitcnt lgkmcnt(6)\n"
    "v_mfma_f32_16x16x32_bf16 v[24:27], v[148:151], v[92:95], 0\n"
    "ds_read_b128 v[116:119], v156 offset:8192\n"
    "v_mfma_f32_16x16x32_bf16 v[24:27], v[152:155], v[96:99], v[24:27]\n"
    "ds_read_b128 v[120:123], v157 offset:8192\n"
    "v_mfma_f32_16x16x32_bf16 v[20:23], v[144:147], v[96:99], 0\n"
    "ds_read_b64_tr_b16 v[128:129], v160 offset:2048\n"
    "v_mfma_f32_16x16x32_bf16 v[20:23], v[140:143], v[92:95], v[20:23]\n"
    "ds_read_b64_tr_b16 v[130:131], v160 offset:10240\n"
    "s_waitcnt lgkmcnt(8)\n"
    "v_mfma_f32_16x16x32_bf16 v[36:39], v[140:143], v[100:103], 0\n"
    "ds_read_b64_tr_b16 v[136:137], v161 offset:2048\n"
    "v_mfma_f32_16x16x32_bf16 v[36:39], v[144:147], v[104:107], v[36:39]\n"
    "ds_read_b64_tr_b16 v[138:139], v161 offset:10240\n"
    "v_mfma_f32_16x16x32_bf16 v[40:43], v[152:155], v[104:107], 0\n"
    "ds_read_b64_tr_b16 v[132:133], v161 offset:0\n"
    "v_mfma_f32_16x16x32_bf16 v[40:43], v[148:151], v[100:103], v[40:43]\n"
    "ds_read_b64_tr_b16 v[134:135], v161 offset:8192\n"
    "s_waitcnt lgkmcnt(10)\n"
    "v_mfma_f32_16x16x32_bf16 v[56:59], v[148:151], v[108:111], 0\n"
    "v_mfma_f32_16x16x32_bf16 v[56:59], v[152:155], v[112:115], v[56:59]\n"
    "v_mfma_f32_16x16x32_bf16 v[52:55], v[144:147], v[112:115], 0\n"
    "v_mfma_f32_16x16x32_bf16 v[52:55], v[140:143], v[108:111], v[52:55]\n"
    "s_waitcnt lgkmcnt(6)\n"
    "v_mfma_f32_16x16x32_bf16 v[68:71], v[140:143], v[116:119], 0\n"
    "v_mfma_f32_16x16x32_bf16 v[68:71], v[144:147], v[120:123], v[68:71]\n"
    "v_mfma_f32_16x16x32_bf16 v[72:75], v[152:155], v[120:123], 0\n"
    "s_waitcnt lgkmcnt(0)\n"
    "s_waitcnt vmcnt(4)\n"
    "v_mfma_f32_16x16x32_bf16 v[72:75], v[148:151], v[116:119], v[72:75]\n"
    "s_barrier\n"
    "v_mfma_f32_16x16x32_bf16 v[12:15], v[124:127], v[84:87], 0\n"
    "ds_read_b64_tr_b16 v[140:141], v160 offset:16384\n"
    "v_mfma_f32_16x16x32_bf16 v[12:15], v[128:131], v[88:91], v[12:15]\n"
    "ds_read_b64_tr_b16 v[142:143], v160 offset:24576\n"
    "v_mfma_f32_16x16x32_bf16 v[16:19], v[136:139], v[88:91], 0\n"
    "ds_read_b64_tr_b16 v[144:145], v160 offset:18432\n"
    "v_mfma_f32_16x16x32_bf16 v[16:19], v[132:135], v[84:87], v[16:19]\n"
    "ds_read_b64_tr_b16 v[146:147], v160 offset:26624\n"
    "v_mfma_f32_16x16x32_bf16 v[32:35], v[132:135], v[92:95], 0\n"
    "ds_read_b64_tr_b16 v[152:153], v161 offset:18432\n"
    "v_mfma_f32_16x16x32_bf16 v[32:35], v[136:139], v[96:99], v[32:35]\n"
    "ds_read_b64_tr_b16 v[154:155], v161 offset:26624\n"
    "v_mfma_f32_16x16x32_bf16 v[28:31], v[128:131], v[96:99], 0\n"
    "ds_read_b64_tr_b16 v[148:149], v161 offset:16384\n"
    "v_mfma_f32_16x16x32_bf16 v[28:31], v[124:127], v[92:95], v[28:31]\n"
    "ds_read_b128 v[88:91], v157 offset:10240\n"
    "v_mfma_f32_16x16x32_bf16 v[44:47], v[124:127], v[100:103], 0\n"
    "ds_read_b128 v[84:87], v156 offset:10240\n"
    "v_mfma_f32_16x16x32_bf16 v[44:47], v[128:131], v[104:107], v[44:47]\n"
    "ds_read_b64_tr_b16 v[150:151], v161 offset:24576\n"
    "v_mfma_f32_16x16x32_bf16 v[48:51], v[136:139], v[104:107], 0\n"
    "ds_read_b128 v[96:99], v157 offset:12288\n"
    "v_mfma_f32_16x16x32_bf16 v[48:51], v[132:135], v[100:103], v[48:51]\n"
    "ds_read_b128 v[92:95], v156 offset:12288\n"
    "v_mfma_f32_16x16x32_bf16 v[64:67], v[132:135], v[108:111], 0\n"
    "ds_read_b128 v[104:107], v157 offset:14336\n"
    "v_mfma_f32_16x16x32_bf16 v[64:67], v[136:139], v[112:115], v[64:67]\n"
    "ds_read_b128 v[100:103], v156 offset:14336\n"
    "v_mfma_f32_16x16x32_bf16 v[60:63], v[128:131], v[112:115], 0\n"
    "v_mfma_f32_16x16x32_bf16 v[60:63], v[124:127], v[108:111], v[60:63]\n"
    "v_mfma_f32_16x16x32_bf16 v[76:79], v[124:127], v[116:119], 0\n"
    "v_mfma_f32_16x16x32_bf16 v[76:79], v[128:131], v[120:123], v[76:79]\n"
    "v_mfma_f32_16x16x32_bf16 v[80:83], v[136:139], v[120:123], 0\n"
    "s_waitcnt lgkmcnt(4)\n"
    "s_waitcnt vmcnt(0)\n"
    "v_mfma_f32_16x16x32_bf16 v[80:83], v[132:135], v[116:119], v[80:83]\n"
    "s_barrier\n"
    "v_mfma_f32_16x16x32_bf16 v[4:7], v[140:143], v[84:87], v[4:7]\n"
    "ds_read_b128 v[112:115], v157 offset:16384\n"
    "v_mfma_f32_16x16x32_bf16 v[4:7], v[144:147], v[88:91], v[4:7]\n"
    "ds_read_b128 v[108:111], v156 offset:16384\n"
    "v_mfma_f32_16x16x32_bf16 v[8:11], v[152:155], v[88:91], v[8:11]\n"
    "ds_read_b64_tr_b16 v[124:125], v160 offset:32768\n"
    "v_mfma_f32_16x16x32_bf16 v[8:11], v[148:151], v[84:87], v[8:11]\n"
    "ds_read_b64_tr_b16 v[126:127], v160 offset:40960\n"
    "s_waitcnt lgkmcnt(6)\n"
    "v_mfma_f32_16x16x32_bf16 v[24:27], v[148:151], v[92:95], v[24:27]\n"
    "ds_read_b128 v[120:123], v157 offset:18432\n"
    "v_mfma_f32_16x16x32_bf16 v[24:27], v[152:155], v[96:99], v[24:27]\n"
    "ds_read_b128 v[116:119], v156 offset:18432\n"
    "v_mfma_f32_16x16x32_bf16 v[20:23], v[144:147], v[96:99], v[20:23]\n"
    "ds_read_b64_tr_b16 v[128:129], v160 offset:34816\n"
    "v_mfma_f32_16x16x32_bf16 v[20:23], v[140:143], v[92:95], v[20:23]\n"
    "ds_read_b64_tr_b16 v[130:131], v160 offset:43008\n"
    "s_waitcnt lgkmcnt(8)\n"
    "v_mfma_f32_16x16x32_bf16 v[36:39], v[140:143], v[100:103], v[36:39]\n"
    "ds_read_b64_tr_b16 v[136:137], v161 offset:34816\n"
    "v_mfma_f32_16x16x32_bf16 v[36:39], v[144:147], v[104:107], v[36:39]\n"
    "ds_read_b64_tr_b16 v[138:139], v161 offset:43008\n"
    "v_mfma_f32_16x16x32_bf16 v[40:43], v[152:155], v[104:107], v[40:43]\n"
    "ds_read_b64_tr_b16 v[132:133], v161 offset:32768\n"
    "v_mfma_f32_16x16x32_bf16 v[40:43], v[148:151], v[100:103], v[40:43]\n"
    "ds_read_b64_tr_b16 v[134:135], v161 offset:40960\n"
    "s_waitcnt lgkmcnt(10)\n"
    "v_mfma_f32_16x16x32_bf16 v[56:59], v[148:151], v[108:111], v[56:59]\n"
    "v_mfma_f32_16x16x32_bf16 v[56:59], v[152:155], v[112:115], v[56:59]\n"
    "v_mfma_f32_16x16x32_bf16 v[52:55], v[144:147], v[112:115], v[52:55]\n"
    "v_mfma_f32_16x16x32_bf16 v[52:55], v[140:143], v[108:111], v[52:55]\n"
    "s_waitcnt lgkmcnt(6)\n"
    "v_mfma_f32_16x16x32_bf16 v[68:71], v[140:143], v[116:119], v[68:71]\n"
    "v_mfma_f32_16x16x32_bf16 v[68:71], v[144:147], v[120:123], v[68:71]\n"
    "v_mfma_f32_16x16x32_bf16 v[72:75], v[152:155], v[120:123], v[72:75]\n"
    "s_waitcnt lgkmcnt(0)\n"
    "v_mfma_f32_16x16x32_bf16 v[72:75], v[148:151], v[116:119], v[72:75]\n"
    "s_barrier\n"
    "v_mfma_f32_16x16x32_bf16 v[12:15], v[124:127], v[84:87], v[12:15]\n"
    "ds_read_b64_tr_b16 v[140:141], v160 offset:49152\n"
    "v_mfma_f32_16x16x32_bf16 v[12:15], v[128:131], v[88:91], v[12:15]\n"
    "ds_read_b64_tr_b16 v[142:143], v160 offset:57344\n"
    "v_mfma_f32_16x16x32_bf16 v[16:19], v[136:139], v[88:91], v[16:19]\n"
    "ds_read_b64_tr_b16 v[144:145], v160 offset:51200\n"
    "v_mfma_f32_16x16x32_bf16 v[16:19], v[132:135], v[84:87], v[16:19]\n"
    "ds_read_b64_tr_b16 v[146:147], v160 offset:59392\n"
    "v_mfma_f32_16x16x32_bf16 v[32:35], v[132:135], v[92:95], v[32:35]\n"
    "ds_read_b64_tr_b16 v[152:153], v161 offset:51200\n"
    "v_mfma_f32_16x16x32_bf16 v[32:35], v[136:139], v[96:99], v[32:35]\n"
    "ds_read_b64_tr_b16 v[154:155], v161 offset:59392\n"
    "v_mfma_f32_16x16x32_bf16 v[28:31], v[128:131], v[96:99], v[28:31]\n"
    "ds_read_b64_tr_b16 v[148:149], v161 offset:49152\n"
    "v_mfma_f32_16x16x32_bf16 v[28:31], v[124:127], v[92:95], v[28:31]\n"
    "ds_read_b128 v[88:91], v157 offset:20480\n"
    "v_mfma_f32_16x16x32_bf16 v[44:47], v[124:127], v[100:103], v[44:47]\n"
    "ds_read_b128 v[84:87], v156 offset:20480\n"
    "v_mfma_f32_16x16x32_bf16 v[44:47], v[128:131], v[104:107], v[44:47]\n"
    "ds_read_b64_tr_b16 v[150:151], v161 offset:57344\n"
    "v_mfma_f32_16x16x32_bf16 v[48:51], v[136:139], v[104:107], v[48:51]\n"
    "ds_read_b128 v[96:99], v157 offset:22528\n"
    "v_mfma_f32_16x16x32_bf16 v[48:51], v[132:135], v[100:103], v[48:51]\n"
    "ds_read_b128 v[92:95], v156 offset:22528\n"
    "v_mfma_f32_16x16x32_bf16 v[64:67], v[132:135], v[108:111], v[64:67]\n"
    "ds_read_b128 v[104:107], v157 offset:24576\n"
    "v_mfma_f32_16x16x32_bf16 v[64:67], v[136:139], v[112:115], v[64:67]\n"
    "ds_read_b128 v[100:103], v156 offset:24576\n"
    "v_mfma_f32_16x16x32_bf16 v[60:63], v[128:131], v[112:115], v[60:63]\n"
    "v_mfma_f32_16x16x32_bf16 v[60:63], v[124:127], v[108:111], v[60:63]\n"
    "v_mfma_f32_16x16x32_bf16 v[76:79], v[124:127], v[116:119], v[76:79]\n"
    "v_mfma_f32_16x16x32_bf16 v[76:79], v[128:131], v[120:123], v[76:79]\n"
    "v_mfma_f32_16x16x32_bf16 v[80:83], v[136:139], v[120:123], v[80:83]\n"
    "s_waitcnt lgkmcnt(4)\n"
    "v_mfma_f32_16x16x32_bf16 v[80:83], v[132:135], v[116:119], v[80:83]\n"
    "s_barrier\n"
    "v_mfma_f32_16x16x32_bf16 v[4:7], v[140:143], v[84:87], v[4:7]\n"
    "ds_read_b128 v[112:115], v157 offset:26624\n"
    "v_mfma_f32_16x16x32_bf16 v[4:7], v[144:147], v[88:91], v[4:7]\n"
    "ds_read_b128 v[108:111], v156 offset:26624\n"
    "v_mfma_f32_16x16x32_bf16 v[8:11], v[152:155], v[88:91], v[8:11]\n"
    "ds_read_b64_tr_b16 v[124:125], v160 offset:0\n"
    "v_mfma_f32_16x16x32_bf16 v[8:11], v[148:151], v[84:87], v[8:11]\n"
    "ds_read_b64_tr_b16 v[126:127], v160 offset:8192\n"
    "s_waitcnt lgkmcnt(6)\n"
    "v_mfma_f32_16x16x32_bf16 v[24:27], v[148:151], v[92:95], v[24:27]\n"
    "ds_read_b128 v[120:123], v157 offset:28672\n"
    "v_mfma_f32_16x16x32_bf16 v[24:27], v[152:155], v[96:99], v[24:27]\n"
    "ds_read_b128 v[116:119], v156 offset:28672\n"
    "v_mfma_f32_16x16x32_bf16 v[20:23], v[144:147], v[96:99], v[20:23]\n"
    "ds_read_b64_tr_b16 v[128:129], v160 offset:2048\n"
    "v_mfma_f32_16x16x32_bf16 v[20:23], v[140:143], v[92:95], v[20:23]\n"
    "ds_read_b64_tr_b16 v[130:131], v160 offset:10240\n"
    "s_waitcnt lgkmcnt(8)\n"
    "v_mfma_f32_16x16x32_bf16 v[36:39], v[140:143], v[100:103], v[36:39]\n"
    "ds_read_b64_tr_b16 v[136:137], v161 offset:2048\n"
    "v_mfma_f32_16x16x32_bf16 v[36:39], v[144:147], v[104:107], v[36:39]\n"
    "ds_read_b64_tr_b16 v[138:139], v161 offset:10240\n"
    "v_mfma_f32_16x16x32_bf16 v[40:43], v[152:155], v[104:107], v[40:43]\n"
    "ds_read_b64_tr_b16 v[132:133], v161 offset:0\n"
    "v_mfma_f32_16x16x32_bf16 v[40:43], v[148:151], v[100:103], v[40:43]\n"
    "ds_read_b64_tr_b16 v[134:135], v161 offset:8192\n"
    "s_waitcnt lgkmcnt(10)\n"
    "v_mfma_f32_16x16x32_bf16 v[56:59], v[148:151], v[108:111], v[56:59]\n"
    "v_mfma_f32_16x16x32_bf16 v[56:59], v[152:155], v[112:115], v[56:59]\n"
    "v_mfma_f32_16x16x32_bf16 v[52:55], v[144:147], v[112:115], v[52:55]\n"
    "v_mfma_f32_16x16x32_bf16 v[52:55], v[140:143], v[108:111], v[52:55]\n"
    "s_waitcnt lgkmcnt(6)\n"
    "v_mfma_f32_16x16x32_bf16 v[68:71], v[140:143], v[116:119], v[68:71]\n"
    "v_mfma_f32_16x16x32_bf16 v[68:71], v[144:147], v[120:123], v[68:71]\n"
    "v_mfma_f32_16x16x32_bf16 v[72:75], v[152:155], v[120:123], v[72:75]\n"
    "s_waitcnt lgkmcnt(0)\n"
    "v_mfma_f32_16x16x32_bf16 v[72:75], v[148:151], v[116:119], v[72:75]\n"
    "s_barrier\n"
    "v_mfma_f32_16x16x32_bf16 v[12:15], v[124:127], v[84:87], v[12:15]\n"
    "ds_read_b64_tr_b16 v[140:141], v160 offset:16384\n"
    "v_mfma_f32_16x16x32_bf16 v[12:15], v[128:131], v[88:91], v[12:15]\n"
    "ds_read_b64_tr_b16 v[142:143], v160 offset:24576\n"
    "v_mfma_f32_16x16x32_bf16 v[16:19], v[136:139], v[88:91], v[16:19]\n"
    "ds_read_b64_tr_b16 v[144:145], v160 offset:18432\n"
    "v_mfma_f32_16x16x32_bf16 v[16:19], v[132:135], v[84:87], v[16:19]\n"
    "ds_read_b64_tr_b16 v[146:147], v160 offset:26624\n"
    "v_mfma_f32_16x16x32_bf16 v[32:35], v[132:135], v[92:95], v[32:35]\n"
    "ds_read_b64_tr_b16 v[152:153], v161 offset:18432\n"
    "v_mfma_f32_16x16x32_bf16 v[32:35], v[136:139], v[96:99], v[32:35]\n"
    "ds_read_b64_tr_b16 v[154:155], v161 offset:26624\n"
    "v_mfma_f32_16x16x32_bf16 v[28:31], v[128:131], v[96:99], v[28:31]\n"
    "ds_read_b64_tr_b16 v[148:149], v161 offset:16384\n"
    "v_mfma_f32_16x16x32_bf16 v[28:31], v[124:127], v[92:95], v[28:31]\n"
    "ds_read_b128 v[88:91], v157 offset:30720\n"
    "v_mfma_f32_16x16x32_bf16 v[44:47], v[124:127], v[100:103], v[44:47]\n"
    "ds_read_b128 v[84:87], v156 offset:30720\n"
    "v_mfma_f32_16x16x32_bf16 v[44:47], v[128:131], v[104:107], v[44:47]\n"
    "ds_read_b64_tr_b16 v[150:151], v161 offset:24576\n"
    "v_mfma_f32_16x16x32_bf16 v[48:51], v[136:139], v[104:107], v[48:51]\n"
    "ds_read_b128 v[96:99], v157 offset:32768\n"
    "v_mfma_f32_16x16x32_bf16 v[48:51], v[132:135], v[100:103], v[48:51]\n"
    "ds_read_b128 v[92:95], v156 offset:32768\n"
    "v_mfma_f32_16x16x32_bf16 v[64:67], v[132:135], v[108:111], v[64:67]\n"
    "ds_read_b128 v[104:107], v157 offset:34816\n"
    "v_mfma_f32_16x16x32_bf16 v[64:67], v[136:139], v[112:115], v[64:67]\n"
    "ds_read_b128 v[100:103], v156 offset:34816\n"
    "v_mfma_f32_16x16x32_bf16 v[60:63], v[128:131], v[112:115], v[60:63]\n"
    "v_mfma_f32_16x16x32_bf16 v[60:63], v[124:127], v[108:111], v[60:63]\n"
    "v_mfma_f32_16x16x32_bf16 v[76:79], v[124:127], v[116:119], v[76:79]\n"
    "v_mfma_f32_16x16x32_bf16 v[76:79], v[128:131], v[120:123], v[76:79]\n"
    "v_mfma_f32_16x16x32_bf16 v[80:83], v[136:139], v[120:123], v[80:83]\n"
    "s_waitcnt lgkmcnt(4)\n"
    "v_mfma_f32_16x16x32_bf16 v[80:83], v[132:135], v[116:119], v[80:83]\n"
    "s_barrier\n"
    "v_mfma_f32_16x16x32_bf16 v[4:7], v[140:143], v[84:87], v[4:7]\n"
    "ds_read_b128 v[112:115], v157 offset:36864\n"
    "v_mfma_f32_16x16x32_bf16 v[4:7], v[144:147], v[88:91], v[4:7]\n"
    "ds_read_b128 v[108:111], v156 offset:36864\n"
    "v_mfma_f32_16x16x32_bf16 v[8:11], v[152:155], v[88:91], v[8:11]\n"
    "ds_read_b64_tr_b16 v[124:125], v160 offset:32768\n"
    "v_mfma_f32_16x16x32_bf16 v[8:11], v[148:151], v[84:87], v[8:11]\n"
    "ds_read_b64_tr_b16 v[126:127], v160 offset:40960\n"
    "s_waitcnt lgkmcnt(6)\n"
    "v_mfma_f32_16x16x32_bf16 v[24:27], v[148:151], v[92:95], v[24:27]\n"
    "ds_read_b128 v[120:123], v157 offset:38912\n"
    "v_mfma_f32_16x16x32_bf16 v[24:27], v[152:155], v[96:99], v[24:27]\n"
    "ds_read_b128 v[116:119], v156 offset:38912\n"
    "v_mfma_f32_16x16x32_bf16 v[20:23], v[144:147], v[96:99], v[20:23]\n"
    "ds_read_b64_tr_b16 v[128:129], v160 offset:34816\n"
    "v_mfma_f32_16x16x32_bf16 v[20:23], v[140:143], v[92:95], v[20:23]\n"
    "ds_read_b64_tr_b16 v[130:131], v160 offset:43008\n"
    "s_waitcnt lgkmcnt(8)\n"
    "v_mfma_f32_16x16x32_bf16 v[36:39], v[140:143], v[100:103], v[36:39]\n"
    "ds_read_b64_tr_b16 v[136:137], v161 offset:34816\n"
    "v_mfma_f32_16x16x32_bf16 v[36:39], v[144:147], v[104:107], v[36:39]\n"
    "ds_read_b64_tr_b16 v[138:139], v161 offset:43008\n"
    "v_mfma_f32_16x16x32_bf16 v[40:43], v[152:155], v[104:107], v[40:43]\n"
    "ds_read_b64_tr_b16 v[132:133], v161 offset:32768\n"
    "v_mfma_f32_16x16x32_bf16 v[40:43], v[148:151], v[100:103], v[40:43]\n"
    "ds_read_b64_tr_b16 v[134:135], v161 offset:40960\n"
    "s_waitcnt lgkmcnt(10)\n"
    "v_mfma_f32_16x16x32_bf16 v[56:59], v[148:151], v[108:111], v[56:59]\n"
    "v_mfma_f32_16x16x32_bf16 v[56:59], v[152:155], v[112:115], v[56:59]\n"
    "v_mfma_f32_16x16x32_bf16 v[52:55], v[144:147], v[112:115], v[52:55]\n"
    "v_mfma_f32_16x16x32_bf16 v[52:55], v[140:143], v[108:111], v[52:55]\n"
    "s_waitcnt lgkmcnt(6)\n"
    "v_mfma_f32_16x16x32_bf16 v[68:71], v[140:143], v[116:119], v[68:71]\n"
    "v_mfma_f32_16x16x32_bf16 v[68:71], v[144:147], v[120:123], v[68:71]\n"
    "v_mfma_f32_16x16x32_bf16 v[72:75], v[152:155], v[120:123], v[72:75]\n"
    "s_waitcnt lgkmcnt(0)\n"
    "v_mfma_f32_16x16x32_bf16 v[72:75], v[148:151], v[116:119], v[72:75]\n"
    "s_barrier\n"
    "v_mfma_f32_16x16x32_bf16 v[12:15], v[124:127], v[84:87], v[12:15]\n"
    "ds_read_b64_tr_b16 v[140:141], v160 offset:49152\n"
    "v_mfma_f32_16x16x32_bf16 v[12:15], v[128:131], v[88:91], v[12:15]\n"
    "ds_read_b64_tr_b16 v[142:143], v160 offset:57344\n"
    "v_mfma_f32_16x16x32_bf16 v[16:19], v[136:139], v[88:91], v[16:19]\n"
    "ds_read_b64_tr_b16 v[144:145], v160 offset:51200\n"
    "v_mfma_f32_16x16x32_bf16 v[16:19], v[132:135], v[84:87], v[16:19]\n"
    "ds_read_b64_tr_b16 v[146:147], v160 offset:59392\n"
    "v_mfma_f32_16x16x32_bf16 v[32:35], v[132:135], v[92:95], v[32:35]\n"
    "ds_read_b64_tr_b16 v[152:153], v161 offset:51200\n"
    "v_mfma_f32_16x16x32_bf16 v[32:35], v[136:139], v[96:99], v[32:35]\n"
    "ds_read_b64_tr_b16 v[154:155], v161 offset:59392\n"
    "v_mfma_f32_16x16x32_bf16 v[28:31], v[128:131], v[96:99], v[28:31]\n"
    "ds_read_b64_tr_b16 v[148:149], v161 offset:49152\n"
    "v_mfma_f32_16x16x32_bf16 v[28:31], v[124:127], v[92:95], v[28:31]\n"
    "ds_read_b128 v[88:91], v157 offset:40960\n"
    "v_mfma_f32_16x16x32_bf16 v[44:47], v[124:127], v[100:103], v[44:47]\n"
    "ds_read_b128 v[84:87], v156 offset:40960\n"
    "v_mfma_f32_16x16x32_bf16 v[44:47], v[128:131], v[104:107], v[44:47]\n"
    "ds_read_b64_tr_b16 v[150:151], v161 offset:57344\n"
    "v_mfma_f32_16x16x32_bf16 v[48:51], v[136:139], v[104:107], v[48:51]\n"
    "ds_read_b128 v[96:99], v157 offset:43008\n"
    "v_mfma_f32_16x16x32_bf16 v[48:51], v[132:135], v[100:103], v[48:51]\n"
    "ds_read_b128 v[92:95], v156 offset:43008\n"
    "v_mfma_f32_16x16x32_bf16 v[64:67], v[132:135], v[108:111], v[64:67]\n"
    "ds_read_b128 v[104:107], v157 offset:45056\n"
    "v_mfma_f32_16x16x32_bf16 v[64:67], v[136:139], v[112:115], v[64:67]\n"
    "ds_read_b128 v[100:103], v156 offset:45056\n"
    "v_mfma_f32_16x16x32_bf16 v[60:63], v[128:131], v[112:115], v[60:63]\n"
    "v_mfma_f32_16x16x32_bf16 v[60:63], v[124:127], v[108:111], v[60:63]\n"
    "v_mfma_f32_16x16x32_bf16 v[76:79], v[124:127], v[116:119], v[76:79]\n"
    "v_mfma_f32_16x16x32_bf16 v[76:79], v[128:131], v[120:123], v[76:79]\n"
    "v_mfma_f32_16x16x32_bf16 v[80:83], v[136:139], v[120:123], v[80:83]\n"
    "s_waitcnt lgkmcnt(4)\n"
    "v_mfma_f32_16x16x32_bf16 v[80:83], v[132:135], v[116:119], v[80:83]\n"
    "s_barrier\n"
    "v_mfma_f32_16x16x32_bf16 v[4:7], v[140:143], v[84:87], v[4:7]\n"
    "ds_read_b128 v[112:115], v157 offset:47104\n"
    "v_mfma_f32_16x16x32_bf16 v[4:7], v[144:147], v[88:91], v[4:7]\n"
    "ds_read_b128 v[108:111], v156 offset:47104\n"
    "v_mfma_f32_16x16x32_bf16 v[8:11], v[152:155], v[88:91], v[8:11]\n"
    "ds_read_b64_tr_b16 v[124:125], v160 offset:0\n"
    "v_mfma_f32_16x16x32_bf16 v[8:11], v[148:151], v[84:87], v[8:11]\n"
    "ds_read_b64_tr_b16 v[126:127], v160 offset:8192\n"
    "s_waitcnt lgkmcnt(6)\n"
    "v_mfma_f32_16x16x32_bf16 v[24:27], v[148:151], v[92:95], v[24:27]\n"
    "ds_read_b128 v[120:123], v157 offset:49152\n"
    "v_mfma_f32_16x16x32_bf16 v[24:27], v[152:155], v[96:99], v[24:27]\n"
    "ds_read_b128 v[116:119], v156 offset:49152\n"
    "v_mfma_f32_16x16x32_bf16 v[20:23], v[144:147], v[96:99], v[20:23]\n"
    "ds_read_b64_tr_b16 v[128:129], v160 offset:2048\n"
    "v_mfma_f32_16x16x32_bf16 v[20:23], v[140:143], v[92:95], v[20:23]\n"
    "ds_read_b64_tr_b16 v[130:131], v160 offset:10240\n"
    "s_waitcnt lgkmcnt(8)\n"
    "v_mfma_f32_16x16x32_bf16 v[36:39], v[140:143], v[100:103], v[36:39]\n"
    "ds_read_b64_tr_b16 v[136:137], v161 offset:2048\n"
    "v_mfma_f32_16x16x32_bf16 v[36:39], v[144:147], v[104:107], v[36:39]\n"
    "ds_read_b64_tr_b16 v[138:139], v161 offset:10240\n"
    "v_mfma_f32_16x16x32_bf16 v[40:43], v[152:155], v[104:107], v[40:43]\n"
    "ds_read_b64_tr_b16 v[132:133], v161 offset:0\n"
    "v_mfma_f32_16x16x32_bf16 v[40:43], v[148:151], v[100:103], v[40:43]\n"
    "ds_read_b64_tr_b16 v[134:135], v161 offset:8192\n"
    "s_waitcnt lgkmcnt(10)\n"
    "v_mfma_f32_16x16x32_bf16 v[56:59], v[148:151], v[108:111], v[56:59]\n"
    "v_mfma_f32_16x16x32_bf16 v[56:59], v[152:155], v[112:115], v[56:59]\n"
    "v_mfma_f32_16x16x32_bf16 v[52:55], v[144:147], v[112:115], v[52:55]\n"
    "v_mfma_f32_16x16x32_bf16 v[52:55], v[140:143], v[108:111], v[52:55]\n"
    "s_waitcnt lgkmcnt(6)\n"
    "v_mfma_f32_16x16x32_bf16 v[68:71], v[140:143], v[116:119], v[68:71]\n"
    ".Lpage6:\n"
    "s_cbranch_execz .Ltouch_next\n"
    "v_mfma_f32_16x16x32_bf16 v[68:71], v[144:147], v[120:123], v[68:71]\n"
    "v_mfma_f32_16x16x32_bf16 v[72:75], v[152:155], v[120:123], v[72:75]\n"
    "s_waitcnt lgkmcnt(0)\n"
    "v_mfma_f32_16x16x32_bf16 v[72:75], v[148:151], v[116:119], v[72:75]\n"
    "s_barrier\n"
    "v_mfma_f32_16x16x32_bf16 v[12:15], v[124:127], v[84:87], v[12:15]\n"
    "v_add_u32 v156, 51200, v156\n"
    "v_add_u32 v157, 51200, v157\n"
    "v_mfma_f32_16x16x32_bf16 v[12:15], v[128:131], v[88:91], v[12:15]\n"
    "ds_read_b64_tr_b16 v[140:141], v160 offset:16384\n"
    "v_mfma_f32_16x16x32_bf16 v[16:19], v[136:139], v[88:91], v[16:19]\n"
    "ds_read_b64_tr_b16 v[142:143], v160 offset:24576\n"
    "v_mfma_f32_16x16x32_bf16 v[16:19], v[132:135], v[84:87], v[16:19]\n"
    "ds_read_b64_tr_b16 v[144:145], v160 offset:18432\n"
    "v_mfma_f32_16x16x32_bf16 v[32:35], v[132:135], v[92:95], v[32:35]\n"
    "ds_read_b64_tr_b16 v[146:147], v160 offset:26624\n"
    "v_mfma_f32_16x16x32_bf16 v[32:35], v[136:139], v[96:99], v[32:35]\n"
    "ds_read_b64_tr_b16 v[152:153], v161 offset:18432\n"
    "v_mfma_f32_16x16x32_bf16 v[28:31], v[128:131], v[96:99], v[28:31]\n"
    "ds_read_b64_tr_b16 v[154:155], v161 offset:26624\n"
    "v_mfma_f32_16x16x32_bf16 v[28:31], v[124:127], v[92:95], v[28:31]\n"
    "ds_read_b64_tr_b16 v[148:149], v161 offset:16384\n"
    "v_mfma_f32_16x16x32_bf16 v[44:47], v[124:127], v[100:103], v[44:47]\n"
    "ds_read_b128 v[88:91], v157 offset:0\n"
    "v_mfma_f32_16x16x32_bf16 v[44:47], v[128:131], v[104:107], v[44:47]\n"
    "ds_read_b128 v[84:87], v156 offset:0\n"
    "v_mfma_f32_16x16x32_bf16 v[48:51], v[136:139], v[104:107], v[48:51]\n"
    "ds_read_b64_tr_b16 v[150:151], v161 offset:24576\n"
    "v_mfma_f32_16x16x32_bf16 v[48:51], v[132:135], v[100:103], v[48:51]\n"
    "ds_read_b128 v[96:99], v157 offset:2048\n"
    "v_mfma_f32_16x16x32_bf16 v[64:67], v[132:135], v[108:111], v[64:67]\n"
    "ds_read_b128 v[92:95], v156 offset:2048\n"
    "v_mfma_f32_16x16x32_bf16 v[64:67], v[136:139], v[112:115], v[64:67]\n"
    "ds_read_b128 v[104:107], v157 offset:4096\n"
    "v_mfma_f32_16x16x32_bf16 v[60:63], v[128:131], v[112:115], v[60:63]\n"
    "ds_read_b128 v[100:103], v156 offset:4096\n"
    "v_mfma_f32_16x16x32_bf16 v[60:63], v[124:127], v[108:111], v[60:63]\n"
    "v_mfma_f32_16x16x32_bf16 v[76:79], v[124:127], v[116:119], v[76:79]\n"
    "v_mfma_f32_16x16x32_bf16 v[76:79], v[128:131], v[120:123], v[76:79]\n"
    "v_mfma_f32_16x16x32_bf16 v[80:83], v[136:139], v[120:123], v[80:83]\n"
    "s_waitcnt lgkmcnt(4)\n"
    "v_mfma_f32_16x16x32_bf16 v[80:83], v[132:135], v[116:119], v[80:83]\n"
    "s_barrier\n"
    "v_mfma_f32_16x16x32_bf16 v[4:7], v[140:143], v[84:87], v[4:7]\n"
    "ds_read_b128 v[112:115], v157 offset:6144\n"
    "v_mfma_f32_16x16x32_bf16 v[4:7], v[144:147], v[88:91], v[4:7]\n"
    "ds_read_b128 v[108:111], v156 offset:6144\n"
    "v_mfma_f32_16x16x32_bf16 v[8:11], v[152:155], v[88:91], v[8:11]\n"
    "ds_read_b64_tr_b16 v[124:125], v160 offset:32768\n"
    "v_mfma_f32_16x16x32_bf16 v[8:11], v[148:151], v[84:87], v[8:11]\n"
    "ds_read_b64_tr_b16 v[126:127], v160 offset:40960\n"
    "s_waitcnt lgkmcnt(6)\n"
    "v_mfma_f32_16x16x32_bf16 v[24:27], v[148:151], v[92:95], v[24:27]\n"
    "ds_read_b128 v[120:123], v157 offset:8192\n"
    "v_mfma_f32_16x16x32_bf16 v[24:27], v[152:155], v[96:99], v[24:27]\n"
    "ds_read_b128 v[116:119], v156 offset:8192\n"
    "v_mfma_f32_16x16x32_bf16 v[20:23], v[144:147], v[96:99], v[20:23]\n"
    "ds_read_b64_tr_b16 v[128:129], v160 offset:34816\n"
    "v_mfma_f32_16x16x32_bf16 v[20:23], v[140:143], v[92:95], v[20:23]\n"
    "ds_read_b64_tr_b16 v[130:131], v160 offset:43008\n"
    "s_waitcnt lgkmcnt(8)\n"
    "v_mfma_f32_16x16x32_bf16 v[36:39], v[140:143], v[100:103], v[36:39]\n"
    "ds_read_b64_tr_b16 v[136:137], v161 offset:34816\n"
    "v_mfma_f32_16x16x32_bf16 v[36:39], v[144:147], v[104:107], v[36:39]\n"
    "ds_read_b64_tr_b16 v[138:139], v161 offset:43008\n"
    "v_mfma_f32_16x16x32_bf16 v[40:43], v[152:155], v[104:107], v[40:43]\n"
    "ds_read_b64_tr_b16 v[132:133], v161 offset:32768\n"
    "v_mfma_f32_16x16x32_bf16 v[40:43], v[148:151], v[100:103], v[40:43]\n"
    "ds_read_b64_tr_b16 v[134:135], v161 offset:40960\n"
    "s_waitcnt lgkmcnt(10)\n"
    "v_mfma_f32_16x16x32_bf16 v[56:59], v[148:151], v[108:111], v[56:59]\n"
    "v_mfma_f32_16x16x32_bf16 v[56:59], v[152:155], v[112:115], v[56:59]\n"
    "v_mfma_f32_16x16x32_bf16 v[52:55], v[144:147], v[112:115], v[52:55]\n"
    "v_mfma_f32_16x16x32_bf16 v[52:55], v[140:143], v[108:111], v[52:55]\n"
    "s_waitcnt lgkmcnt(6)\n"
    "v_mfma_f32_16x16x32_bf16 v[68:71], v[140:143], v[116:119], v[68:71]\n"
    "v_mfma_f32_16x16x32_bf16 v[68:71], v[144:147], v[120:123], v[68:71]\n"
    "v_mfma_f32_16x16x32_bf16 v[72:75], v[152:155], v[120:123], v[72:75]\n"
    "s_waitcnt lgkmcnt(0)\n"
    "v_mfma_f32_16x16x32_bf16 v[72:75], v[148:151], v[116:119], v[72:75]\n"
    "s_barrier\n"
    "v_mfma_f32_16x16x32_bf16 v[12:15], v[124:127], v[84:87], v[12:15]\n"
    "ds_read_b64_tr_b16 v[140:141], v160 offset:49152\n"
    "v_mfma_f32_16x16x32_bf16 v[12:15], v[128:131], v[88:91], v[12:15]\n"
    "ds_read_b64_tr_b16 v[142:143], v160 offset:57344\n"
    "v_mfma_f32_16x16x32_bf16 v[16:19], v[136:139], v[88:91], v[16:19]\n"
    "ds_read_b64_tr_b16 v[144:145], v160 offset:51200\n"
    "v_mfma_f32_16x16x32_bf16 v[16:19], v[132:135], v[84:87], v[16:19]\n"
    "ds_read_b64_tr_b16 v[146:147], v160 offset:59392\n"
    "v_mfma_f32_16x16x32_bf16 v[32:35], v[132:135], v[92:95], v[32:35]\n"
    "ds_read_b64_tr_b16 v[152:153], v161 offset:51200\n"
    "v_mfma_f32_16x16x32_bf16 v[32:35], v[136:139], v[96:99], v[32:35]\n"
    "ds_read_b64_tr_b16 v[154:155], v161 offset:59392\n"
    "v_mfma_f32_16x16x32_bf16 v[28:31], v[128:131], v[96:99], v[28:31]\n"
    "ds_read_b64_tr_b16 v[148:149], v161 offset:49152\n"
    "v_mfma_f32_16x16x32_bf16 v[28:31], v[124:127], v[92:95], v[28:31]\n"
    "ds_read_b128 v[88:91], v157 offset:10240\n"
    "v_mfma_f32_16x16x32_bf16 v[44:47], v[124:127], v[100:103], v[44:47]\n"
    "ds_read_b128 v[84:87], v156 offset:10240\n"
    "v_mfma_f32_16x16x32_bf16 v[44:47], v[128:131], v[104:107], v[44:47]\n"
    "ds_read_b64_tr_b16 v[150:151], v161 offset:57344\n"
    "v_mfma_f32_16x16x32_bf16 v[48:51], v[136:139], v[104:107], v[48:51]\n"
    "ds_read_b128 v[96:99], v157 offset:12288\n"
    "v_mfma_f32_16x16x32_bf16 v[48:51], v[132:135], v[100:103], v[48:51]\n"
    "ds_read_b128 v[92:95], v156 offset:12288\n"
    "v_mfma_f32_16x16x32_bf16 v[64:67], v[132:135], v[108:111], v[64:67]\n"
    "ds_read_b128 v[104:107], v157 offset:14336\n"
    "v_mfma_f32_16x16x32_bf16 v[64:67], v[136:139], v[112:115], v[64:67]\n"
    "ds_read_b128 v[100:103], v156 offset:14336\n"
    "v_mfma_f32_16x16x32_bf16 v[60:63], v[128:131], v[112:115], v[60:63]\n"
    "v_mfma_f32_16x16x32_bf16 v[60:63], v[124:127], v[108:111], v[60:63]\n"
    "v_mfma_f32_16x16x32_bf16 v[76:79], v[124:127], v[116:119], v[76:79]\n"
    "v_mfma_f32_16x16x32_bf16 v[76:79], v[128:131], v[120:123], v[76:79]\n"
    "v_mfma_f32_16x16x32_bf16 v[80:83], v[136:139], v[120:123], v[80:83]\n"
    "s_waitcnt lgkmcnt(4)\n"
    "v_mfma_f32_16x16x32_bf16 v[80:83], v[132:135], v[116:119], v[80:83]\n"
    "s_barrier\n"
    "v_mfma_f32_16x16x32_bf16 v[4:7], v[140:143], v[84:87], v[4:7]\n"
    "ds_read_b128 v[112:115], v157 offset:16384\n"
    "v_mfma_f32_16x16x32_bf16 v[4:7], v[144:147], v[88:91], v[4:7]\n"
    "ds_read_b128 v[108:111], v156 offset:16384\n"
    "v_mfma_f32_16x16x32_bf16 v[8:11], v[152:155], v[88:91], v[8:11]\n"
    "ds_read_b64_tr_b16 v[124:125], v160 offset:0\n"
    "v_mfma_f32_16x16x32_bf16 v[8:11], v[148:151], v[84:87], v[8:11]\n"
    "ds_read_b64_tr_b16 v[126:127], v160 offset:8192\n"
    "s_waitcnt lgkmcnt(6)\n"
    "v_mfma_f32_16x16x32_bf16 v[24:27], v[148:151], v[92:95], v[24:27]\n"
    "ds_read_b128 v[120:123], v157 offset:18432\n"
    "v_mfma_f32_16x16x32_bf16 v[24:27], v[152:155], v[96:99], v[24:27]\n"
    "ds_read_b128 v[116:119], v156 offset:18432\n"
    "v_mfma_f32_16x16x32_bf16 v[20:23], v[144:147], v[96:99], v[20:23]\n"
    "ds_read_b64_tr_b16 v[128:129], v160 offset:2048\n"
    "v_mfma_f32_16x16x32_bf16 v[20:23], v[140:143], v[92:95], v[20:23]\n"
    "ds_read_b64_tr_b16 v[130:131], v160 offset:10240\n"
    "s_waitcnt lgkmcnt(8)\n"
    "v_mfma_f32_16x16x32_bf16 v[36:39], v[140:143], v[100:103], v[36:39]\n"
    "ds_read_b64_tr_b16 v[136:137], v161 offset:2048\n"
    "v_mfma_f32_16x16x32_bf16 v[36:39], v[144:147], v[104:107], v[36:39]\n"
    "ds_read_b64_tr_b16 v[138:139], v161 offset:10240\n"
    "v_mfma_f32_16x16x32_bf16 v[40:43], v[152:155], v[104:107], v[40:43]\n"
    "ds_read_b64_tr_b16 v[132:133], v161 offset:0\n"
    "v_mfma_f32_16x16x32_bf16 v[40:43], v[148:151], v[100:103], v[40:43]\n"
    "ds_read_b64_tr_b16 v[134:135], v161 offset:8192\n"
    "s_waitcnt lgkmcnt(10)\n"
    "v_mfma_f32_16x16x32_bf16 v[56:59], v[148:151], v[108:111], v[56:59]\n"
    "v_mfma_f32_16x16x32_bf16 v[56:59], v[152:155], v[112:115], v[56:59]\n"
    "v_mfma_f32_16x16x32_bf16 v[52:55], v[144:147], v[112:115], v[52:55]\n"
    "v_mfma_f32_16x16x32_bf16 v[52:55], v[140:143], v[108:111], v[52:55]\n"
    "s_waitcnt lgkmcnt(6)\n"
    "v_mfma_f32_16x16x32_bf16 v[68:71], v[140:143], v[116:119], v[68:71]\n"
    "v_mfma_f32_16x16x32_bf16 v[68:71], v[144:147], v[120:123], v[68:71]\n"
    "v_mfma_f32_16x16x32_bf16 v[72:75], v[152:155], v[120:123], v[72:75]\n"
    "s_waitcnt lgkmcnt(0)\n"
    "v_mfma_f32_16x16x32_bf16 v[72:75], v[148:151], v[116:119], v[72:75]\n"
    "s_barrier\n"
    "v_mfma_f32_16x16x32_bf16 v[12:15], v[124:127], v[84:87], v[12:15]\n"
    "ds_read_b64_tr_b16 v[140:141], v160 offset:16384\n"
    "v_mfma_f32_16x16x32_bf16 v[12:15], v[128:131], v[88:91], v[12:15]\n"
    "ds_read_b64_tr_b16 v[142:143], v160 offset:24576\n"
    "v_mfma_f32_16x16x32_bf16 v[16:19], v[136:139], v[88:91], v[16:19]\n"
    "ds_read_b64_tr_b16 v[144:145], v160 offset:18432\n"
    "v_mfma_f32_16x16x32_bf16 v[16:19], v[132:135], v[84:87], v[16:19]\n"
    "ds_read_b64_tr_b16 v[146:147], v160 offset:26624\n"
    "v_mfma_f32_16x16x32_bf16 v[32:35], v[132:135], v[92:95], v[32:35]\n"
    "ds_read_b64_tr_b16 v[152:153], v161 offset:18432\n"
    "v_mfma_f32_16x16x32_bf16 v[32:35], v[136:139], v[96:99], v[32:35]\n"
    "ds_read_b64_tr_b16 v[154:155], v161 offset:26624\n"
    "v_mfma_f32_16x16x32_bf16 v[28:31], v[128:131], v[96:99], v[28:31]\n"
    "ds_read_b64_tr_b16 v[148:149], v161 offset:16384\n"
    "v_mfma_f32_16x16x32_bf16 v[28:31], v[124:127], v[92:95], v[28:31]\n"
    "ds_read_b128 v[88:91], v157 offset:20480\n"
    "v_mfma_f32_16x16x32_bf16 v[44:47], v[124:127], v[100:103], v[44:47]\n"
    "ds_read_b128 v[84:87], v156 offset:20480\n"
    "v_mfma_f32_16x16x32_bf16 v[44:47], v[128:131], v[104:107], v[44:47]\n"
    "ds_read_b64_tr_b16 v[150:151], v161 offset:24576\n"
    "v_mfma_f32_16x16x32_bf16 v[48:51], v[136:139], v[104:107], v[48:51]\n"
    "ds_read_b128 v[96:99], v157 offset:22528\n"
    "v_mfma_f32_16x16x32_bf16 v[48:51], v[132:135], v[100:103], v[48:51]\n"
    "ds_read_b128 v[92:95], v156 offset:22528\n"
    "v_mfma_f32_16x16x32_bf16 v[64:67], v[132:135], v[108:111], v[64:67]\n"
    "ds_read_b128 v[104:107], v157 offset:24576\n"
    "v_mfma_f32_16x16x32_bf16 v[64:67], v[136:139], v[112:115], v[64:67]\n"
    "ds_read_b128 v[100:103], v156 offset:24576\n"
    "v_mfma_f32_16x16x32_bf16 v[60:63], v[128:131], v[112:115], v[60:63]\n"
    "v_mfma_f32_16x16x32_bf16 v[60:63], v[124:127], v[108:111], v[60:63]\n"
    "v_mfma_f32_16x16x32_bf16 v[76:79], v[124:127], v[116:119], v[76:79]\n"
    "v_mfma_f32_16x16x32_bf16 v[76:79], v[128:131], v[120:123], v[76:79]\n"
    "v_mfma_f32_16x16x32_bf16 v[80:83], v[136:139], v[120:123], v[80:83]\n"
    "s_waitcnt lgkmcnt(4)\n"
    "v_mfma_f32_16x16x32_bf16 v[80:83], v[132:135], v[116:119], v[80:83]\n"
    "s_barrier\n"
    "v_mfma_f32_16x16x32_bf16 v[4:7], v[140:143], v[84:87], v[4:7]\n"
    "ds_read_b128 v[112:115], v157 offset:26624\n"
    "v_mfma_f32_16x16x32_bf16 v[4:7], v[144:147], v[88:91], v[4:7]\n"
    "ds_read_b128 v[108:111], v156 offset:26624\n"
    "v_mfma_f32_16x16x32_bf16 v[8:11], v[152:155], v[88:91], v[8:11]\n"
    "ds_read_b64_tr_b16 v[124:125], v160 offset:32768\n"
    "v_mfma_f32_16x16x32_bf16 v[8:11], v[148:151], v[84:87], v[8:11]\n"
    "ds_read_b64_tr_b16 v[126:127], v160 offset:40960\n"
    "s_waitcnt lgkmcnt(6)\n"
    "v_mfma_f32_16x16x32_bf16 v[24:27], v[148:151], v[92:95], v[24:27]\n"
    "ds_read_b128 v[120:123], v157 offset:28672\n"
    "v_mfma_f32_16x16x32_bf16 v[24:27], v[152:155], v[96:99], v[24:27]\n"
    "ds_read_b128 v[116:119], v156 offset:28672\n"
    "v_mfma_f32_16x16x32_bf16 v[20:23], v[144:147], v[96:99], v[20:23]\n"
    "ds_read_b64_tr_b16 v[128:129], v160 offset:34816\n"
    "v_mfma_f32_16x16x32_bf16 v[20:23], v[140:143], v[92:95], v[20:23]\n"
    "ds_read_b64_tr_b16 v[130:131], v160 offset:43008\n"
    "s_waitcnt lgkmcnt(8)\n"
    "v_mfma_f32_16x16x32_bf16 v[36:39], v[140:143], v[100:103], v[36:39]\n"
    "ds_read_b64_tr_b16 v[136:137], v161 offset:34816\n"
    "v_mfma_f32_16x16x32_bf16 v[36:39], v[144:147], v[104:107], v[36:39]\n"
    "ds_read_b64_tr_b16 v[138:139], v161 offset:43008\n"
    "v_mfma_f32_16x16x32_bf16 v[40:43], v[152:155], v[104:107], v[40:43]\n"
    "ds_read_b64_tr_b16 v[132:133], v161 offset:32768\n"
    "v_mfma_f32_16x16x32_bf16 v[40:43], v[148:151], v[100:103], v[40:43]\n"
    "ds_read_b64_tr_b16 v[134:135], v161 offset:40960\n"
    "s_waitcnt lgkmcnt(10)\n"
    "v_mfma_f32_16x16x32_bf16 v[56:59], v[148:151], v[108:111], v[56:59]\n"
    "v_mfma_f32_16x16x32_bf16 v[56:59], v[152:155], v[112:115], v[56:59]\n"
    "v_mfma_f32_16x16x32_bf16 v[52:55], v[144:147], v[112:115], v[52:55]\n"
    "v_mfma_f32_16x16x32_bf16 v[52:55], v[140:143], v[108:111], v[52:55]\n"
    "s_waitcnt lgkmcnt(6)\n"
    "v_mfma_f32_16x16x32_bf16 v[68:71], v[140:143], v[116:119], v[68:71]\n"
    "v_mfma_f32_16x16x32_bf16 v[68:71], v[144:147], v[120:123], v[68:71]\n"
    "v_mfma_f32_16x16x32_bf16 v[72:75], v[152:155], v[120:123], v[72:75]\n"
    "s_waitcnt lgkmcnt(0)\n"
    "v_mfma_f32_16x16x32_bf16 v[72:75], v[148:151], v[116:119], v[72:75]\n"
    "s_barrier\n"
    "v_mfma_f32_16x16x32_bf16 v[12:15], v[124:127], v[84:87], v[12:15]\n"
    "ds_read_b64_tr_b16 v[140:141], v160 offset:49152\n"
    "v_mfma_f32_16x16x32_bf16 v[12:15], v[128:131], v[88:91], v[12:15]\n"
    "ds_read_b64_tr_b16 v[142:143], v160 offset:57344\n"
    "v_mfma_f32_16x16x32_bf16 v[16:19], v[136:139], v[88:91], v[16:19]\n"
    "ds_read_b64_tr_b16 v[144:145], v160 offset:51200\n"
    "v_mfma_f32_16x16x32_bf16 v[16:19], v[132:135], v[84:87], v[16:19]\n"
    "ds_read_b64_tr_b16 v[146:147], v160 offset:59392\n"
    "v_mfma_f32_16x16x32_bf16 v[32:35], v[132:135], v[92:95], v[32:35]\n"
    "ds_read_b64_tr_b16 v[152:153], v161 offset:51200\n"
    "v_mfma_f32_16x16x32_bf16 v[32:35], v[136:139], v[96:99], v[32:35]\n"
    "ds_read_b64_tr_b16 v[154:155], v161 offset:59392\n"
    "v_mfma_f32_16x16x32_bf16 v[28:31], v[128:131], v[96:99], v[28:31]\n"
    "ds_read_b64_tr_b16 v[148:149], v161 offset:49152\n"
    "v_mfma_f32_16x16x32_bf16 v[28:31], v[124:127], v[92:95], v[28:31]\n"
    "ds_read_b128 v[88:91], v157 offset:30720\n"
    "v_mfma_f32_16x16x32_bf16 v[44:47], v[124:127], v[100:103], v[44:47]\n"
    "ds_read_b128 v[84:87], v156 offset:30720\n"
    "v_mfma_f32_16x16x32_bf16 v[44:47], v[128:131], v[104:107], v[44:47]\n"
    "ds_read_b64_tr_b16 v[150:151], v161 offset:57344\n"
    "v_mfma_f32_16x16x32_bf16 v[48:51], v[136:139], v[104:107], v[48:51]\n"
    "ds_read_b128 v[96:99], v157 offset:32768\n"
    "v_mfma_f32_16x16x32_bf16 v[48:51], v[132:135], v[100:103], v[48:51]\n"
    "ds_read_b128 v[92:95], v156 offset:32768\n"
    "v_mfma_f32_16x16x32_bf16 v[64:67], v[132:135], v[108:111], v[64:67]\n"
    "ds_read_b128 v[104:107], v157 offset:34816\n"
    "v_mfma_f32_16x16x32_bf16 v[64:67], v[136:139], v[112:115], v[64:67]\n"
    "ds_read_b128 v[100:103], v156 offset:34816\n"
    "v_mfma_f32_16x16x32_bf16 v[60:63], v[128:131], v[112:115], v[60:63]\n"
    "v_mfma_f32_16x16x32_bf16 v[60:63], v[124:127], v[108:111], v[60:63]\n"
    "v_mfma_f32_16x16x32_bf16 v[76:79], v[124:127], v[116:119], v[76:79]\n"
    "v_mfma_f32_16x16x32_bf16 v[76:79], v[128:131], v[120:123], v[76:79]\n"
    "v_mfma_f32_16x16x32_bf16 v[80:83], v[136:139], v[120:123], v[80:83]\n"
    "s_waitcnt lgkmcnt(4)\n"
    "v_mfma_f32_16x16x32_bf16 v[80:83], v[132:135], v[116:119], v[80:83]\n"
    "s_barrier\n"
    "v_mfma_f32_16x16x32_bf16 v[4:7], v[140:143], v[84:87], v[4:7]\n"
    "ds_read_b128 v[112:115], v157 offset:36864\n"
    "v_mfma_f32_16x16x32_bf16 v[4:7], v[144:147], v[88:91], v[4:7]\n"
    "ds_read_b128 v[108:111], v156 offset:36864\n"
    "v_mfma_f32_16x16x32_bf16 v[8:11], v[152:155], v[88:91], v[8:11]\n"
    "ds_read_b64_tr_b16 v[124:125], v160 offset:0\n"
    "v_mfma_f32_16x16x32_bf16 v[8:11], v[148:151], v[84:87], v[8:11]\n"
    "ds_read_b64_tr_b16 v[126:127], v160 offset:8192\n"
    "s_waitcnt lgkmcnt(6)\n"
    "v_mfma_f32_16x16x32_bf16 v[24:27], v[148:151], v[92:95], v[24:27]\n"
    "ds_read_b128 v[120:123], v157 offset:38912\n"
    "v_mfma_f32_16x16x32_bf16 v[24:27], v[152:155], v[96:99], v[24:27]\n"
    "ds_read_b128 v[116:119], v156 offset:38912\n"
    "v_mfma_f32_16x16x32_bf16 v[20:23], v[144:147], v[96:99], v[20:23]\n"
    "ds_read_b64_tr_b16 v[128:129], v160 offset:2048\n"
    "v_mfma_f32_16x16x32_bf16 v[20:23], v[140:143], v[92:95], v[20:23]\n"
    "ds_read_b64_tr_b16 v[130:131], v160 offset:10240\n"
    "s_waitcnt lgkmcnt(8)\n"
    "v_mfma_f32_16x16x32_bf16 v[36:39], v[140:143], v[100:103], v[36:39]\n"
    "ds_read_b64_tr_b16 v[136:137], v161 offset:2048\n"
    "v_mfma_f32_16x16x32_bf16 v[36:39], v[144:147], v[104:107], v[36:39]\n"
    "ds_read_b64_tr_b16 v[138:139], v161 offset:10240\n"
    "v_mfma_f32_16x16x32_bf16 v[40:43], v[152:155], v[104:107], v[40:43]\n"
    "ds_read_b64_tr_b16 v[132:133], v161 offset:0\n"
    "v_mfma_f32_16x16x32_bf16 v[40:43], v[148:151], v[100:103], v[40:43]\n"
    "ds_read_b64_tr_b16 v[134:135], v161 offset:8192\n"
    "s_waitcnt lgkmcnt(10)\n"
    "v_mfma_f32_16x16x32_bf16 v[56:59], v[148:151], v[108:111], v[56:59]\n"
    "v_mfma_f32_16x16x32_bf16 v[56:59], v[152:155], v[112:115], v[56:59]\n"
    "v_mfma_f32_16x16x32_bf16 v[52:55], v[144:147], v[112:115], v[52:55]\n"
    "v_mfma_f32_16x16x32_bf16 v[52:55], v[140:143], v[108:111], v[52:55]\n"
    "s_waitcnt lgkmcnt(6)\n"
    "v_mfma_f32_16x16x32_bf16 v[68:71], v[140:143], v[116:119], v[68:71]\n"
    "v_mfma_f32_16x16x32_bf16 v[68:71], v[144:147], v[120:123], v[68:71]\n"
    "v_mfma_f32_16x16x32_bf16 v[72:75], v[152:155], v[120:123], v[72:75]\n"
    "s_waitcnt lgkmcnt(0)\n"
    "v_mfma_f32_16x16x32_bf16 v[72:75], v[148:151], v[116:119], v[72:75]\n"
    "s_barrier\n"
    "v_mfma_f32_16x16x32_bf16 v[12:15], v[124:127], v[84:87], v[12:15]\n"
    "ds_read_b128 v[140:143], v159 offset:0\n"
    "v_mfma_f32_16x16x32_bf16 v[12:15], v[128:131], v[88:91], v[12:15]\n"
    "ds_read_b128 v[144:147], v159 offset:64\n"
    "v_mfma_f32_16x16x32_bf16 v[16:19], v[136:139], v[88:91], v[16:19]\n"
    "ds_read_b128 v[148:151], v159 offset:512\n"
    "v_mfma_f32_16x16x32_bf16 v[16:19], v[132:135], v[84:87], v[16:19]\n"
    "ds_read_b128 v[152:155], v159 offset:576\n"
    "v_mfma_f32_16x16x32_bf16 v[32:35], v[132:135], v[92:95], v[32:35]\n"
    "v_mfma_f32_16x16x32_bf16 v[32:35], v[136:139], v[96:99], v[32:35]\n"
    "v_mfma_f32_16x16x32_bf16 v[28:31], v[128:131], v[96:99], v[28:31]\n"
    "v_mfma_f32_16x16x32_bf16 v[28:31], v[124:127], v[92:95], v[28:31]\n"
    "ds_read_b64 v[84:85], v158 offset:0\n"
    "v_mfma_f32_16x16x32_bf16 v[44:47], v[124:127], v[100:103], v[44:47]\n"
    "ds_read_b64 v[86:87], v158 offset:128\n"
    "v_mfma_f32_16x16x32_bf16 v[44:47], v[128:131], v[104:107], v[44:47]\n"
    "ds_read_b64 v[88:89], v158 offset:256\n"
    "v_mfma_f32_16x16x32_bf16 v[48:51], v[136:139], v[104:107], v[48:51]\n"
    "ds_read_b64 v[90:91], v158 offset:384\n"
    "v_mfma_f32_16x16x32_bf16 v[48:51], v[132:135], v[100:103], v[48:51]\n"
    "ds_read_b64 v[92:93], v158 offset:512\n"
    "v_mfma_f32_16x16x32_bf16 v[64:67], v[132:135], v[108:111], v[64:67]\n"
    "v_mfma_f32_16x16x32_bf16 v[64:67], v[136:139], v[112:115], v[64:67]\n"
    "v_mfma_f32_16x16x32_bf16 v[60:63], v[128:131], v[112:115], v[60:63]\n"
    "v_mfma_f32_16x16x32_bf16 v[60:63], v[124:127], v[108:111], v[60:63]\n"
    "v_mfma_f32_16x16x32_bf16 v[76:79], v[124:127], v[116:119], v[76:79]\n"
    "v_mfma_f32_16x16x32_bf16 v[76:79], v[128:131], v[120:123], v[76:79]\n"
    "v_mfma_f32_16x16x32_bf16 v[80:83], v[136:139], v[120:123], v[80:83]\n"
    "v_mfma_f32_16x16x32_bf16 v[80:83], v[132:135], v[116:119], v[80:83]\n"
    "s_waitcnt lgkmcnt(5)\n"
    "s_setprio 0\n"
    "s_barrier\n"
    "s_waitcnt lgkmcnt(0)\n"
    "v_fma_f32 v104, v84, v4, v140\n"
    "v_fma_f32 v105, v84, v5, v141\n"
    "v_fma_f32 v106, v84, v6, v142\n"
    "v_fma_f32 v107, v84, v7, v143\n"
    "v_fma_f32 v108, v84, v8, v144\n"
    "v_fma_f32 v109, v84, v9, v145\n"
    "v_fma_f32 v110, v84, v10, v146\n"
    "v_fma_f32 v111, v84, v11, v147\n"
    "v_fmac_f32_dpp v104, v68, v85 row_newbcast:15 row_mask:0xf bank_mask:0xf\n"
    "v_fmac_f32_dpp v105, v69, v85 row_newbcast:15 row_mask:0xf bank_mask:0xf\n"
    "v_fmac_f32_dpp v106, v70, v85 row_newbcast:15 row_mask:0xf bank_mask:0xf\n"
    "v_fmac_f32_dpp v107, v71, v85 row_newbcast:15 row_mask:0xf bank_mask:0xf\n"
    "v_fmac_f32_dpp v108, v72, v85 row_newbcast:15 row_mask:0xf bank_mask:0xf\n"
    "v_fmac_f32_dpp v109, v73, v85 row_newbcast:15 row_mask:0xf bank_mask:0xf\n"
    "v_fmac_f32_dpp v110, v74, v85 row_newbcast:15 row_mask:0xf bank_mask:0xf\n"
    "v_fmac_f32_dpp v111, v75, v85 row_newbcast:15 row_mask:0xf bank_mask:0xf\n"
    "v_fma_f32 v112, -v104, v104, -s21\n"
    "v_fma_f32 v113, -v105, v105, -s21\n"
    "v_fma_f32 v114, -v106, v106, -s21\n"
    "v_fma_f32 v115, -v107, v107, -s21\n"
    "v_fma_f32 v116, -v108, v108, -s21\n"
    "v_fma_f32 v117, -v109, v109, -s21\n"
    "v_fma_f32 v118, -v110, v110, -s21\n"
    "v_fma_f32 v119, -v111, v111, -s21\n"
    "v_mul_f32 v112, v104, v112\n"
    "v_mul_f32 v113, v105, v113\n"
    "v_mul_f32 v114, v106, v114\n"
    "v_mul_f32 v115, v107, v115\n"
    "v_mul_f32 v116, v108, v116\n"
    "v_mul_f32 v117, v109, v117\n"
    "v_mul_f32 v118, v110, v118\n"
    "v_mul_f32 v119, v111, v119\n"
    "v_exp_f32 v112, v112\n"
    "v_exp_f32 v113, v113\n"
    "v_exp_f32 v114, v114\n"
    "v_exp_f32 v115, v115\n"
    "v_exp_f32 v116, v116\n"
    "v_exp_f32 v117, v117\n"
    "v_exp_f32 v118, v118\n"
    "v_exp_f32 v119, v119\n"
    "v_fma_f32 v112, v112, s20, s20\n"
    "v_fma_f32 v113, v113, s20, s20\n"
    "v_fma_f32 v114, v114, s20, s20\n"
    "v_fma_f32 v115, v115, s20, s20\n"
    "v_fma_f32 v116, v116, s20, s20\n"
    "v_fma_f32 v117, v117, s20, s20\n"
    "v_fma_f32 v118, v118, s20, s20\n"
    "s_barrier\n"
    "v_fma_f32 v119, v119, s20, s20\n"
    "v_rcp_f32 v112, v112\n"
    "v_rcp_f32 v113, v113\n"
    "v_rcp_f32 v114, v114\n"
    "v_rcp_f32 v115, v115\n"
    "v_rcp_f32 v116, v116\n"
    "v_rcp_f32 v117, v117\n"
    "v_rcp_f32 v118, v118\n"
    "v_rcp_f32 v119, v119\n"
    "v_mul_f32 v104, v104, v112\n"
    "v_mul_f32 v105, v105, v113\n"
    "v_mul_f32 v106, v106, v114\n"
    "v_mul_f32 v107, v107, v115\n"
    "v_mul_f32 v108, v108, v116\n"
    "v_mul_f32 v109, v109, v117\n"
    "v_mul_f32 v110, v110, v118\n"
    "v_mul_f32 v111, v111, v119\n"
    "v_cvt_pk_bf16_f32 v4, v104, v105\n"
    "v_cvt_pk_bf16_f32 v5, v106, v107\n"
    "v_cvt_pk_bf16_f32 v6, v108, v109\n"
    "v_cvt_pk_bf16_f32 v7, v110, v111\n"
    "v_fma_f32 v104, v86, v20, v140\n"
    "v_fma_f32 v105, v86, v21, v141\n"
    "v_fma_f32 v106, v86, v22, v142\n"
    "v_fma_f32 v107, v86, v23, v143\n"
    "v_fma_f32 v108, v86, v24, v144\n"
    "v_fma_f32 v109, v86, v25, v145\n"
    "v_fma_f32 v110, v86, v26, v146\n"
    "v_fma_f32 v111, v86, v27, v147\n"
    "v_fmac_f32_dpp v104, v68, v87 row_newbcast:15 row_mask:0xf bank_mask:0xf\n"
    "v_fmac_f32_dpp v105, v69, v87 row_newbcast:15 row_mask:0xf bank_mask:0xf\n"
    "v_fmac_f32_dpp v106, v70, v87 row_newbcast:15 row_mask:0xf bank_mask:0xf\n"
    "v_fmac_f32_dpp v107, v71, v87 row_newbcast:15 row_mask:0xf bank_mask:0xf\n"
    "v_fmac_f32_dpp v108, v72, v87 row_newbcast:15 row_mask:0xf bank_mask:0xf\n"
    "v_fmac_f32_dpp v109, v73, v87 row_newbcast:15 row_mask:0xf bank_mask:0xf\n"
    "v_fmac_f32_dpp v110, v74, v87 row_newbcast:15 row_mask:0xf bank_mask:0xf\n"
    "v_fmac_f32_dpp v111, v75, v87 row_newbcast:15 row_mask:0xf bank_mask:0xf\n"
    "v_fma_f32 v112, -v104, v104, -s21\n"
    "v_fma_f32 v113, -v105, v105, -s21\n"
    "v_fma_f32 v114, -v106, v106, -s21\n"
    "v_fma_f32 v115, -v107, v107, -s21\n"
    "v_fma_f32 v116, -v108, v108, -s21\n"
    "v_fma_f32 v117, -v109, v109, -s21\n"
    "v_fma_f32 v118, -v110, v110, -s21\n"
    "v_fma_f32 v119, -v111, v111, -s21\n"
    "v_mul_f32 v112, v104, v112\n"
    "s_barrier\n"
    "v_mul_f32 v113, v105, v113\n"
    "v_mul_f32 v114, v106, v114\n"
    "v_mul_f32 v115, v107, v115\n"
    "v_mul_f32 v116, v108, v116\n"
    "v_mul_f32 v117, v109, v117\n"
    "v_mul_f32 v118, v110, v118\n"
    "v_mul_f32 v119, v111, v119\n"
    "v_exp_f32 v112, v112\n"
    "v_exp_f32 v113, v113\n"
    "v_exp_f32 v114, v114\n"
    "v_exp_f32 v115, v115\n"
    "v_exp_f32 v116, v116\n"
    "v_exp_f32 v117, v117\n"
    "v_exp_f32 v118, v118\n"
    "v_exp_f32 v119, v119\n"
    "v_fma_f32 v112, v112, s20, s20\n"
    "v_fma_f32 v113, v113, s20, s20\n"
    "v_fma_f32 v114, v114, s20, s20\n"
    "v_fma_f32 v115, v115, s20, s20\n"
    "v_fma_f32 v116, v116, s20, s20\n"
    "v_fma_f32 v117, v117, s20, s20\n"
    "v_fma_f32 v118, v118, s20, s20\n"
    "v_fma_f32 v119, v119, s20, s20\n"
    "v_rcp_f32 v112, v112\n"
    "v_rcp_f32 v113, v113\n"
    "v_rcp_f32 v114, v114\n"
    "v_rcp_f32 v115, v115\n"
    "v_rcp_f32 v116, v116\n"
    "v_rcp_f32 v117, v117\n"
    "v_rcp_f32 v118, v118\n"
    "v_rcp_f32 v119, v119\n"
    "v_mul_f32 v104, v104, v112\n"
    "v_mul_f32 v105, v105, v113\n"
    "v_mul_f32 v106, v106, v114\n"
    "v_mul_f32 v107, v107, v115\n"
    "v_mul_f32 v108, v108, v116\n"
    "v_mul_f32 v109, v109, v117\n"
    "v_mul_f32 v110, v110, v118\n"
    "s_barrier\n"
    "v_mul_f32 v111, v111, v119\n"
    "v_cvt_pk_bf16_f32 v20, v104, v105\n"
    "v_cvt_pk_bf16_f32 v21, v106, v107\n"
    "v_cvt_pk_bf16_f32 v22, v108, v109\n"
    "v_cvt_pk_bf16_f32 v23, v110, v111\n"
    "v_fma_f32 v104, v88, v36, v140\n"
    "v_fma_f32 v105, v88, v37, v141\n"
    "v_fma_f32 v106, v88, v38, v142\n"
    "v_fma_f32 v107, v88, v39, v143\n"
    "v_fma_f32 v108, v88, v40, v144\n"
    "v_fma_f32 v109, v88, v41, v145\n"
    "v_fma_f32 v110, v88, v42, v146\n"
    "v_fma_f32 v111, v88, v43, v147\n"
    "v_fmac_f32_dpp v104, v68, v89 row_newbcast:15 row_mask:0xf bank_mask:0xf\n"
    "v_fmac_f32_dpp v105, v69, v89 row_newbcast:15 row_mask:0xf bank_mask:0xf\n"
    "v_fmac_f32_dpp v106, v70, v89 row_newbcast:15 row_mask:0xf bank_mask:0xf\n"
    "v_fmac_f32_dpp v107, v71, v89 row_newbcast:15 row_mask:0xf bank_mask:0xf\n"
    "v_fmac_f32_dpp v108, v72, v89 row_newbcast:15 row_mask:0xf bank_mask:0xf\n"
    "v_fmac_f32_dpp v109, v73, v89 row_newbcast:15 row_mask:0xf bank_mask:0xf\n"
    "v_fmac_f32_dpp v110, v74, v89 row_newbcast:15 row_mask:0xf bank_mask:0xf\n"
    "v_fmac_f32_dpp v111, v75, v89 row_newbcast:15 row_mask:0xf bank_mask:0xf\n"
    "v_fma_f32 v112, -v104, v104, -s21\n"
    "v_fma_f32 v113, -v105, v105, -s21\n"
    "v_fma_f32 v114, -v106, v106, -s21\n"
    "v_fma_f32 v115, -v107, v107, -s21\n"
    "v_fma_f32 v116, -v108, v108, -s21\n"
    "v_fma_f32 v117, -v109, v109, -s21\n"
    "v_fma_f32 v118, -v110, v110, -s21\n"
    "v_fma_f32 v119, -v111, v111, -s21\n"
    "v_mul_f32 v112, v104, v112\n"
    "v_mul_f32 v113, v105, v113\n"
    "v_mul_f32 v114, v106, v114\n"
    "v_mul_f32 v115, v107, v115\n"
    "v_mul_f32 v116, v108, v116\n"
    "v_mul_f32 v117, v109, v117\n"
    "v_mul_f32 v118, v110, v118\n"
    "v_mul_f32 v119, v111, v119\n"
    "v_exp_f32 v112, v112\n"
    "v_exp_f32 v113, v113\n"
    "v_exp_f32 v114, v114\n"
    "v_exp_f32 v115, v115\n"
    "v_exp_f32 v116, v116\n"
    "v_exp_f32 v117, v117\n"
    "v_exp_f32 v118, v118\n"
    "v_exp_f32 v119, v119\n"
    "v_fma_f32 v112, v112, s20, s20\n"
    "s_barrier\n"
    "v_fma_f32 v113, v113, s20, s20\n"
    "v_fma_f32 v114, v114, s20, s20\n"
    "v_fma_f32 v115, v115, s20, s20\n"
    "v_fma_f32 v116, v116, s20, s20\n"
    "v_fma_f32 v117, v117, s20, s20\n"
    "v_fma_f32 v118, v118, s20, s20\n"
    "v_fma_f32 v119, v119, s20, s20\n"
    "v_rcp_f32 v112, v112\n"
    "v_rcp_f32 v113, v113\n"
    "v_rcp_f32 v114, v114\n"
    "v_rcp_f32 v115, v115\n"
    "v_rcp_f32 v116, v116\n"
    "v_rcp_f32 v117, v117\n"
    "v_rcp_f32 v118, v118\n"
    "v_rcp_f32 v119, v119\n"
    "v_mul_f32 v104, v104, v112\n"
    "v_mul_f32 v105, v105, v113\n"
    "v_mul_f32 v106, v106, v114\n"
    "v_mul_f32 v107, v107, v115\n"
    "v_mul_f32 v108, v108, v116\n"
    "v_mul_f32 v109, v109, v117\n"
    "v_mul_f32 v110, v110, v118\n"
    "v_mul_f32 v111, v111, v119\n"
    "v_cvt_pk_bf16_f32 v36, v104, v105\n"
    "v_cvt_pk_bf16_f32 v37, v106, v107\n"
    "v_cvt_pk_bf16_f32 v38, v108, v109\n"
    "v_cvt_pk_bf16_f32 v39, v110, v111\n"
    "v_fma_f32 v104, v90, v52, v140\n"
    "v_fma_f32 v105, v90, v53, v141\n"
    "v_fma_f32 v106, v90, v54, v142\n"
    "v_fma_f32 v107, v90, v55, v143\n"
    "v_fma_f32 v108, v90, v56, v144\n"
    "v_fma_f32 v109, v90, v57, v145\n"
    "v_fma_f32 v110, v90, v58, v146\n"
    "v_fma_f32 v111, v90, v59, v147\n"
    "v_fmac_f32_dpp v104, v68, v91 row_newbcast:15 row_mask:0xf bank_mask:0xf\n"
    "v_fmac_f32_dpp v105, v69, v91 row_newbcast:15 row_mask:0xf bank_mask:0xf\n"
    "v_fmac_f32_dpp v106, v70, v91 row_newbcast:15 row_mask:0xf bank_mask:0xf\n"
    "v_fmac_f32_dpp v107, v71, v91 row_newbcast:15 row_mask:0xf bank_mask:0xf\n"
    "v_fmac_f32_dpp v108, v72, v91 row_newbcast:15 row_mask:0xf bank_mask:0xf\n"
    "v_fmac_f32_dpp v109, v73, v91 row_newbcast:15 row_mask:0xf bank_mask:0xf\n"
    "v_fmac_f32_dpp v110, v74, v91 row_newbcast:15 row_mask:0xf bank_mask:0xf\n"
    "v_fmac_f32_dpp v111, v75, v91 row_newbcast:15 row_mask:0xf bank_mask:0xf\n"
    "v_fma_f32 v112, -v104, v104, -s21\n"
    "v_fma_f32 v113, -v105, v105, -s21\n"
    "v_fma_f32 v114, -v106, v106, -s21\n"
    "s_barrier\n"
    "v_fma_f32 v115, -v107, v107, -s21\n"
    "v_fma_f32 v116, -v108, v108, -s21\n"
    "v_fma_f32 v117, -v109, v109, -s21\n"
    "v_fma_f32 v118, -v110, v110, -s21\n"
    "v_fma_f32 v119, -v111, v111, -s21\n"
    "v_mul_f32 v112, v104, v112\n"
    "v_mul_f32 v113, v105, v113\n"
    "v_mul_f32 v114, v106, v114\n"
    "v_mul_f32 v115, v107, v115\n"
    "v_mul_f32 v116, v108, v116\n"
    "v_mul_f32 v117, v109, v117\n"
    "v_mul_f32 v118, v110, v118\n"
    "v_mul_f32 v119, v111, v119\n"
    "v_exp_f32 v112, v112\n"
    "v_exp_f32 v113, v113\n"
    "v_exp_f32 v114, v114\n"
    "v_exp_f32 v115, v115\n"
    "v_exp_f32 v116, v116\n"
    "v_exp_f32 v117, v117\n"
    "v_exp_f32 v118, v118\n"
    "v_exp_f32 v119, v119\n"
    "v_fma_f32 v112, v112, s20, s20\n"
    "v_fma_f32 v113, v113, s20, s20\n"
    "ds_write_b128 v1, v[4:7] offset:0\n"
    "v_fma_f32 v114, v114, s20, s20\n"
    ".Lpage7:\n"
    "s_cbranch_execz .Ltouch_next\n"
    "v_fma_f32 v115, v115, s20, s20\n"
    "v_fma_f32 v116, v116, s20, s20\n"
    "v_fma_f32 v117, v117, s20, s20\n"
    "v_fma_f32 v118, v118, s20, s20\n"
    "v_fma_f32 v119, v119, s20, s20\n"
    "v_rcp_f32 v112, v112\n"
    "v_rcp_f32 v113, v113\n"
    "v_rcp_f32 v114, v114\n"
    "v_rcp_f32 v115, v115\n"
    "v_rcp_f32 v116, v116\n"
    "v_rcp_f32 v117, v117\n"
    "v_rcp_f32 v118, v118\n"
    "v_rcp_f32 v119, v119\n"
    "v_mul_f32 v104, v104, v112\n"
    "s_barrier\n"
    "v_mul_f32 v105, v105, v113\n"
    "v_mul_f32 v106, v106, v114\n"
    "v_mul_f32 v107, v107, v115\n"
    "v_mul_f32 v108, v108, v116\n"
    "v_mul_f32 v109, v109, v117\n"
    "v_mul_f32 v110, v110, v118\n"
    "v_mul_f32 v111, v111, v119\n"
    "v_cvt_pk_bf16_f32 v52, v104, v105\n"
    "v_cvt_pk_bf16_f32 v53, v106, v107\n"
    "v_cvt_pk_bf16_f32 v54, v108, v109\n"
    "v_cvt_pk_bf16_f32 v55, v110, v111\n"
    "v_fma_f32 v104, v92, v68, v140\n"
    "v_fma_f32 v105, v92, v69, v141\n"
    "v_fma_f32 v106, v92, v70, v142\n"
    "v_fma_f32 v107, v92, v71, v143\n"
    "v_fma_f32 v108, v92, v72, v144\n"
    "v_fma_f32 v109, v92, v73, v145\n"
    "v_fma_f32 v110, v92, v74, v146\n"
    "v_fma_f32 v111, v92, v75, v147\n"
    "v_fmac_f32_dpp v104, v68, v93 row_newbcast:15 row_mask:0xf bank_mask:0xf\n"
    "v_fmac_f32_dpp v105, v69, v93 row_newbcast:15 row_mask:0xf bank_mask:0xf\n"
    "v_fmac_f32_dpp v106, v70, v93 row_newbcast:15 row_mask:0xf bank_mask:0xf\n"
    "v_fmac_f32_dpp v107, v71, v93 row_newbcast:15 row_mask:0xf bank_mask:0xf\n"
    "v_fmac_f32_dpp v108, v72, v93 row_newbcast:15 row_mask:0xf bank_mask:0xf\n"
    "v_fmac_f32_dpp v109, v73, v93 row_newbcast:15 row_mask:0xf bank_mask:0xf\n"
    "v_fmac_f32_dpp v110, v74, v93 row_newbcast:15 row_mask:0xf bank_mask:0xf\n"
    "v_fmac_f32_dpp v111, v75, v93 row_newbcast:15 row_mask:0xf bank_mask:0xf\n"
    "ds_write_b128 v1, v[20:23] offset:1024\n"
    "v_fma_f32 v112, -v104, v104, -s21\n"
    "v_fma_f32 v113, -v105, v105, -s21\n"
    "v_fma_f32 v114, -v106, v106, -s21\n"
    "v_fma_f32 v115, -v107, v107, -s21\n"
    "v_fma_f32 v116, -v108, v108, -s21\n"
    "v_fma_f32 v117, -v109, v109, -s21\n"
    "v_fma_f32 v118, -v110, v110, -s21\n"
    "v_fma_f32 v119, -v111, v111, -s21\n"
    "v_mul_f32 v112, v104, v112\n"
    "v_mul_f32 v113, v105, v113\n"
    "v_mul_f32 v114, v106, v114\n"
    "v_mul_f32 v115, v107, v115\n"
    "v_mul_f32 v116, v108, v116\n"
    "v_mul_f32 v117, v109, v117\n"
    "v_mul_f32 v118, v110, v118\n"
    "v_mul_f32 v119, v111, v119\n"
    "v_exp_f32 v112, v112\n"
    "v_exp_f32 v113, v113\n"
    "v_exp_f32 v114, v114\n"
    "v_exp_f32 v115, v115\n"
    "v_exp_f32 v116, v116\n"
    "s_barrier\n"
    "v_exp_f32 v117, v117\n"
    "v_exp_f32 v118, v118\n"
    "v_exp_f32 v119, v119\n"
    "v_fma_f32 v112, v112, s20, s20\n"
    "v_fma_f32 v113, v113, s20, s20\n"
    "v_fma_f32 v114, v114, s20, s20\n"
    "v_fma_f32 v115, v115, s20, s20\n"
    "v_fma_f32 v116, v116, s20, s20\n"
    "v_fma_f32 v117, v117, s20, s20\n"
    "v_fma_f32 v118, v118, s20, s20\n"
    "v_fma_f32 v119, v119, s20, s20\n"
    "v_rcp_f32 v112, v112\n"
    "v_rcp_f32 v113, v113\n"
    "v_rcp_f32 v114, v114\n"
    "v_rcp_f32 v115, v115\n"
    "v_rcp_f32 v116, v116\n"
    "v_rcp_f32 v117, v117\n"
    "v_rcp_f32 v118, v118\n"
    "v_rcp_f32 v119, v119\n"
    "v_mul_f32 v104, v104, v112\n"
    "v_mul_f32 v105, v105, v113\n"
    "v_mul_f32 v106, v106, v114\n"
    "v_mul_f32 v107, v107, v115\n"
    "v_mul_f32 v108, v108, v116\n"
    "ds_write_b128 v1, v[36:39] offset:2048\n"
    "v_mul_f32 v109, v109, v117\n"
    "v_mul_f32 v110, v110, v118\n"
    "v_mul_f32 v111, v111, v119\n"
    "v_cvt_pk_bf16_f32 v68, v104, v105\n"
    "v_cvt_pk_bf16_f32 v69, v106, v107\n"
    "v_cvt_pk_bf16_f32 v70, v108, v109\n"
    "v_cvt_pk_bf16_f32 v71, v110, v111\n"
    "v_fma_f32 v104, v84, v12, v148\n"
    "v_fma_f32 v105, v84, v13, v149\n"
    "v_fma_f32 v106, v84, v14, v150\n"
    "v_fma_f32 v107, v84, v15, v151\n"
    "v_fma_f32 v108, v84, v16, v152\n"
    "v_fma_f32 v109, v84, v17, v153\n"
    "v_fma_f32 v110, v84, v18, v154\n"
    "v_fma_f32 v111, v84, v19, v155\n"
    "v_fmac_f32_dpp v104, v76, v85 row_newbcast:15 row_mask:0xf bank_mask:0xf\n"
    "v_fmac_f32_dpp v105, v77, v85 row_newbcast:15 row_mask:0xf bank_mask:0xf\n"
    "v_fmac_f32_dpp v106, v78, v85 row_newbcast:15 row_mask:0xf bank_mask:0xf\n"
    "v_fmac_f32_dpp v107, v79, v85 row_newbcast:15 row_mask:0xf bank_mask:0xf\n"
    "v_fmac_f32_dpp v108, v80, v85 row_newbcast:15 row_mask:0xf bank_mask:0xf\n"
    "s_barrier\n"
    "v_fmac_f32_dpp v109, v81, v85 row_newbcast:15 row_mask:0xf bank_mask:0xf\n"
    "v_fmac_f32_dpp v110, v82, v85 row_newbcast:15 row_mask:0xf bank_mask:0xf\n"
    "v_fmac_f32_dpp v111, v83, v85 row_newbcast:15 row_mask:0xf bank_mask:0xf\n"
    "v_fma_f32 v112, -v104, v104, -s21\n"
    "v_fma_f32 v113, -v105, v105, -s21\n"
    "v_fma_f32 v114, -v106, v106, -s21\n"
    "v_fma_f32 v115, -v107, v107, -s21\n"
    "v_fma_f32 v116, -v108, v108, -s21\n"
    "v_fma_f32 v117, -v109, v109, -s21\n"
    "v_fma_f32 v118, -v110, v110, -s21\n"
    "v_fma_f32 v119, -v111, v111, -s21\n"
    "v_mul_f32 v112, v104, v112\n"
    "v_mul_f32 v113, v105, v113\n"
    "v_mul_f32 v114, v106, v114\n"
    "v_mul_f32 v115, v107, v115\n"
    "v_mul_f32 v116, v108, v116\n"
    "v_mul_f32 v117, v109, v117\n"
    "v_mul_f32 v118, v110, v118\n"
    "v_mul_f32 v119, v111, v119\n"
    "v_exp_f32 v112, v112\n"
    "v_exp_f32 v113, v113\n"
    "v_exp_f32 v114, v114\n"
    "ds_write_b128 v1, v[52:55] offset:3072\n"
    "v_exp_f32 v115, v115\n"
    "v_exp_f32 v116, v116\n"
    "v_exp_f32 v117, v117\n"
    "v_exp_f32 v118, v118\n"
    "v_exp_f32 v119, v119\n"
    "v_fma_f32 v112, v112, s20, s20\n"
    "v_fma_f32 v113, v113, s20, s20\n"
    "v_fma_f32 v114, v114, s20, s20\n"
    "v_fma_f32 v115, v115, s20, s20\n"
    "v_fma_f32 v116, v116, s20, s20\n"
    "v_fma_f32 v117, v117, s20, s20\n"
    "v_fma_f32 v118, v118, s20, s20\n"
    "v_fma_f32 v119, v119, s20, s20\n"
    "v_rcp_f32 v112, v112\n"
    "v_rcp_f32 v113, v113\n"
    "v_rcp_f32 v114, v114\n"
    "v_rcp_f32 v115, v115\n"
    "v_rcp_f32 v116, v116\n"
    "s_barrier\n"
    "v_rcp_f32 v117, v117\n"
    "v_rcp_f32 v118, v118\n"
    "v_rcp_f32 v119, v119\n"
    "v_mul_f32 v104, v104, v112\n"
    "v_mul_f32 v105, v105, v113\n"
    "v_mul_f32 v106, v106, v114\n"
    "v_mul_f32 v107, v107, v115\n"
    "v_mul_f32 v108, v108, v116\n"
    "v_mul_f32 v109, v109, v117\n"
    "v_mul_f32 v110, v110, v118\n"
    "v_mul_f32 v111, v111, v119\n"
    "v_cvt_pk_bf16_f32 v12, v104, v105\n"
    "v_cvt_pk_bf16_f32 v13, v106, v107\n"
    "v_cvt_pk_bf16_f32 v14, v108, v109\n"
    "v_cvt_pk_bf16_f32 v15, v110, v111\n"
    "v_fma_f32 v104, v86, v28, v148\n"
    "v_fma_f32 v105, v86, v29, v149\n"
    "v_fma_f32 v106, v86, v30, v150\n"
    "v_fma_f32 v107, v86, v31, v151\n"
    "v_fma_f32 v108, v86, v32, v152\n"
    "v_fma_f32 v109, v86, v33, v153\n"
    "v_fma_f32 v110, v86, v34, v154\n"
    "v_fma_f32 v111, v86, v35, v155\n"
    "v_fmac_f32_dpp v104, v76, v87 row_newbcast:15 row_mask:0xf bank_mask:0xf\n"
    "v_fmac_f32_dpp v105, v77, v87 row_newbcast:15 row_mask:0xf bank_mask:0xf\n"
    "v_fmac_f32_dpp v106, v78, v87 row_newbcast:15 row_mask:0xf bank_mask:0xf\n"
    "v_fmac_f32_dpp v107, v79, v87 row_newbcast:15 row_mask:0xf bank_mask:0xf\n"
    "v_fmac_f32_dpp v108, v80, v87 row_newbcast:15 row_mask:0xf bank_mask:0xf\n"
    "v_fmac_f32_dpp v109, v81, v87 row_newbcast:15 row_mask:0xf bank_mask:0xf\n"
    "v_fmac_f32_dpp v110, v82, v87 row_newbcast:15 row_mask:0xf bank_mask:0xf\n"
    "v_fmac_f32_dpp v111, v83, v87 row_newbcast:15 row_mask:0xf bank_mask:0xf\n"
    "v_fma_f32 v112, -v104, v104, -s21\n"
    "ds_write_b128 v1, v[68:71] offset:4096\n"
    "v_fma_f32 v113, -v105, v105, -s21\n"
    "v_fma_f32 v114, -v106, v106, -s21\n"
    "v_fma_f32 v115, -v107, v107, -s21\n"
    "v_fma_f32 v116, -v108, v108, -s21\n"
    "v_fma_f32 v117, -v109, v109, -s21\n"
    "v_fma_f32 v118, -v110, v110, -s21\n"
    "v_fma_f32 v119, -v111, v111, -s21\n"
    "v_mul_f32 v112, v104, v112\n"
    "v_mul_f32 v113, v105, v113\n"
    "v_mul_f32 v114, v106, v114\n"
    "v_mul_f32 v115, v107, v115\n"
    "v_mul_f32 v116, v108, v116\n"
    "v_mul_f32 v117, v109, v117\n"
    "v_mul_f32 v118, v110, v118\n"
    "v_mul_f32 v119, v111, v119\n"
    "v_exp_f32 v112, v112\n"
    "v_exp_f32 v113, v113\n"
    "s_barrier\n"
    "v_exp_f32 v114, v114\n"
    "v_exp_f32 v115, v115\n"
    "v_exp_f32 v116, v116\n"
    "v_exp_f32 v117, v117\n"
    "v_exp_f32 v118, v118\n"
    "v_exp_f32 v119, v119\n"
    "v_fma_f32 v112, v112, s20, s20\n"
    "v_fma_f32 v113, v113, s20, s20\n"
    "v_fma_f32 v114, v114, s20, s20\n"
    "v_fma_f32 v115, v115, s20, s20\n"
    "v_fma_f32 v116, v116, s20, s20\n"
    "v_fma_f32 v117, v117, s20, s20\n"
    "v_fma_f32 v118, v118, s20, s20\n"
    "v_fma_f32 v119, v119, s20, s20\n"
    "v_rcp_f32 v112, v112\n"
    "v_rcp_f32 v113, v113\n"
    "v_rcp_f32 v114, v114\n"
    "ds_write_b128 v1, v[12:15] offset:20480\n"
    "v_rcp_f32 v115, v115\n"
    "v_rcp_f32 v116, v116\n"
    "v_rcp_f32 v117, v117\n"
    "v_rcp_f32 v118, v118\n"
    "v_rcp_f32 v119, v119\n"
    "v_mul_f32 v104, v104, v112\n"
    "v_mul_f32 v105, v105, v113\n"
    "v_mul_f32 v106, v106, v114\n"
    "v_mul_f32 v107, v107, v115\n"
    "v_mul_f32 v108, v108, v116\n"
    "v_mul_f32 v109, v109, v117\n"
    "v_mul_f32 v110, v110, v118\n"
    "v_mul_f32 v111, v111, v119\n"
    "v_cvt_pk_bf16_f32 v28, v104, v105\n"
    "v_cvt_pk_bf16_f32 v29, v106, v107\n"
    "v_cvt_pk_bf16_f32 v30, v108, v109\n"
    "v_cvt_pk_bf16_f32 v31, v110, v111\n"
    "v_fma_f32 v104, v88, v44, v148\n"
    "v_fma_f32 v105, v88, v45, v149\n"
    "v_fma_f32 v106, v88, v46, v150\n"
    "v_fma_f32 v107, v88, v47, v151\n"
    "v_fma_f32 v108, v88, v48, v152\n"
    "v_fma_f32 v109, v88, v49, v153\n"
    "v_fma_f32 v110, v88, v50, v154\n"
    "s_waitcnt lgkmcnt(1)\n"
    "s_barrier\n"
    "v_fma_f32 v111, v88, v51, v155\n"
    "v_fmac_f32_dpp v104, v76, v89 row_newbcast:15 row_mask:0xf bank_mask:0xf\n"
    "v_fmac_f32_dpp v105, v77, v89 row_newbcast:15 row_mask:0xf bank_mask:0xf\n"
    "v_fmac_f32_dpp v106, v78, v89 row_newbcast:15 row_mask:0xf bank_mask:0xf\n"
    "v_fmac_f32_dpp v107, v79, v89 row_newbcast:15 row_mask:0xf bank_mask:0xf\n"
    "v_fmac_f32_dpp v108, v80, v89 row_newbcast:15 row_mask:0xf bank_mask:0xf\n"
    "v_fmac_f32_dpp v109, v81, v89 row_newbcast:15 row_mask:0xf bank_mask:0xf\n"
    "v_fmac_f32_dpp v110, v82, v89 row_newbcast:15 row_mask:0xf bank_mask:0xf\n"
    "v_fmac_f32_dpp v111, v83, v89 row_newbcast:15 row_mask:0xf bank_mask:0xf\n"
    "v_fma_f32 v112, -v104, v104, -s21\n"
    "v_fma_f32 v113, -v105, v105, -s21\n"
    "v_fma_f32 v114, -v106, v106, -s21\n"
    "v_fma_f32 v115, -v107, v107, -s21\n"
    "v_fma_f32 v116, -v108, v108, -s21\n"
    "v_fma_f32 v117, -v109, v109, -s21\n"
    "v_fma_f32 v118, -v110, v110, -s21\n"
    "v_fma_f32 v119, -v111, v111, -s21\n"
    "v_mul_f32 v112, v104, v112\n"
    "v_mul_f32 v113, v105, v113\n"
    "v_mul_f32 v114, v106, v114\n"
    "v_mul_f32 v115, v107, v115\n"
    "v_mul_f32 v116, v108, v116\n"
    "v_mul_f32 v117, v109, v117\n"
    "v_mul_f32 v118, v110, v118\n"
    "v_mul_f32 v119, v111, v119\n"
    "v_exp_f32 v112, v112\n"
    "v_exp_f32 v113, v113\n"
    "v_exp_f32 v114, v114\n"
    "ds_write_b128 v1, v[28:31] offset:21504\n"
    "v_exp_f32 v115, v115\n"
    "v_exp_f32 v116, v116\n"
    "v_exp_f32 v117, v117\n"
    "v_exp_f32 v118, v118\n"
    "v_exp_f32 v119, v119\n"
    "v_fma_f32 v112, v112, s20, s20\n"
    "v_fma_f32 v113, v113, s20, s20\n"
    "v_fma_f32 v114, v114, s20, s20\n"
    "v_fma_f32 v115, v115, s20, s20\n"
    "v_fma_f32 v116, v116, s20, s20\n"
    "v_fma_f32 v117, v117, s20, s20\n"
    "v_fma_f32 v118, v118, s20, s20\n"
    "v_fma_f32 v119, v119, s20, s20\n"
    "v_rcp_f32 v112, v112\n"
    "v_rcp_f32 v113, v113\n"
    "s_barrier\n"
    "v_rcp_f32 v114, v114\n"
    "v_rcp_f32 v115, v115\n"
    "v_rcp_f32 v116, v116\n"
    "v_rcp_f32 v117, v117\n"
    "v_rcp_f32 v118, v118\n"
    "v_rcp_f32 v119, v119\n"
    "v_mul_f32 v104, v104, v112\n"
    "v_mul_f32 v105, v105, v113\n"
    "v_mul_f32 v106, v106, v114\n"
    "v_mul_f32 v107, v107, v115\n"
    "v_mul_f32 v108, v108, v116\n"
    "v_mul_f32 v109, v109, v117\n"
    "v_mul_f32 v110, v110, v118\n"
    "v_mul_f32 v111, v111, v119\n"
    "v_cvt_pk_bf16_f32 v44, v104, v105\n"
    "v_cvt_pk_bf16_f32 v45, v106, v107\n"
    "v_cvt_pk_bf16_f32 v46, v108, v109\n"
    "v_cvt_pk_bf16_f32 v47, v110, v111\n"
    "v_fma_f32 v104, v90, v60, v148\n"
    "v_fma_f32 v105, v90, v61, v149\n"
    "v_fma_f32 v106, v90, v62, v150\n"
    "ds_write_b128 v1, v[44:47] offset:22528\n"
    "v_fma_f32 v107, v90, v63, v151\n"
    "v_fma_f32 v108, v90, v64, v152\n"
    "v_fma_f32 v109, v90, v65, v153\n"
    "v_fma_f32 v110, v90, v66, v154\n"
    "v_fma_f32 v111, v90, v67, v155\n"
    "v_fmac_f32_dpp v104, v76, v91 row_newbcast:15 row_mask:0xf bank_mask:0xf\n"
    "v_fmac_f32_dpp v105, v77, v91 row_newbcast:15 row_mask:0xf bank_mask:0xf\n"
    "v_fmac_f32_dpp v106, v78, v91 row_newbcast:15 row_mask:0xf bank_mask:0xf\n"
    "v_fmac_f32_dpp v107, v79, v91 row_newbcast:15 row_mask:0xf bank_mask:0xf\n"
    "v_fmac_f32_dpp v108, v80, v91 row_newbcast:15 row_mask:0xf bank_mask:0xf\n"
    "v_fmac_f32_dpp v109, v81, v91 row_newbcast:15 row_mask:0xf bank_mask:0xf\n"
    "v_fmac_f32_dpp v110, v82, v91 row_newbcast:15 row_mask:0xf bank_mask:0xf\n"
    "v_fmac_f32_dpp v111, v83, v91 row_newbcast:15 row_mask:0xf bank_mask:0xf\n"
    "v_fma_f32 v112, -v104, v104, -s21\n"
    "v_fma_f32 v113, -v105, v105, -s21\n"
    "v_fma_f32 v114, -v106, v106, -s21\n"
    "v_fma_f32 v115, -v107, v107, -s21\n"
    "v_fma_f32 v116, -v108, v108, -s21\n"
    "v_fma_f32 v117, -v109, v109, -s21\n"
    "v_fma_f32 v118, -v110, v110, -s21\n"
    "v_fma_f32 v119, -v111, v111, -s21\n"
    "v_mul_f32 v112, v104, v112\n"
    "v_mul_f32 v113, v105, v113\n"
    "v_mul_f32 v114, v106, v114\n"
    "v_mul_f32 v115, v107, v115\n"
    "v_mul_f32 v116, v108, v116\n"
    "v_mul_f32 v117, v109, v117\n"
    "v_mul_f32 v118, v110, v118\n"
    "s_barrier\n"
    "v_mul_f32 v119, v111, v119\n"
    "v_exp_f32 v112, v112\n"
    "v_exp_f32 v113, v113\n"
    "v_exp_f32 v114, v114\n"
    "v_exp_f32 v115, v115\n"
    "v_exp_f32 v116, v116\n"
    "v_exp_f32 v117, v117\n"
    "v_exp_f32 v118, v118\n"
    "v_exp_f32 v119, v119\n"
    "v_fma_f32 v112, v112, s20, s20\n"
    "v_fma_f32 v113, v113, s20, s20\n"
    "v_fma_f32 v114, v114, s20, s20\n"
    "v_fma_f32 v115, v115, s20, s20\n"
    "v_fma_f32 v116, v116, s20, s20\n"
    "v_fma_f32 v117, v117, s20, s20\n"
    "v_fma_f32 v118, v118, s20, s20\n"
    "v_fma_f32 v119, v119, s20, s20\n"
    "v_rcp_f32 v112, v112\n"
    "v_rcp_f32 v113, v113\n"
    "v_rcp_f32 v114, v114\n"
    "v_rcp_f32 v115, v115\n"
    "v_rcp_f32 v116, v116\n"
    "v_rcp_f32 v117, v117\n"
    "v_rcp_f32 v118, v118\n"
    "v_rcp_f32 v119, v119\n"
    "v_mul_f32 v104, v104, v112\n"
    "v_mul_f32 v105, v105, v113\n"
    "v_mul_f32 v106, v106, v114\n"
    "v_mul_f32 v107, v107, v115\n"
    "v_mul_f32 v108, v108, v116\n"
    "v_mul_f32 v109, v109, v117\n"
    "v_mul_f32 v110, v110, v118\n"
    "v_mul_f32 v111, v111, v119\n"
    "v_cvt_pk_bf16_f32 v60, v104, v105\n"
    "v_cvt_pk_bf16_f32 v61, v106, v107\n"
    "v_cvt_pk_bf16_f32 v62, v108, v109\n"
    "v_cvt_pk_bf16_f32 v63, v110, v111\n"
    "v_fma_f32 v104, v92, v76, v148\n"
    "s_barrier\n"
    "v_fma_f32 v105, v92, v77, v149\n"
    "v_fma_f32 v106, v92, v78, v150\n"
    "v_fma_f32 v107, v92, v79, v151\n"
    "v_fma_f32 v108, v92, v80, v152\n"
    "v_fma_f32 v109, v92, v81, v153\n"
    "v_fma_f32 v110, v92, v82, v154\n"
    "v_fma_f32 v111, v92, v83, v155\n"
    "v_fmac_f32_dpp v104, v76, v93 row_newbcast:15 row_mask:0xf bank_mask:0xf\n"
    "v_fmac_f32_dpp v105, v77, v93 row_newbcast:15 row_mask:0xf bank_mask:0xf\n"
    "v_fmac_f32_dpp v106, v78, v93 row_newbcast:15 row_mask:0xf bank_mask:0xf\n"
    "v_fmac_f32_dpp v107, v79, v93 row_newbcast:15 row_mask:0xf bank_mask:0xf\n"
    "v_fmac_f32_dpp v108, v80, v93 row_newbcast:15 row_mask:0xf bank_mask:0xf\n"
    "v_fmac_f32_dpp v109, v81, v93 row_newbcast:15 row_mask:0xf bank_mask:0xf\n"
    "v_fmac_f32_dpp v110, v82, v93 row_newbcast:15 row_mask:0xf bank_mask:0xf\n"
    "v_fmac_f32_dpp v111, v83, v93 row_newbcast:15 row_mask:0xf bank_mask:0xf\n"
    "v_fma_f32 v112, -v104, v104, -s21\n"
    "v_fma_f32 v113, -v105, v105, -s21\n"
    "v_fma_f32 v114, -v106, v106, -s21\n"
    "v_fma_f32 v115, -v107, v107, -s21\n"
    "v_fma_f32 v116, -v108, v108, -s21\n"
    "v_fma_f32 v117, -v109, v109, -s21\n"
    "v_fma_f32 v118, -v110, v110, -s21\n"
    "v_fma_f32 v119, -v111, v111, -s21\n"
    "v_mul_f32 v112, v104, v112\n"
    "v_mul_f32 v113, v105, v113\n"
    "v_mul_f32 v114, v106, v114\n"
    "v_mul_f32 v115, v107, v115\n"
    "ds_write_b128 v1, v[60:63] offset:23552\n"
    "v_mul_f32 v116, v108, v116\n"
    "v_mul_f32 v117, v109, v117\n"
    "v_mul_f32 v118, v110, v118\n"
    "v_mul_f32 v119, v111, v119\n"
    "v_exp_f32 v112, v112\n"
    "v_exp_f32 v113, v113\n"
    "v_exp_f32 v114, v114\n"
    "v_exp_f32 v115, v115\n"
    "v_exp_f32 v116, v116\n"
    "v_exp_f32 v117, v117\n"
    "v_exp_f32 v118, v118\n"
    "v_exp_f32 v119, v119\n"
    "v_fma_f32 v112, v112, s20, s20\n"
    "v_fma_f32 v113, v113, s20, s20\n"
    "v_fma_f32 v114, v114, s20, s20\n"
    "v_fma_f32 v115, v115, s20, s20\n"
    "v_fma_f32 v116, v116, s20, s20\n"
    "v_fma_f32 v117, v117, s20, s20\n"
    "v_fma_f32 v118, v118, s20, s20\n"
    "s_waitcnt lgkmcnt(1)\n"
    "s_barrier\n"
    "v_fma_f32 v119, v119, s20, s20\n"
    "v_rcp_f32 v112, v112\n"
    "v_rcp_f32 v113, v113\n"
    "v_rcp_f32 v114, v114\n"
    "v_rcp_f32 v115, v115\n"
    "v_rcp_f32 v116, v116\n"
    "v_rcp_f32 v117, v117\n"
    "v_rcp_f32 v118, v118\n"
    "v_rcp_f32 v119, v119\n"
    "v_mul_f32 v104, v104, v112\n"
    "v_mul_f32 v105, v105, v113\n"
    "v_mul_f32 v106, v106, v114\n"
    "v_mul_f32 v107, v107, v115\n"
    "v_mul_f32 v108, v108, v116\n"
    "v_mul_f32 v109, v109, v117\n"
    "v_mul_f32 v110, v110, v118\n"
    "v_mul_f32 v111, v111, v119\n"
    "v_cvt_pk_bf16_f32 v76, v104, v105\n"
    "v_cvt_pk_bf16_f32 v77, v106, v107\n"
    "v_cvt_pk_bf16_f32 v78, v108, v109\n"
    "v_cvt_pk_bf16_f32 v79, v110, v111\n"
    "s_nop 3\n"
    "ds_write_b128 v1, v[76:79] offset:24576\n"
    "s_waitcnt lgkmcnt(0)\n"
    "s_barrier\n"
    "v_lshrrev_b32 v74, 2, v0\n"
    "v_and_b32 v75, 3, v0\n"
    "v_lshrrev_b32 v76, 2, v74\n"
    "v_mul_u32_u24 v76, 20, v76\n"
    "v_and_b32 v77, 3, v74\n"
    "v_add_u32 v76, v76, v77\n"
    "v_mul_u32_u24 v76, 2304, v76\n"
    "v_lshl_add_u32 v70, v75, 4, v76\n"
    "s_mul_i32 s25, s10, 79\n"
    "s_lshl_b32 s31, s14, 2\n"
    "s_add_u32 s25, s25, s31\n"
    "s_mul_i32 s25, s25, 2304\n"
    "s_lshl_b32 s26, s11, 8\n"
    "s_lshl_b32 s31, s15, 7\n"
    "s_add_u32 s26, s26, s31\n"
    "s_add_u32 s25, s25, s26\n"
    "s_mul_i32 s31, s12, 6144\n"
    "s_barrier\n"
    "v_lshlrev_b32 v76, 2, v75\n"
    "v_xor_b32 v76, v76, v74\n"
    "v_lshl_add_u32 v76, v75, 4, v76\n"
    "v_lshl_add_u32 v71, v76, 4, s31\n"
    "v_lshrrev_b32 v76, 4, v0\n"
    "v_lshlrev_b32 v76, 2, v76\n"
    "v_xor_b32 v76, v76, v0\n"
    "v_lshl_add_u32 v69, v76, 4, s31\n"
    "s_mul_i32 s31, s15, 10240\n"
    "s_lshl_b32 s95, s14, 11\n"
    "s_add_u32 s31, s31, s95\n"
    "s_add_u32 s31, s31, 112640\n"
    "s_barrier\n"
    "v_lshl_add_u32 v72, v0, 4, s31\n"
    "v_lshlrev_b32 v73, 8, v0\n"
    "s_mov_b32 s88, s62\n"
    "s_and_b32 s89, s63, 0xffff\n"
    "s_mov_b32 s90, 4718592\n"
    "s_mov_b32 s91, 0x20000\n"
    "s_mul_i32 s27, s10, 9\n"
    "s_add_u32 s27, s27, s11\n"
    "s_mul_i32 s92, s27, 81920\n"
    "s_lshl_b32 s30, s12, 10\n"
    "s_add_u32 s92, s92, s30\n"
    "s_add_u32 s92, s92, 1048576\n"
    "s_add_u32 s93, s92, 4096\n"
    "s_add_u32 s94, s92, 8192\n"
    "s_add_u32 s26, s25, 36864\n"
    "s_cmp_eq_u32 s14, 0\n"
    "s_cselect_b32 s94, s94, 0x40000000\n"
    "s_cselect_b32 s26, s26, 0x40000000\n"
    "s_lshl_b32 s27, s27, 11\n"
    "s_lshl_b32 s30, s15, 7\n"
    "s_add_u32 s27, s27, s30\n"
    "s_add_u32 s27, s27, 524296\n"
    "s_lshl_b32 s30, s12, 11\n"
    "s_add_u32 s30, s30, 45056\n"
    "v_add_u32 v158, s30, v156\n"
    "v_add_u32 v159, s30, v157\n"
    "s_add_u32 s29, s11, s14\n"
    "s_add_u32 s29, s29, 5\n"
    "s_sub_u32 s30, s29, 9\n"
    "s_cmp_ge_u32 s29, 9\n"
    "s_cselect_b32 s29, s30, s29\n"
    "s_mul_i32 s28, s10, 9\n"
    "s_add_u32 s28, s28, s29\n"
    "s_add_u32 s29, s14, 4\n"
    "s_lshl_b32 s23, s28, 11\n"
    "s_lshl_b32 s30, s29, 8\n"
    "s_add_u32 s23, s23, s30\n"
    "s_lshl_b32 s30, s15, 7\n"
    "s_add_u32 s23, s23, s30\n"
    "s_add_u32 s23, s23, 524288\n"
    "s_lshl_b32 s28, s28, 3\n"
    "s_add_u32 s28, s28, s29\n"
    "s_mul_i32 s28, s28, 10\n"
    "s_add_u32 s28, s28, s15\n"
    "s_lshl_b32 s28, s28, 10\n"
    "s_add_u32 s22, s28, 1048576\n"
    "v_lshlrev_b32 v156, 4, v0\n"
    "v_add_u32 v157, 51200, v156\n"
    "ds_read_b128 v[84:87], v156 offset:0\n"
    "ds_read_b128 v[88:91], v156 offset:5120\n"
    "ds_read_b128 v[92:95], v156 offset:1024\n"
    "ds_read_b128 v[96:99], v156 offset:6144\n"
    "ds_read_b128 v[100:103], v156 offset:2048\n"
    "ds_read_b128 v[104:107], v156 offset:7168\n"
    "s_barrier\n"
    "s_setprio 2\n"
    "ds_read_b64_tr_b16 v[140:141], v160 offset:57344\n"
    "ds_read_b64_tr_b16 v[142:143], v160 offset:8192\n"
    "ds_read_b64_tr_b16 v[144:145], v160 offset:59392\n"
    "ds_read_b64_tr_b16 v[146:147], v160 offset:10240\n"
    "ds_read_b64_tr_b16 v[152:153], v161 offset:59392\n"
    "ds_read_b64_tr_b16 v[154:155], v161 offset:10240\n"
    "ds_read_b64_tr_b16 v[148:149], v161 offset:57344\n"
    "ds_read_b64_tr_b16 v[150:151], v161 offset:8192\n"
    "ds_read_b128 v[108:111], v156 offset:3072\n"
    "ds_read_b128 v[112:115], v156 offset:8192\n"
    "ds_read_b128 v[116:119], v156 offset:4096\n"
    "ds_read_b128 v[120:123], v156 offset:9216\n"
    "s_waitcnt lgkmcnt(4)\n"
    "s_barrier\n"
    "v_mfma_f32_16x16x32_bf16 v[4:7], v[140:143], v[84:87], 0\n"
    "ds_read_b64_tr_b16 v[124:125], v160 offset:16384\n"
    "v_mfma_f32_16x16x32_bf16 v[4:7], v[144:147], v[88:91], v[4:7]\n"
    "ds_read_b64_tr_b16 v[126:127], v160 offset:24576\n"
    "v_mfma_f32_16x16x32_bf16 v[8:11], v[152:155], v[88:91], 0\n"
    "ds_read_b64_tr_b16 v[128:129], v160 offset:18432\n"
    "v_mfma_f32_16x16x32_bf16 v[8:11], v[148:151], v[84:87], v[8:11]\n"
    "ds_read_b64_tr_b16 v[130:131], v160 offset:26624\n"
    "v_mfma_f32_16x16x32_bf16 v[16:19], v[148:151], v[92:95], 0\n"
    "ds_read_b64_tr_b16 v[136:137], v161 offset:18432\n"
    "v_mfma_f32_16x16x32_bf16 v[16:19], v[152:155], v[96:99], v[16:19]\n"
    "ds_read_b64_tr_b16 v[138:139], v161 offset:26624\n"
    "v_mfma_f32_16x16x32_bf16 v[12:15], v[144:147], v[96:99], 0\n"
    "ds_read_b64_tr_b16 v[132:133], v161 offset:16384\n"
    "v_mfma_f32_16x16x32_bf16 v[12:15], v[140:143], v[92:95], v[12:15]\n"
    "ds_read_b128 v[88:91], v156 offset:15360\n"
    "v_mfma_f32_16x16x32_bf16 v[20:23], v[140:143], v[100:103], 0\n"
    "ds_read_b128 v[84:87], v156 offset:10240\n"
    "v_mfma_f32_16x16x32_bf16 v[20:23], v[144:147], v[104:107], v[20:23]\n"
    "ds_read_b64_tr_b16 v[134:135], v161 offset:24576\n"
    "v_mfma_f32_16x16x32_bf16 v[24:27], v[152:155], v[104:107], 0\n"
    "ds_read_b128 v[96:99], v156 offset:16384\n"
    "v_mfma_f32_16x16x32_bf16 v[24:27], v[148:151], v[100:103], v[24:27]\n"
    "ds_read_b128 v[92:95], v156 offset:11264\n"
    "s_waitcnt lgkmcnt(14)\n"
    "v_mfma_f32_16x16x32_bf16 v[32:35], v[148:151], v[108:111], 0\n"
    "ds_read_b128 v[104:107], v156 offset:17408\n"
    "v_mfma_f32_16x16x32_bf16 v[32:35], v[152:155], v[112:115], v[32:35]\n"
    "ds_read_b128 v[100:103], v156 offset:12288\n"
    "v_mfma_f32_16x16x32_bf16 v[28:31], v[144:147], v[112:115], 0\n"
    "ds_read_b128 v[112:115], v156 offset:18432\n"
    "v_mfma_f32_16x16x32_bf16 v[28:31], v[140:143], v[108:111], v[28:31]\n"
    "ds_read_b128 v[108:111], v156 offset:13312\n"
    "s_waitcnt lgkmcnt(15)\n"
    "v_mfma_f32_16x16x32_bf16 v[36:39], v[140:143], v[116:119], 0\n"
    "v_mfma_f32_16x16x32_bf16 v[36:39], v[144:147], v[120:123], v[36:39]\n"
    "v_mfma_f32_16x16x32_bf16 v[40:43], v[152:155], v[120:123], 0\n"
    "ds_read_b128 v[120:123], v156 offset:19456\n"
    "s_waitcnt lgkmcnt(7)\n"
    "v_mfma_f32_16x16x32_bf16 v[40:43], v[148:151], v[116:119], v[40:43]\n"
    "s_barrier\n"
    "v_mfma_f32_16x16x32_bf16 v[4:7], v[124:127], v[84:87], v[4:7]\n"
    "ds_read_b64_tr_b16 v[140:141], v160 offset:32768\n"
    "v_mfma_f32_16x16x32_bf16 v[4:7], v[128:131], v[88:91], v[4:7]\n"
    "ds_read_b64_tr_b16 v[142:143], v160 offset:40960\n"
    "v_mfma_f32_16x16x32_bf16 v[8:11], v[136:139], v[88:91], v[8:11]\n"
    "ds_read_b128 v[116:119], v156 offset:14336\n"
    "v_mfma_f32_16x16x32_bf16 v[8:11], v[132:135], v[84:87], v[8:11]\n"
    "ds_read_b64_tr_b16 v[144:145], v160 offset:34816\n"
    "s_waitcnt lgkmcnt(9)\n"
    "v_mfma_f32_16x16x32_bf16 v[16:19], v[132:135], v[92:95], v[16:19]\n"
    "ds_read_b64_tr_b16 v[146:147], v160 offset:43008\n"
    "v_mfma_f32_16x16x32_bf16 v[16:19], v[136:139], v[96:99], v[16:19]\n"
    "ds_read_b64_tr_b16 v[152:153], v161 offset:34816\n"
    "v_mfma_f32_16x16x32_bf16 v[12:15], v[128:131], v[96:99], v[12:15]\n"
    "ds_read_b64_tr_b16 v[154:155], v161 offset:43008\n"
    "v_mfma_f32_16x16x32_bf16 v[12:15], v[124:127], v[92:95], v[12:15]\n"
    "ds_read_b64_tr_b16 v[148:149], v161 offset:32768\n"
    "s_waitcnt lgkmcnt(11)\n"
    "v_mfma_f32_16x16x32_bf16 v[20:23], v[124:127], v[100:103], v[20:23]\n"
    "ds_read_b128 v[88:91], v156 offset:25600\n"
    "v_mfma_f32_16x16x32_bf16 v[20:23], v[128:131], v[104:107], v[20:23]\n"
    "ds_read_b128 v[84:87], v156 offset:20480\n"
    "v_mfma_f32_16x16x32_bf16 v[24:27], v[136:139], v[104:107], v[24:27]\n"
    "ds_read_b64_tr_b16 v[150:151], v161 offset:40960\n"
    "v_mfma_f32_16x16x32_bf16 v[24:27], v[132:135], v[100:103], v[24:27]\n"
    "ds_read_b128 v[96:99], v156 offset:26624\n"
    "s_waitcnt lgkmcnt(13)\n"
    "v_mfma_f32_16x16x32_bf16 v[32:35], v[132:135], v[108:111], v[32:35]\n"
    "ds_read_b128 v[92:95], v156 offset:21504\n"
    "v_mfma_f32_16x16x32_bf16 v[32:35], v[136:139], v[112:115], v[32:35]\n"
    "ds_read_b128 v[104:107], v156 offset:27648\n"
    "v_mfma_f32_16x16x32_bf16 v[28:31], v[128:131], v[112:115], v[28:31]\n"
    "ds_read_b128 v[100:103], v156 offset:22528\n"
    "v_mfma_f32_16x16x32_bf16 v[28:31], v[124:127], v[108:111], v[28:31]\n"
    "ds_read_b128 v[112:115], v156 offset:28672\n"
    "s_waitcnt lgkmcnt(13)\n"
    "v_mfma_f32_16x16x32_bf16 v[36:39], v[124:127], v[116:119], v[36:39]\n"
    "ds_read_b128 v[108:111], v156 offset:23552\n"
    "v_mfma_f32_16x16x32_bf16 v[36:39], v[128:131], v[120:123], v[36:39]\n"
    "v_mfma_f32_16x16x32_bf16 v[40:43], v[136:139], v[120:123], v[40:43]\n"
    "ds_read_b128 v[120:123], v156 offset:29696\n"
    "s_waitcnt lgkmcnt(7)\n"
    "v_mfma_f32_16x16x32_bf16 v[40:43], v[132:135], v[116:119], v[40:43]\n"
    "s_barrier\n"
    "v_mfma_f32_16x16x32_bf16 v[4:7], v[140:143], v[84:87], v[4:7]\n"
    "ds_read_b64_tr_b16 v[124:125], v160 offset:49152\n"
    "v_mfma_f32_16x16x32_bf16 v[4:7], v[144:147], v[88:91], v[4:7]\n"
    "ds_read_b64_tr_b16 v[126:127], v160 offset:57344\n"
    "v_mfma_f32_16x16x32_bf16 v[8:11], v[152:155], v[88:91], v[8:11]\n"
    "ds_read_b128 v[116:119], v156 offset:24576\n"
    "v_mfma_f32_16x16x32_bf16 v[8:11], v[148:151], v[84:87], v[8:11]\n"
    "ds_read_b64_tr_b16 v[128:129], v160 offset:51200\n"
    "s_waitcnt lgkmcnt(9)\n"
    "v_mfma_f32_16x16x32_bf16 v[16:19], v[148:151], v[92:95], v[16:19]\n"
    "ds_read_b64_tr_b16 v[130:131], v160 offset:59392\n"
    "v_mfma_f32_16x16x32_bf16 v[16:19], v[152:155], v[96:99], v[16:19]\n"
    "ds_read_b64_tr_b16 v[136:137], v161 offset:51200\n"
    "v_mfma_f32_16x16x32_bf16 v[12:15], v[144:147], v[96:99], v[12:15]\n"
    "ds_read_b64_tr_b16 v[138:139], v161 offset:59392\n"
    ".Lpage8:\n"
    "s_cbranch_execz .Ltouch_next\n"
    "v_mfma_f32_16x16x32_bf16 v[12:15], v[140:143], v[92:95], v[12:15]\n"
    "ds_read_b64_tr_b16 v[132:133], v161 offset:49152\n"
    "s_waitcnt lgkmcnt(11)\n"
    "v_mfma_f32_16x16x32_bf16 v[20:23], v[140:143], v[100:103], v[20:23]\n"
    "ds_read_b128 v[88:91], v156 offset:35840\n"
    "v_mfma_f32_16x16x32_bf16 v[20:23], v[144:147], v[104:107], v[20:23]\n"
    "ds_read_b128 v[84:87], v156 offset:30720\n"
    "v_mfma_f32_16x16x32_bf16 v[24:27], v[152:155], v[104:107], v[24:27]\n"
    "ds_read_b64_tr_b16 v[134:135], v161 offset:57344\n"
    "v_mfma_f32_16x16x32_bf16 v[24:27], v[148:151], v[100:103], v[24:27]\n"
    "ds_read_b128 v[96:99], v156 offset:36864\n"
    "s_waitcnt lgkmcnt(13)\n"
    "v_mfma_f32_16x16x32_bf16 v[32:35], v[148:151], v[108:111], v[32:35]\n"
    "ds_read_b128 v[92:95], v156 offset:31744\n"
    "v_mfma_f32_16x16x32_bf16 v[32:35], v[152:155], v[112:115], v[32:35]\n"
    "ds_read_b128 v[104:107], v156 offset:37888\n"
    "v_mfma_f32_16x16x32_bf16 v[28:31], v[144:147], v[112:115], v[28:31]\n"
    "ds_read_b128 v[100:103], v156 offset:32768\n"
    "v_mfma_f32_16x16x32_bf16 v[28:31], v[140:143], v[108:111], v[28:31]\n"
    "ds_read_b128 v[112:115], v156 offset:38912\n"
    "s_waitcnt lgkmcnt(13)\n"
    "v_mfma_f32_16x16x32_bf16 v[36:39], v[140:143], v[116:119], v[36:39]\n"
    "ds_read_b128 v[108:111], v156 offset:33792\n"
    "v_mfma_f32_16x16x32_bf16 v[36:39], v[144:147], v[120:123], v[36:39]\n"
    "v_mfma_f32_16x16x32_bf16 v[40:43], v[152:155], v[120:123], v[40:43]\n"
    "ds_read_b128 v[120:123], v156 offset:39936\n"
    "s_waitcnt lgkmcnt(7)\n"
    "v_mfma_f32_16x16x32_bf16 v[40:43], v[148:151], v[116:119], v[40:43]\n"
    "s_barrier\n"
    "v_mfma_f32_16x16x32_bf16 v[4:7], v[124:127], v[84:87], v[4:7]\n"
    "ds_read_b128 v[140:143], v158 offset:8192\n"
    "v_mfma_f32_16x16x32_bf16 v[4:7], v[128:131], v[88:91], v[4:7]\n"
    "ds_read_b128 v[144:147], v159 offset:8192\n"
    "v_mfma_f32_16x16x32_bf16 v[8:11], v[136:139], v[88:91], v[8:11]\n"
    "ds_read_b128 v[116:119], v156 offset:34816\n"
    "v_mfma_f32_16x16x32_bf16 v[8:11], v[132:135], v[84:87], v[8:11]\n"
    "ds_read_b128 v[152:155], v159 offset:16384\n"
    "s_waitcnt lgkmcnt(9)\n"
    "v_mfma_f32_16x16x32_bf16 v[16:19], v[132:135], v[92:95], v[16:19]\n"
    "ds_read_b128 v[148:151], v158 offset:16384\n"
    "v_mfma_f32_16x16x32_bf16 v[16:19], v[136:139], v[96:99], v[16:19]\n"
    "ds_read_b64_tr_b16 v[88:89], v162 offset:5120\n"
    "v_mfma_f32_16x16x32_bf16 v[12:15], v[128:131], v[96:99], v[12:15]\n"
    "ds_read_b64_tr_b16 v[90:91], v163 offset:5120\n"
    "v_mfma_f32_16x16x32_bf16 v[12:15], v[124:127], v[92:95], v[12:15]\n"
    "ds_read_b64_tr_b16 v[84:85], v162 offset:0\n"
    "s_waitcnt lgkmcnt(11)\n"
    "v_mfma_f32_16x16x32_bf16 v[20:23], v[124:127], v[100:103], v[20:23]\n"
    "ds_read_b64_tr_b16 v[86:87], v163 offset:0\n"
    "v_mfma_f32_16x16x32_bf16 v[20:23], v[128:131], v[104:107], v[20:23]\n"
    "ds_read_b64_tr_b16 v[96:97], v162 offset:6144\n"
    "v_mfma_f32_16x16x32_bf16 v[24:27], v[136:139], v[104:107], v[24:27]\n"
    "ds_read_b64_tr_b16 v[98:99], v163 offset:6144\n"
    "v_mfma_f32_16x16x32_bf16 v[24:27], v[132:135], v[100:103], v[24:27]\n"
    "ds_read_b64_tr_b16 v[92:93], v162 offset:1024\n"
    "s_waitcnt lgkmcnt(13)\n"
    "v_mfma_f32_16x16x32_bf16 v[32:35], v[132:135], v[108:111], v[32:35]\n"
    "ds_read_b64_tr_b16 v[94:95], v163 offset:1024\n"
    "v_mfma_f32_16x16x32_bf16 v[32:35], v[136:139], v[112:115], v[32:35]\n"
    "ds_read_b64_tr_b16 v[104:105], v162 offset:7168\n"
    "v_mfma_f32_16x16x32_bf16 v[28:31], v[128:131], v[112:115], v[28:31]\n"
    "ds_read_b64_tr_b16 v[106:107], v163 offset:7168\n"
    "v_mfma_f32_16x16x32_bf16 v[28:31], v[124:127], v[108:111], v[28:31]\n"
    "ds_read_b64_tr_b16 v[100:101], v162 offset:2048\n"
    "s_waitcnt lgkmcnt(13)\n"
    "v_mfma_f32_16x16x32_bf16 v[36:39], v[124:127], v[116:119], v[36:39]\n"
    "ds_read_b64_tr_b16 v[102:103], v163 offset:2048\n"
    "v_mfma_f32_16x16x32_bf16 v[36:39], v[128:131], v[120:123], v[36:39]\n"
    "ds_read_b64_tr_b16 v[112:113], v162 offset:8192\n"
    "v_mfma_f32_16x16x32_bf16 v[40:43], v[136:139], v[120:123], v[40:43]\n"
    "ds_read_b64_tr_b16 v[114:115], v163 offset:8192\n"
    "s_waitcnt lgkmcnt(10)\n"
    "v_mfma_f32_16x16x32_bf16 v[40:43], v[132:135], v[116:119], v[40:43]\n"
    "s_barrier\n"
    "v_mfma_f32_16x16x32_bf16 v[4:7], v[140:143], v[84:87], v[4:7]\n"
    "ds_read_b64_tr_b16 v[108:109], v162 offset:3072\n"
    "v_mfma_f32_16x16x32_bf16 v[4:7], v[144:147], v[88:91], v[4:7]\n"
    "ds_read_b64_tr_b16 v[110:111], v163 offset:3072\n"
    "v_mfma_f32_16x16x32_bf16 v[8:11], v[152:155], v[88:91], v[8:11]\n"
    "ds_read_b128 v[124:127], v158 offset:24576\n"
    "v_mfma_f32_16x16x32_bf16 v[8:11], v[148:151], v[84:87], v[8:11]\n"
    "ds_read_b128 v[128:131], v159 offset:24576\n"
    "s_waitcnt lgkmcnt(10)\n"
    "v_mfma_f32_16x16x32_bf16 v[16:19], v[148:151], v[92:95], v[16:19]\n"
    "ds_read_b64_tr_b16 v[120:121], v162 offset:9216\n"
    "v_mfma_f32_16x16x32_bf16 v[16:19], v[152:155], v[96:99], v[16:19]\n"
    "ds_read_b64_tr_b16 v[122:123], v163 offset:9216\n"
    "v_mfma_f32_16x16x32_bf16 v[12:15], v[144:147], v[96:99], v[12:15]\n"
    "ds_read_b64_tr_b16 v[116:117], v162 offset:4096\n"
    "v_mfma_f32_16x16x32_bf16 v[12:15], v[140:143], v[92:95], v[12:15]\n"
    "ds_read_b64_tr_b16 v[118:119], v163 offset:4096\n"
    "s_waitcnt lgkmcnt(10)\n"
    "v_mfma_f32_16x16x32_bf16 v[20:23], v[140:143], v[100:103], v[20:23]\n"
    "ds_read_b128 v[136:139], v159 offset:32768\n"
    "v_mfma_f32_16x16x32_bf16 v[20:23], v[144:147], v[104:107], v[20:23]\n"
    "ds_read_b128 v[132:135], v158 offset:32768\n"
    "v_mfma_f32_16x16x32_bf16 v[24:27], v[152:155], v[104:107], v[24:27]\n"
    "ds_read_b64_tr_b16 v[88:89], v162 offset:15360\n"
    "v_mfma_f32_16x16x32_bf16 v[24:27], v[148:151], v[100:103], v[24:27]\n"
    "ds_read_b64_tr_b16 v[90:91], v163 offset:15360\n"
    "s_waitcnt lgkmcnt(10)\n"
    "v_mfma_f32_16x16x32_bf16 v[32:35], v[148:151], v[108:111], v[32:35]\n"
    "ds_read_b64_tr_b16 v[84:85], v162 offset:10240\n"
    "v_mfma_f32_16x16x32_bf16 v[32:35], v[152:155], v[112:115], v[32:35]\n"
    "ds_read_b64_tr_b16 v[86:87], v163 offset:10240\n"
    "v_mfma_f32_16x16x32_bf16 v[28:31], v[144:147], v[112:115], v[28:31]\n"
    "ds_read_b64_tr_b16 v[96:97], v162 offset:16384\n"
    "v_mfma_f32_16x16x32_bf16 v[28:31], v[140:143], v[108:111], v[28:31]\n"
    "ds_read_b64_tr_b16 v[98:99], v163 offset:16384\n"
    "s_waitcnt lgkmcnt(8)\n"
    "v_mfma_f32_16x16x32_bf16 v[36:39], v[140:143], v[116:119], v[36:39]\n"
    "ds_read_b64_tr_b16 v[92:93], v162 offset:11264\n"
    "v_mfma_f32_16x16x32_bf16 v[36:39], v[144:147], v[120:123], v[36:39]\n"
    "ds_read_b64_tr_b16 v[94:95], v163 offset:11264\n"
    "v_mfma_f32_16x16x32_bf16 v[40:43], v[152:155], v[120:123], v[40:43]\n"
    "ds_read_b64_tr_b16 v[104:105], v162 offset:17408\n"
    "s_waitcnt lgkmcnt(5)\n"
    "v_mfma_f32_16x16x32_bf16 v[40:43], v[148:151], v[116:119], v[40:43]\n"
    "s_barrier\n"
    "v_mfma_f32_16x16x32_bf16 v[4:7], v[124:127], v[84:87], v[4:7]\n"
    "ds_read_b64_tr_b16 v[106:107], v163 offset:17408\n"
    "v_mfma_f32_16x16x32_bf16 v[4:7], v[128:131], v[88:91], v[4:7]\n"
    "ds_read_b64_tr_b16 v[100:101], v162 offset:12288\n"
    "ds_read_b64_tr_b16 v[102:103], v163 offset:12288\n"
    "v_mfma_f32_16x16x32_bf16 v[8:11], v[136:139], v[88:91], v[8:11]\n"
    "ds_read_b64_tr_b16 v[112:113], v162 offset:18432\n"
    "v_mfma_f32_16x16x32_bf16 v[8:11], v[132:135], v[84:87], v[8:11]\n"
    "ds_read_b64_tr_b16 v[114:115], v163 offset:18432\n"
    "s_waitcnt lgkmcnt(6)\n"
    "v_mfma_f32_16x16x32_bf16 v[16:19], v[132:135], v[92:95], v[16:19]\n"
    "ds_read_b64_tr_b16 v[108:109], v162 offset:13312\n"
    "v_mfma_f32_16x16x32_bf16 v[16:19], v[136:139], v[96:99], v[16:19]\n"
    "ds_read_b64_tr_b16 v[110:111], v163 offset:13312\n"
    "v_mfma_f32_16x16x32_bf16 v[12:15], v[128:131], v[96:99], v[12:15]\n"
    "ds_read_b64_tr_b16 v[140:141], v160 offset:40960\n"
    "v_mfma_f32_16x16x32_bf16 v[12:15], v[124:127], v[92:95], v[12:15]\n"
    "ds_read_b64_tr_b16 v[142:143], v160 offset:49152\n"
    "s_waitcnt lgkmcnt(6)\n"
    "v_mfma_f32_16x16x32_bf16 v[20:23], v[124:127], v[100:103], v[20:23]\n"
    "ds_read_b64_tr_b16 v[120:121], v162 offset:19456\n"
    "v_mfma_f32_16x16x32_bf16 v[20:23], v[128:131], v[104:107], v[20:23]\n"
    "ds_read_b64_tr_b16 v[122:123], v163 offset:19456\n"
    "ds_read_b64_tr_b16 v[116:117], v162 offset:14336\n"
    "v_mfma_f32_16x16x32_bf16 v[24:27], v[136:139], v[104:107], v[24:27]\n"
    "ds_read_b64_tr_b16 v[118:119], v163 offset:14336\n"
    "ds_read_b64_tr_b16 v[144:145], v160 offset:43008\n"
    "v_mfma_f32_16x16x32_bf16 v[24:27], v[132:135], v[100:103], v[24:27]\n"
    "ds_read_b64_tr_b16 v[146:147], v160 offset:51200\n"
    "ds_read_b64_tr_b16 v[152:153], v161 offset:43008\n"
    "s_waitcnt lgkmcnt(9)\n"
    "v_mfma_f32_16x16x32_bf16 v[32:35], v[132:135], v[108:111], v[32:35]\n"
    "ds_read_b64_tr_b16 v[154:155], v161 offset:51200\n"
    "v_mfma_f32_16x16x32_bf16 v[32:35], v[136:139], v[112:115], v[32:35]\n"
    "ds_read_b64_tr_b16 v[148:149], v161 offset:40960\n"
    "ds_read_b128 v[88:91], v156 offset:46080\n"
    "v_mfma_f32_16x16x32_bf16 v[28:31], v[128:131], v[112:115], v[28:31]\n"
    "ds_read_b128 v[84:87], v156 offset:40960\n"
    "ds_read_b64_tr_b16 v[150:151], v161 offset:49152\n"
    "v_mfma_f32_16x16x32_bf16 v[28:31], v[124:127], v[108:111], v[28:31]\n"
    "ds_read_b128 v[96:99], v156 offset:47104\n"
    "s_waitcnt lgkmcnt(9)\n"
    "v_mfma_f32_16x16x32_bf16 v[36:39], v[124:127], v[116:119], v[36:39]\n"
    "ds_read_b128 v[92:95], v156 offset:41984\n"
    "v_mfma_f32_16x16x32_bf16 v[36:39], v[128:131], v[120:123], v[36:39]\n"
    "ds_read_b128 v[104:107], v156 offset:48128\n"
    "v_mfma_f32_16x16x32_bf16 v[40:43], v[136:139], v[120:123], v[40:43]\n"
    "ds_read_b128 v[100:103], v156 offset:43008\n"
    "s_waitcnt lgkmcnt(4)\n"
    "v_mfma_f32_16x16x32_bf16 v[40:43], v[132:135], v[116:119], v[40:43]\n"
    "s_barrier\n"
    "v_mfma_f32_16x16x32_bf16 v[4:7], v[140:143], v[84:87], v[4:7]\n"
    "ds_read_b128 v[112:115], v156 offset:49152\n"
    "v_mfma_f32_16x16x32_bf16 v[4:7], v[144:147], v[88:91], v[4:7]\n"
    "ds_read_b128 v[108:111], v156 offset:44032\n"
    "v_mfma_f32_16x16x32_bf16 v[8:11], v[152:155], v[88:91], v[8:11]\n"
    "ds_read_b64_tr_b16 v[124:125], v160 offset:57344\n"
    "v_mfma_f32_16x16x32_bf16 v[8:11], v[148:151], v[84:87], v[8:11]\n"
    "ds_read_b64_tr_b16 v[126:127], v160 offset:8192\n"
    "s_waitcnt lgkmcnt(6)\n"
    "v_mfma_f32_16x16x32_bf16 v[16:19], v[148:151], v[92:95], v[16:19]\n"
    "ds_read_b128 v[120:123], v156 offset:50176\n"
    "v_mfma_f32_16x16x32_bf16 v[16:19], v[152:155], v[96:99], v[16:19]\n"
    "ds_read_b128 v[116:119], v156 offset:45056\n"
    "v_mfma_f32_16x16x32_bf16 v[12:15], v[144:147], v[96:99], v[12:15]\n"
    "ds_read_b64_tr_b16 v[128:129], v160 offset:59392\n"
    "v_mfma_f32_16x16x32_bf16 v[12:15], v[140:143], v[92:95], v[12:15]\n"
    "ds_read_b64_tr_b16 v[130:131], v160 offset:10240\n"
    "s_waitcnt lgkmcnt(8)\n"
    "v_mfma_f32_16x16x32_bf16 v[20:23], v[140:143], v[100:103], v[20:23]\n"
    "ds_read_b64_tr_b16 v[136:137], v161 offset:59392\n"
    "v_mfma_f32_16x16x32_bf16 v[20:23], v[144:147], v[104:107], v[20:23]\n"
    "ds_read_b64_tr_b16 v[138:139], v161 offset:10240\n"
    "v_mfma_f32_16x16x32_bf16 v[24:27], v[152:155], v[104:107], v[24:27]\n"
    "ds_read_b64_tr_b16 v[132:133], v161 offset:57344\n"
    "v_mfma_f32_16x16x32_bf16 v[24:27], v[148:151], v[100:103], v[24:27]\n"
    "ds_read_b128 v[88:91], v157 offset:5120\n"
    "s_waitcnt lgkmcnt(10)\n"
    "v_mfma_f32_16x16x32_bf16 v[32:35], v[148:151], v[108:111], v[32:35]\n"
    "ds_read_b128 v[84:87], v157 offset:0\n"
    "v_mfma_f32_16x16x32_bf16 v[32:35], v[152:155], v[112:115], v[32:35]\n"
    "ds_read_b64_tr_b16 v[134:135], v161 offset:8192\n"
    "v_mfma_f32_16x16x32_bf16 v[28:31], v[144:147], v[112:115], v[28:31]\n"
    "ds_read_b128 v[96:99], v157 offset:6144\n"
    "v_mfma_f32_16x16x32_bf16 v[28:31], v[140:143], v[108:111], v[28:31]\n"
    "ds_read_b128 v[92:95], v157 offset:1024\n"
    "s_waitcnt lgkmcnt(10)\n"
    "v_mfma_f32_16x16x32_bf16 v[36:39], v[140:143], v[116:119], v[36:39]\n"
    "ds_read_b128 v[104:107], v157 offset:7168\n"
    "v_mfma_f32_16x16x32_bf16 v[36:39], v[144:147], v[120:123], v[36:39]\n"
    "ds_read_b128 v[100:103], v157 offset:2048\n"
    "v_mfma_f32_16x16x32_bf16 v[40:43], v[152:155], v[120:123], v[40:43]\n"
    "ds_read_b128 v[112:115], v157 offset:8192\n"
    "s_waitcnt lgkmcnt(5)\n"
    "v_mfma_f32_16x16x32_bf16 v[40:43], v[148:151], v[116:119], v[40:43]\n"
    "s_barrier\n"
    "v_mfma_f32_16x16x32_bf16 v[4:7], v[124:127], v[84:87], v[4:7]\n"
    "ds_read_b128 v[108:111], v157 offset:3072\n"
    "v_mfma_f32_16x16x32_bf16 v[4:7], v[128:131], v[88:91], v[4:7]\n"
    "ds_read_b64_tr_b16 v[140:141], v160 offset:16384\n"
    "v_mfma_f32_16x16x32_bf16 v[8:11], v[136:139], v[88:91], v[8:11]\n"
    "ds_read_b64_tr_b16 v[142:143], v160 offset:24576\n"
    "v_mfma_f32_16x16x32_bf16 v[8:11], v[132:135], v[84:87], v[8:11]\n"
    "ds_read_b128 v[120:123], v157 offset:9216\n"
    "s_waitcnt lgkmcnt(7)\n"
    "v_mfma_f32_16x16x32_bf16 v[16:19], v[132:135], v[92:95], v[16:19]\n"
    "ds_read_b128 v[116:119], v157 offset:4096\n"
    "v_mfma_f32_16x16x32_bf16 v[16:19], v[136:139], v[96:99], v[16:19]\n"
    "ds_read_b64_tr_b16 v[144:145], v160 offset:18432\n"
    "v_mfma_f32_16x16x32_bf16 v[12:15], v[128:131], v[96:99], v[12:15]\n"
    "ds_read_b64_tr_b16 v[146:147], v160 offset:26624\n"
    "v_mfma_f32_16x16x32_bf16 v[12:15], v[124:127], v[92:95], v[12:15]\n"
    "ds_read_b64_tr_b16 v[152:153], v161 offset:18432\n"
    "s_waitcnt lgkmcnt(9)\n"
    "v_mfma_f32_16x16x32_bf16 v[20:23], v[124:127], v[100:103], v[20:23]\n"
    "ds_read_b64_tr_b16 v[154:155], v161 offset:26624\n"
    "v_mfma_f32_16x16x32_bf16 v[20:23], v[128:131], v[104:107], v[20:23]\n"
    "ds_read_b64_tr_b16 v[148:149], v161 offset:16384\n"
    "v_mfma_f32_16x16x32_bf16 v[24:27], v[136:139], v[104:107], v[24:27]\n"
    "ds_read_b128 v[88:91], v157 offset:15360\n"
    "v_mfma_f32_16x16x32_bf16 v[24:27], v[132:135], v[100:103], v[24:27]\n"
    "ds_read_b128 v[84:87], v157 offset:10240\n"
    "s_waitcnt lgkmcnt(11)\n"
    "v_mfma_f32_16x16x32_bf16 v[32:35], v[132:135], v[108:111], v[32:35]\n"
    "ds_read_b64_tr_b16 v[150:151], v161 offset:24576\n"
    "v_mfma_f32_16x16x32_bf16 v[32:35], v[136:139], v[112:115], v[32:35]\n"
    "ds_read_b128 v[96:99], v157 offset:16384\n"
    "v_mfma_f32_16x16x32_bf16 v[28:31], v[128:131], v[112:115], v[28:31]\n"
    "ds_read_b128 v[92:95], v157 offset:11264\n"
    "v_mfma_f32_16x16x32_bf16 v[28:31], v[124:127], v[108:111], v[28:31]\n"
    "ds_read_b128 v[104:107], v157 offset:17408\n"
    "s_waitcnt lgkmcnt(11)\n"
    "v_mfma_f32_16x16x32_bf16 v[36:39], v[124:127], v[116:119], v[36:39]\n"
    "ds_read_b128 v[100:103], v157 offset:12288\n"
    "v_mfma_f32_16x16x32_bf16 v[36:39], v[128:131], v[120:123], v[36:39]\n"
    "ds_read_b128 v[112:115], v157 offset:18432\n"
    "v_mfma_f32_16x16x32_bf16 v[40:43], v[136:139], v[120:123], v[40:43]\n"
    "ds_read_b128 v[108:111], v157 offset:13312\n"
    "s_waitcnt lgkmcnt(6)\n"
    "v_mfma_f32_16x16x32_bf16 v[40:43], v[132:135], v[116:119], v[40:43]\n"
    "s_barrier\n"
    "v_mfma_f32_16x16x32_bf16 v[4:7], v[140:143], v[84:87], v[4:7]\n"
    "ds_read_b64_tr_b16 v[124:125], v160 offset:32768\n"
    "v_mfma_f32_16x16x32_bf16 v[4:7], v[144:147], v[88:91], v[4:7]\n"
    "ds_read_b64_tr_b16 v[126:127], v160 offset:40960\n"
    "v_mfma_f32_16x16x32_bf16 v[8:11], v[152:155], v[88:91], v[8:11]\n"
    "ds_read_b128 v[120:123], v157 offset:19456\n"
    "v_mfma_f32_16x16x32_bf16 v[8:11], v[148:151], v[84:87], v[8:11]\n"
    "ds_read_b128 v[116:119], v157 offset:14336\n"
    "s_waitcnt lgkmcnt(8)\n"
    "v_mfma_f32_16x16x32_bf16 v[16:19], v[148:151], v[92:95], v[16:19]\n"
    "ds_read_b64_tr_b16 v[128:129], v160 offset:34816\n"
    "v_mfma_f32_16x16x32_bf16 v[16:19], v[152:155], v[96:99], v[16:19]\n"
    "ds_read_b64_tr_b16 v[130:131], v160 offset:43008\n"
    "v_mfma_f32_16x16x32_bf16 v[12:15], v[144:147], v[96:99], v[12:15]\n"
    "ds_read_b64_tr_b16 v[136:137], v161 offset:34816\n"
    "v_mfma_f32_16x16x32_bf16 v[12:15], v[140:143], v[92:95], v[12:15]\n"
    "ds_read_b64_tr_b16 v[138:139], v161 offset:43008\n"
    "s_waitcnt lgkmcnt(10)\n"
    "v_mfma_f32_16x16x32_bf16 v[20:23], v[140:143], v[100:103], v[20:23]\n"
    "ds_read_b64_tr_b16 v[132:133], v161 offset:32768\n"
    "v_mfma_f32_16x16x32_bf16 v[20:23], v[144:147], v[104:107], v[20:23]\n"
    "ds_read_b128 v[88:91], v157 offset:25600\n"
    "v_mfma_f32_16x16x32_bf16 v[24:27], v[152:155], v[104:107], v[24:27]\n"
    "ds_read_b128 v[84:87], v157 offset:20480\n"
    "v_mfma_f32_16x16x32_bf16 v[24:27], v[148:151], v[100:103], v[24:27]\n"
    "ds_read_b64_tr_b16 v[134:135], v161 offset:40960\n"
    "s_waitcnt lgkmcnt(12)\n"
    "v_mfma_f32_16x16x32_bf16 v[32:35], v[148:151], v[108:111], v[32:35]\n"
    "ds_read_b128 v[96:99], v157 offset:26624\n"
    "v_mfma_f32_16x16x32_bf16 v[32:35], v[152:155], v[112:115], v[32:35]\n"
    "ds_read_b128 v[92:95], v157 offset:21504\n"
    "v_mfma_f32_16x16x32_bf16 v[28:31], v[144:147], v[112:115], v[28:31]\n"
    "ds_read_b128 v[104:107], v157 offset:27648\n"
    "v_mfma_f32_16x16x32_bf16 v[28:31], v[140:143], v[108:111], v[28:31]\n"
    "ds_read_b128 v[100:103], v157 offset:22528\n"
    "s_waitcnt lgkmcnt(12)\n"
    "v_mfma_f32_16x16x32_bf16 v[36:39], v[140:143], v[116:119], v[36:39]\n"
    "ds_read_b128 v[112:115], v157 offset:28672\n"
    "v_mfma_f32_16x16x32_bf16 v[36:39], v[144:147], v[120:123], v[36:39]\n"
    "ds_read_b128 v[108:111], v157 offset:23552\n"
    "v_mfma_f32_16x16x32_bf16 v[40:43], v[152:155], v[120:123], v[40:43]\n"
    "ds_read_b128 v[120:123], v157 offset:29696\n"
    "s_waitcnt lgkmcnt(7)\n"
    "v_mfma_f32_16x16x32_bf16 v[40:43], v[148:151], v[116:119], v[40:43]\n"
    "s_barrier\n"
    "v_mfma_f32_16x16x32_bf16 v[4:7], v[124:127], v[84:87], v[4:7]\n"
    "ds_read_b128 v[116:119], v157 offset:24576\n"
    "v_mfma_f32_16x16x32_bf16 v[4:7], v[128:131], v[88:91], v[4:7]\n"
    "v_mfma_f32_16x16x32_bf16 v[8:11], v[136:139], v[88:91], v[8:11]\n"
    "v_mfma_f32_16x16x32_bf16 v[8:11], v[132:135], v[84:87], v[8:11]\n"
    "s_waitcnt lgkmcnt(6)\n"
    "v_mfma_f32_16x16x32_bf16 v[16:19], v[132:135], v[92:95], v[16:19]\n"
    "v_mfma_f32_16x16x32_bf16 v[16:19], v[136:139], v[96:99], v[16:19]\n"
    "v_mfma_f32_16x16x32_bf16 v[12:15], v[128:131], v[96:99], v[12:15]\n"
    "v_mfma_f32_16x16x32_bf16 v[12:15], v[124:127], v[92:95], v[12:15]\n"
    "s_waitcnt lgkmcnt(4)\n"
    "v_mfma_f32_16x16x32_bf16 v[20:23], v[124:127], v[100:103], v[20:23]\n"
    "v_mfma_f32_16x16x32_bf16 v[20:23], v[128:131], v[104:107], v[20:23]\n"
    "v_mfma_f32_16x16x32_bf16 v[24:27], v[136:139], v[104:107], v[24:27]\n"
    "v_mfma_f32_16x16x32_bf16 v[24:27], v[132:135], v[100:103], v[24:27]\n"
    "s_waitcnt lgkmcnt(2)\n"
    "v_mfma_f32_16x16x32_bf16 v[32:35], v[132:135], v[108:111], v[32:35]\n"
    "v_mfma_f32_16x16x32_bf16 v[32:35], v[136:139], v[112:115], v[32:35]\n"
    "v_mfma_f32_16x16x32_bf16 v[28:31], v[128:131], v[112:115], v[28:31]\n"
    "v_mfma_f32_16x16x32_bf16 v[28:31], v[124:127], v[108:111], v[28:31]\n"
    "s_waitcnt lgkmcnt(0)\n"
    "v_mfma_f32_16x16x32_bf16 v[36:39], v[124:127], v[116:119], v[36:39]\n"
    "v_mfma_f32_16x16x32_bf16 v[36:39], v[128:131], v[120:123], v[36:39]\n"
    "v_mfma_f32_16x16x32_bf16 v[40:43], v[136:139], v[120:123], v[40:43]\n"
    "v_mfma_f32_16x16x32_bf16 v[40:43], v[132:135], v[116:119], v[40:43]\n"
    "s_setprio 0\n"
    "s_barrier\n"
    "v_cvt_pk_bf16_f32 v4, v4, v5\n"
    "v_cvt_pk_bf16_f32 v5, v6, v7\n"
    "v_cvt_pk_bf16_f32 v6, v8, v9\n"
    "v_cvt_pk_bf16_f32 v7, v10, v11\n"
    "buffer_store_dwordx4 v[4:7], v156, s[68:71], s22 offen offset:0 sc1\n"
    "s_barrier\n"
    "v_cvt_pk_bf16_f32 v12, v12, v13\n"
    "v_cvt_pk_bf16_f32 v13, v14, v15\n"
    "v_cvt_pk_bf16_f32 v14, v16, v17\n"
    "v_cvt_pk_bf16_f32 v15, v18, v19\n"
    "buffer_store_dwordx4 v[12:15], v156, s[68:71], s22 offen offset:2048 sc1\n"
    "s_barrier\n"
    "v_cvt_pk_bf16_f32 v20, v20, v21\n"
    "v_cvt_pk_bf16_f32 v21, v22, v23\n"
    "v_cvt_pk_bf16_f32 v22, v24, v25\n"
    "v_cvt_pk_bf16_f32 v23, v26, v27\n"
    "s_add_u32 s31, s22, 4096\n"
    "buffer_store_dwordx4 v[20:23], v156, s[68:71], s31 offen offset:0 sc1\n"
    "s_barrier\n"
    "v_cvt_pk_bf16_f32 v28, v28, v29\n"
    "v_cvt_pk_bf16_f32 v29, v30, v31\n"
    "v_cvt_pk_bf16_f32 v30, v32, v33\n"
    "v_cvt_pk_bf16_f32 v31, v34, v35\n"
    "s_add_u32 s31, s22, 4096\n"
    "buffer_store_dwordx4 v[28:31], v156, s[68:71], s31 offen offset:2048 sc1\n"
    "s_barrier\n"
    "v_cvt_pk_bf16_f32 v36, v36, v37\n"
    "v_cvt_pk_bf16_f32 v37, v38, v39\n"
    "v_cvt_pk_bf16_f32 v38, v40, v41\n"
    "v_cvt_pk_bf16_f32 v39, v42, v43\n"
    "s_add_u32 s31, s22, 8192\n"
    "buffer_store_dwordx4 v[36:39], v156, s[68:71], s31 offen offset:0 sc1\n"
    "s_barrier\n"
    "s_add_u32 s31, s92, 0\n"
    "buffer_load_dwordx4 v[132:135], v156, s[68:71], s31 offen sc1\n"
    "s_add_u32 s31, s93, 0\n"
    "buffer_load_dwordx4 v[136:139], v156, s[68:71], s31 offen sc1\n"
    "s_add_u32 s31, s94, 0\n"
    "buffer_load_dwordx4 v[140:143], v156, s[68:71], s31 offen sc1\n"
    "s_barrier\n"
    "s_waitcnt vmcnt(3)\n"
    "s_mov_b64 exec, 0xff\n"
    "buffer_store_dwordx4 v[162:165], v156, s[68:71], s23 offen sc1\n"
    "s_mov_b64 exec, -1\n"
    "s_add_u32 s31, s92, 10240\n"
    "buffer_load_dwordx4 v[144:147], v156, s[68:71], s31 offen sc1\n"
    "s_add_u32 s31, s93, 10240\n"
    "buffer_load_dwordx4 v[148:151], v156, s[68:71], s31 offen sc1\n"
    "s_add_u32 s31, s94, 10240\n"
    "buffer_load_dwordx4 v[152:155], v156, s[68:71], s31 offen sc1\n"
    "s_barrier\n"
    "s_barrier\n"
    "s_mov_b64 exec, 0xf0\n"
    "buffer_load_dwordx2 v[2:3], v73, s[68:71], s27 offen sc1\n"
    "s_mov_b64 exec, -1\n"
    "s_add_u32 s31, s92, 20480\n"
    "buffer_load_dwordx4 v[108:111], v156, s[68:71], s31 offen sc1\n"
    "s_add_u32 s31, s93, 20480\n"
    "buffer_load_dwordx4 v[112:115], v156, s[68:71], s31 offen sc1\n"
    "s_add_u32 s31, s94, 20480\n"
    "buffer_load_dwordx4 v[116:119], v156, s[68:71], s31 offen sc1\n"
    "s_waitcnt vmcnt(10)\n"
    "v_lshlrev_b32 v44, 16, v132\n"
    "v_and_b32 v45, s5, v132\n"
    "v_lshlrev_b32 v46, 16, v133\n"
    "v_and_b32 v47, s5, v133\n"
    "v_lshlrev_b32 v48, 16, v134\n"
    "v_and_b32 v49, s5, v134\n"
    "v_lshlrev_b32 v50, 16, v135\n"
    "v_and_b32 v51, s5, v135\n"
    "s_waitcnt vmcnt(9)\n"
    "v_lshlrev_b32 v52, 16, v136\n"
    "v_and_b32 v53, s5, v136\n"
    "v_lshlrev_b32 v54, 16, v137\n"
    "v_and_b32 v55, s5, v137\n"
    "v_lshlrev_b32 v56, 16, v138\n"
    "v_and_b32 v57, s5, v138\n"
    "v_lshlrev_b32 v58, 16, v139\n"
    "v_and_b32 v59, s5, v139\n"
    "s_waitcnt vmcnt(8)\n"
    "v_lshlrev_b32 v60, 16, v140\n"
    "v_and_b32 v61, s5, v140\n"
    "v_lshlrev_b32 v62, 16, v141\n"
    "v_and_b32 v63, s5, v141\n"
    "v_lshlrev_b32 v64, 16, v142\n"
    "v_and_b32 v65, s5, v142\n"
    "v_lshlrev_b32 v66, 16, v143\n"
    "v_and_b32 v67, s5, v143\n"
    "s_barrier\n"
    "s_add_u32 s31, s92, 30720\n"
    "buffer_load_dwordx4 v[120:123], v156, s[68:71], s31 offen sc1\n"
    "s_add_u32 s31, s93, 30720\n"
    "buffer_load_dwordx4 v[124:127], v156, s[68:71], s31 offen sc1\n"
    "s_add_u32 s31, s94, 30720\n"
    "buffer_load_dwordx4 v[128:131], v156, s[68:71], s31 offen sc1\n"
    "s_waitcnt vmcnt(9)\n"
    "v_lshlrev_b32 v80, 16, v144\n"
    "v_and_b32 v81, s5, v144\n"
    "v_add_f32 v44, v44, v80\n"
    "v_add_f32 v45, v45, v81\n"
    "v_lshlrev_b32 v82, 16, v145\n"
    "v_and_b32 v83, s5, v145\n"
    "v_add_f32 v46, v46, v82\n"
    "v_add_f32 v47, v47, v83\n"
    "v_lshlrev_b32 v80, 16, v146\n"
    "v_and_b32 v81, s5, v146\n"
    "v_add_f32 v48, v48, v80\n"
    "v_add_f32 v49, v49, v81\n"
    "v_lshlrev_b32 v82, 16, v147\n"
    "v_and_b32 v83, s5, v147\n"
    "v_add_f32 v50, v50, v82\n"
    "v_add_f32 v51, v51, v83\n"
    "s_waitcnt vmcnt(8)\n"
    "v_lshlrev_b32 v80, 16, v148\n"
    "v_and_b32 v81, s5, v148\n"
    "v_add_f32 v52, v52, v80\n"
    "v_add_f32 v53, v53, v81\n"
    "v_lshlrev_b32 v82, 16, v149\n"
    "v_and_b32 v83, s5, v149\n"
    "v_add_f32 v54, v54, v82\n"
    "v_add_f32 v55, v55, v83\n"
    "v_lshlrev_b32 v80, 16, v150\n"
    "v_and_b32 v81, s5, v150\n"
    "v_add_f32 v56, v56, v80\n"
    "v_add_f32 v57, v57, v81\n"
    "v_lshlrev_b32 v82, 16, v151\n"
    "v_and_b32 v83, s5, v151\n"
    "v_add_f32 v58, v58, v82\n"
    "v_add_f32 v59, v59, v83\n"
    "s_waitcnt vmcnt(7)\n"
    "v_lshlrev_b32 v80, 16, v152\n"
    "v_and_b32 v81, s5, v152\n"
    "v_add_f32 v60, v60, v80\n"
    "v_add_f32 v61, v61, v81\n"
    "v_lshlrev_b32 v82, 16, v153\n"
    "v_and_b32 v83, s5, v153\n"
    "v_add_f32 v62, v62, v82\n"
    "v_add_f32 v63, v63, v83\n"
    "s_barrier\n"
    "s_mov_b32 s24, 0\n"
    "s_waitcnt vmcnt(6)\n"
    "v_cmp_eq_u64 vcc, v[164:165], v[2:3]\n"
    "s_nop 0\n"
    "s_and_b32 s96, vcc_lo, 0xf0\n"
    "s_and_b32 s97, s96, 48\n"
    "s_cmp_eq_u32 s97, 48\n"
    "s_cbranch_scc1 .LS_ok_2\n"
    "s_mov_b64 exec, 0xf0\n"
    "s_mov_b32 s95, 524288\n"
    "buffer_load_dwordx2 v[76:77], v73, s[68:71], s27 offen sc1\n"
    "s_sleep 4\n"
    "buffer_load_dwordx2 v[78:79], v73, s[68:71], s27 offen sc1\n"
    "s_sleep 4\n"
    ".LS_poll_1:\n"
    "buffer_load_dwordx2 v[74:75], v73, s[68:71], s27 offen sc1\n"
    "s_waitcnt vmcnt(2)\n"
    "v_cmp_eq_u64 vcc, v[164:165], v[76:77]\n"
    "s_nop 0\n"
    "s_and_b32 s96, vcc_lo, 0xf0\n"
    "s_and_b32 s97, s96, 48\n"
    "s_cmp_eq_u32 s97, 48\n"
    "s_cbranch_scc1 .LS_ok_2\n"
    "buffer_load_dwordx2 v[76:77], v73, s[68:71], s27 offen sc1\n"
    "s_waitcnt vmcnt(2)\n"
    "v_cmp_eq_u64 vcc, v[164:165], v[78:79]\n"
    "s_nop 0\n"
    "s_and_b32 s96, vcc_lo, 0xf0\n"
    "s_and_b32 s97, s96, 48\n"
    "s_cmp_eq_u32 s97, 48\n"
    "s_cbranch_scc1 .LS_ok_2\n"
    "buffer_load_dwordx2 v[78:79], v73, s[68:71], s27 offen sc1\n"
    "s_waitcnt vmcnt(2)\n"
    "v_cmp_eq_u64 vcc, v[164:165], v[74:75]\n"
    "s_nop 0\n"
    "s_and_b32 s96, vcc_lo, 0xf0\n"
    "s_and_b32 s97, s96, 48\n"
    "s_cmp_eq_u32 s97, 48\n"
    "s_cbranch_scc1 .LS_ok_2\n"
    "s_sub_u32 s95, s95, 1\n"
    "s_cmp_eq_u32 s95, 0\n"
    "s_cbranch_scc0 .LS_poll_1\n"
    ".LS_ok_2:\n"
    "s_mov_b64 exec, -1\n"
    "s_add_u32 s31, s92, 40960\n"
    "buffer_load_dwordx4 v[84:87], v156, s[68:71], s31 offen sc1\n"
    "s_add_u32 s31, s93, 40960\n"
    "buffer_load_dwordx4 v[88:91], v156, s[68:71], s31 offen sc1\n"
    "s_add_u32 s31, s94, 40960\n"
    "buffer_load_dwordx4 v[92:95], v156, s[68:71], s31 offen sc1\n"
    "s_add_u32 s31, s92, 51200\n"
    "buffer_load_dwordx4 v[96:99], v156, s[68:71], s31 offen sc1\n"
    "s_add_u32 s31, s93, 51200\n"
    "buffer_load_dwordx4 v[100:103], v156, s[68:71], s31 offen sc1\n"
    "s_add_u32 s31, s94, 51200\n"
    "buffer_load_dwordx4 v[104:107], v156, s[68:71], s31 offen sc1\n"
    "s_lshr_b32 s97, s96, 6\n"
    "s_lshl_b32 s24, s24, 2\n"
    "s_or_b32 s24, s24, s97\n"
    "s_cmp_eq_u32 s24, 3\n"
    "s_cselect_b32 s97, 0, 1\n"
    "s_cmp_ge_u32 s24, 3\n"
    "s_cselect_b32 s24, 1, 0\n"
    "s_cmp_eq_u32 s97, 1\n"
    "s_cbranch_scc1 .LS_later_3\n"
    "s_add_u32 s31, s92, 61440\n"
    "buffer_load_dwordx4 v[4:7], v156, s[68:71], s31 offen sc1\n"
    "s_add_u32 s31, s92, 71680\n"
    "buffer_load_dwordx4 v[16:19], v156, s[68:71], s31 offen sc1\n"
    "s_add_u32 s31, s93, 61440\n"
    "buffer_load_dwordx4 v[8:11], v156, s[68:71], s31 offen sc1\n"
    "s_add_u32 s31, s93, 71680\n"
    "buffer_load_dwordx4 v[20:23], v156, s[68:71], s31 offen sc1\n"
    "s_add_u32 s31, s94, 61440\n"
    "buffer_load_dwordx4 v[12:15], v156, s[68:71], s31 offen sc1\n"
    "s_add_u32 s31, s94, 71680\n"
    "buffer_load_dwordx4 v[24:27], v156, s[68:71], s31 offen sc1\n"
    ".LS_later_3:\n"
    "v_lshlrev_b32 v80, 16, v154\n"
    "v_and_b32 v81, s5, v154\n"
    "v_add_f32 v64, v64, v80\n"
    "v_add_f32 v65, v65, v81\n"
    "v_lshlrev_b32 v82, 16, v155\n"
    "v_and_b32 v83, s5, v155\n"
    "v_add_f32 v66, v66, v82\n"
    "v_add_f32 v67, v67, v83\n"
    "s_waitcnt vmcnt(11)\n"
    "v_lshlrev_b32 v80, 16, v108\n"
    "v_and_b32 v81, s5, v108\n"
    "v_add_f32 v44, v44, v80\n"
    "v_add_f32 v45, v45, v81\n"
    "v_lshlrev_b32 v82, 16, v109\n"
    "v_and_b32 v83, s5, v109\n"
    "v_add_f32 v46, v46, v82\n"
    "v_add_f32 v47, v47, v83\n"
    "v_lshlrev_b32 v80, 16, v110\n"
    "v_and_b32 v81, s5, v110\n"
    "v_add_f32 v48, v48, v80\n"
    "v_add_f32 v49, v49, v81\n"
    "v_lshlrev_b32 v82, 16, v111\n"
    "v_and_b32 v83, s5, v111\n"
    "v_add_f32 v50, v50, v82\n"
    "v_add_f32 v51, v51, v83\n"
    "s_waitcnt vmcnt(10)\n"
    "v_lshlrev_b32 v80, 16, v112\n"
    "v_and_b32 v81, s5, v112\n"
    "v_add_f32 v52, v52, v80\n"
    "v_add_f32 v53, v53, v81\n"
    "v_lshlrev_b32 v82, 16, v113\n"
    "v_and_b32 v83, s5, v113\n"
    "v_add_f32 v54, v54, v82\n"
    "v_add_f32 v55, v55, v83\n"
    "v_lshlrev_b32 v80, 16, v114\n"
    "v_and_b32 v81, s5, v114\n"
    "v_add_f32 v56, v56, v80\n"
    "v_add_f32 v57, v57, v81\n"
    "v_lshlrev_b32 v82, 16, v115\n"
    "v_and_b32 v83, s5, v115\n"
    "v_add_f32 v58, v58, v82\n"
    "v_add_f32 v59, v59, v83\n"
    "s_waitcnt vmcnt(9)\n"
    "v_lshlrev_b32 v80, 16, v116\n"
    "v_and_b32 v81, s5, v116\n"
    "v_add_f32 v60, v60, v80\n"
    "v_add_f32 v61, v61, v81\n"
    "v_lshlrev_b32 v82, 16, v117\n"
    "v_and_b32 v83, s5, v117\n"
    "v_add_f32 v62, v62, v82\n"
    "v_add_f32 v63, v63, v83\n"
    "v_lshlrev_b32 v80, 16, v118\n"
    "v_and_b32 v81, s5, v118\n"
    "v_add_f32 v64, v64, v80\n"
    "v_add_f32 v65, v65, v81\n"
    "v_lshlrev_b32 v82, 16, v119\n"
    "v_and_b32 v83, s5, v119\n"
    "v_add_f32 v66, v66, v82\n"
    "v_add_f32 v67, v67, v83\n"
    "s_waitcnt vmcnt(8)\n"
    "v_lshlrev_b32 v80, 16, v120\n"
    "v_and_b32 v81, s5, v120\n"
    "v_add_f32 v44, v44, v80\n"
    "v_add_f32 v45, v45, v81\n"
    "v_lshlrev_b32 v82, 16, v121\n"
    "v_and_b32 v83, s5, v121\n"
    "v_add_f32 v46, v46, v82\n"
    "v_add_f32 v47, v47, v83\n"
    "v_lshlrev_b32 v80, 16, v122\n"
    "v_and_b32 v81, s5, v122\n"
    "v_add_f32 v48, v48, v80\n"
    "v_add_f32 v49, v49, v81\n"
    "v_lshlrev_b32 v82, 16, v123\n"
    "v_and_b32 v83, s5, v123\n"
    "v_add_f32 v50, v50, v82\n"
    "v_add_f32 v51, v51, v83\n"
    "s_waitcnt vmcnt(7)\n"
    "v_lshlrev_b32 v80, 16, v124\n"
    "v_and_b32 v81, s5, v124\n"
    "v_add_f32 v52, v52, v80\n"
    "v_add_f32 v53, v53, v81\n"
    "v_lshlrev_b32 v82, 16, v125\n"
    "v_and_b32 v83, s5, v125\n"
    "v_add_f32 v54, v54, v82\n"
    "v_add_f32 v55, v55, v83\n"
    "v_lshlrev_b32 v80, 16, v126\n"
    "v_and_b32 v81, s5, v126\n"
    "v_add_f32 v56, v56, v80\n"
    "v_add_f32 v57, v57, v81\n"
    "v_lshlrev_b32 v82, 16, v127\n"
    "v_and_b32 v83, s5, v127\n"
    "v_add_f32 v58, v58, v82\n"
    "v_add_f32 v59, v59, v83\n"
    "s_waitcnt vmcnt(6)\n"
    "v_lshlrev_b32 v80, 16, v128\n"
    "v_and_b32 v81, s5, v128\n"
    "v_add_f32 v60, v60, v80\n"
    "v_add_f32 v61, v61, v81\n"
    "v_lshlrev_b32 v82, 16, v129\n"
    "v_and_b32 v83, s5, v129\n"
    "v_add_f32 v62, v62, v82\n"
    "v_add_f32 v63, v63, v83\n"
    "v_lshlrev_b32 v80, 16, v130\n"
    "v_and_b32 v81, s5, v130\n"
    ".Lpage9:\n"
    "s_cbranch_execz .Ltouch_next\n"
    "v_add_f32 v64, v64, v80\n"
    "v_add_f32 v65, v65, v81\n"
    "v_lshlrev_b32 v82, 16, v131\n"
    "v_and_b32 v83, s5, v131\n"
    "v_add_f32 v66, v66, v82\n"
    "v_add_f32 v67, v67, v83\n"
    "s_mov_b64 exec, 0xf0\n"
    "buffer_load_dwordx2 v[2:3], v73, s[68:71], s27 offen sc1\n"
    "s_mov_b64 exec, -1\n"
    "s_waitcnt vmcnt(6)\n"
    "v_lshlrev_b32 v80, 16, v84\n"
    "v_and_b32 v81, s5, v84\n"
    "v_add_f32 v44, v44, v80\n"
    "v_add_f32 v45, v45, v81\n"
    "v_lshlrev_b32 v82, 16, v85\n"
    "v_and_b32 v83, s5, v85\n"
    "v_add_f32 v46, v46, v82\n"
    "v_add_f32 v47, v47, v83\n"
    "v_lshlrev_b32 v80, 16, v86\n"
    "v_and_b32 v81, s5, v86\n"
    "v_add_f32 v48, v48, v80\n"
    "v_add_f32 v49, v49, v81\n"
    "v_lshlrev_b32 v82, 16, v87\n"
    "v_and_b32 v83, s5, v87\n"
    "v_add_f32 v50, v50, v82\n"
    "v_add_f32 v51, v51, v83\n"
    "s_waitcnt vmcnt(5)\n"
    "v_lshlrev_b32 v80, 16, v88\n"
    "v_and_b32 v81, s5, v88\n"
    "v_add_f32 v52, v52, v80\n"
    "v_add_f32 v53, v53, v81\n"
    "v_lshlrev_b32 v82, 16, v89\n"
    "v_and_b32 v83, s5, v89\n"
    "v_add_f32 v54, v54, v82\n"
    "v_add_f32 v55, v55, v83\n"
    "v_lshlrev_b32 v80, 16, v90\n"
    "v_and_b32 v81, s5, v90\n"
    "v_add_f32 v56, v56, v80\n"
    "v_add_f32 v57, v57, v81\n"
    "v_lshlrev_b32 v82, 16, v91\n"
    "v_and_b32 v83, s5, v91\n"
    "v_add_f32 v58, v58, v82\n"
    "v_add_f32 v59, v59, v83\n"
    "s_waitcnt vmcnt(4)\n"
    "v_lshlrev_b32 v80, 16, v92\n"
    "v_and_b32 v81, s5, v92\n"
    "v_add_f32 v60, v60, v80\n"
    "v_add_f32 v61, v61, v81\n"
    "v_lshlrev_b32 v82, 16, v93\n"
    "v_and_b32 v83, s5, v93\n"
    "v_add_f32 v62, v62, v82\n"
    "v_add_f32 v63, v63, v83\n"
    "v_lshlrev_b32 v80, 16, v94\n"
    "v_and_b32 v81, s5, v94\n"
    "v_add_f32 v64, v64, v80\n"
    "v_add_f32 v65, v65, v81\n"
    "v_lshlrev_b32 v82, 16, v95\n"
    "v_and_b32 v83, s5, v95\n"
    "v_add_f32 v66, v66, v82\n"
    "v_add_f32 v67, v67, v83\n"
    "s_waitcnt vmcnt(3)\n"
    "v_lshlrev_b32 v80, 16, v96\n"
    "v_and_b32 v81, s5, v96\n"
    "v_add_f32 v44, v44, v80\n"
    "v_add_f32 v45, v45, v81\n"
    "v_lshlrev_b32 v82, 16, v97\n"
    "v_and_b32 v83, s5, v97\n"
    "v_add_f32 v46, v46, v82\n"
    "v_add_f32 v47, v47, v83\n"
    "v_lshlrev_b32 v80, 16, v98\n"
    "v_and_b32 v81, s5, v98\n"
    "v_add_f32 v48, v48, v80\n"
    "v_add_f32 v49, v49, v81\n"
    "v_lshlrev_b32 v82, 16, v99\n"
    "v_and_b32 v83, s5, v99\n"
    "v_add_f32 v50, v50, v82\n"
    "v_add_f32 v51, v51, v83\n"
    "s_waitcnt vmcnt(2)\n"
    "v_lshlrev_b32 v80, 16, v100\n"
    "v_and_b32 v81, s5, v100\n"
    "v_add_f32 v52, v52, v80\n"
    "v_add_f32 v53, v53, v81\n"
    "v_lshlrev_b32 v82, 16, v101\n"
    "v_and_b32 v83, s5, v101\n"
    "v_add_f32 v54, v54, v82\n"
    "v_add_f32 v55, v55, v83\n"
    "v_lshlrev_b32 v80, 16, v102\n"
    "v_and_b32 v81, s5, v102\n"
    "v_add_f32 v56, v56, v80\n"
    "v_add_f32 v57, v57, v81\n"
    "v_lshlrev_b32 v82, 16, v103\n"
    "v_and_b32 v83, s5, v103\n"
    "v_add_f32 v58, v58, v82\n"
    "v_add_f32 v59, v59, v83\n"
    "s_waitcnt vmcnt(1)\n"
    "v_lshlrev_b32 v80, 16, v104\n"
    "v_and_b32 v81, s5, v104\n"
    "v_add_f32 v60, v60, v80\n"
    "v_add_f32 v61, v61, v81\n"
    "v_lshlrev_b32 v82, 16, v105\n"
    "v_and_b32 v83, s5, v105\n"
    "v_add_f32 v62, v62, v82\n"
    "v_add_f32 v63, v63, v83\n"
    "v_lshlrev_b32 v80, 16, v106\n"
    "v_and_b32 v81, s5, v106\n"
    "v_add_f32 v64, v64, v80\n"
    "v_add_f32 v65, v65, v81\n"
    "v_lshlrev_b32 v82, 16, v107\n"
    "v_and_b32 v83, s5, v107\n"
    "v_add_f32 v66, v66, v82\n"
    "v_add_f32 v67, v67, v83\n"
    "s_waitcnt vmcnt(0)\n"
    "v_cmp_eq_u64 vcc, v[164:165], v[2:3]\n"
    "s_nop 0\n"
    "s_and_b32 s96, vcc_lo, 0xf0\n"
    "s_lshr_b32 s97, s96, 6\n"
    "s_lshl_b32 s24, s24, 2\n"
    "s_or_b32 s24, s24, s97\n"
    "s_cmp_eq_u32 s24, 3\n"
    "s_cselect_b32 s97, 0, 1\n"
    "s_cmp_ge_u32 s24, 3\n"
    "s_cselect_b32 s24, 1, 0\n"
    "s_cmp_eq_u32 s97, 1\n"
    "s_cbranch_scc1 .LS_later_4\n"
    "s_add_u32 s31, s92, 61440\n"
    "buffer_load_dwordx4 v[4:7], v156, s[68:71], s31 offen sc1\n"
    "s_add_u32 s31, s92, 71680\n"
    "buffer_load_dwordx4 v[16:19], v156, s[68:71], s31 offen sc1\n"
    "s_add_u32 s31, s93, 61440\n"
    "buffer_load_dwordx4 v[8:11], v156, s[68:71], s31 offen sc1\n"
    "s_add_u32 s31, s93, 71680\n"
    "buffer_load_dwordx4 v[20:23], v156, s[68:71], s31 offen sc1\n"
    "s_add_u32 s31, s94, 61440\n"
    "buffer_load_dwordx4 v[12:15], v156, s[68:71], s31 offen sc1\n"
    "s_add_u32 s31, s94, 71680\n"
    "buffer_load_dwordx4 v[24:27], v156, s[68:71], s31 offen sc1\n"
    ".LS_later_4:\n"
    "s_mov_b64 exec, 0xf0\n"
    "buffer_load_dwordx2 v[2:3], v73, s[68:71], s27 offen sc1\n"
    "s_mov_b64 exec, -1\n"
    "s_barrier\n"
    "ds_read_b128 v[132:135], v72 offset:0\n"
    "ds_read_b128 v[136:139], v72 offset:1024\n"
    "ds_read_b128 v[140:143], v72 offset:20480\n"
    "ds_read_b128 v[144:147], v72 offset:21504\n"
    "ds_read_b128 v[28:31], v72 offset:4096\n"
    "ds_read_b128 v[32:35], v72 offset:5120\n"
    "ds_read_b128 v[36:39], v72 offset:24576\n"
    "ds_read_b128 v[40:43], v72 offset:25600\n"
    "ds_read_b128 v[108:111], v72 offset:8192\n"
    "ds_read_b128 v[112:115], v72 offset:9216\n"
    "ds_read_b128 v[116:119], v72 offset:28672\n"
    "ds_read_b128 v[120:123], v72 offset:29696\n"
    "s_add_u32 s30, s25, 18432\n"
    "s_waitcnt lgkmcnt(8)\n"
    "v_pk_add_f32 v[44:45], v[44:45], v[132:133]\n"
    "v_pk_add_f32 v[46:47], v[46:47], v[134:135]\n"
    "v_pk_add_f32 v[48:49], v[48:49], v[136:137]\n"
    "v_pk_add_f32 v[50:51], v[50:51], v[138:139]\n"
    "v_pk_add_f32 v[44:45], v[44:45], v[140:141]\n"
    "v_pk_add_f32 v[46:47], v[46:47], v[142:143]\n"
    "v_pk_add_f32 v[48:49], v[48:49], v[144:145]\n"
    "v_pk_add_f32 v[50:51], v[50:51], v[146:147]\n"
    "s_waitcnt lgkmcnt(4)\n"
    "v_pk_add_f32 v[52:53], v[52:53], v[28:29]\n"
    "v_pk_add_f32 v[54:55], v[54:55], v[30:31]\n"
    "v_pk_add_f32 v[56:57], v[56:57], v[32:33]\n"
    "v_pk_add_f32 v[58:59], v[58:59], v[34:35]\n"
    "v_pk_add_f32 v[52:53], v[52:53], v[36:37]\n"
    "v_pk_add_f32 v[54:55], v[54:55], v[38:39]\n"
    "v_pk_add_f32 v[56:57], v[56:57], v[40:41]\n"
    "v_pk_add_f32 v[58:59], v[58:59], v[42:43]\n"
    "s_waitcnt lgkmcnt(0)\n"
    "v_pk_add_f32 v[60:61], v[60:61], v[108:109]\n"
    "v_pk_add_f32 v[62:63], v[62:63], v[110:111]\n"
    "v_pk_add_f32 v[64:65], v[64:65], v[112:113]\n"
    "v_pk_add_f32 v[66:67], v[66:67], v[114:115]\n"
    "v_pk_add_f32 v[60:61], v[60:61], v[116:117]\n"
    "v_pk_add_f32 v[62:63], v[62:63], v[118:119]\n"
    "v_pk_add_f32 v[64:65], v[64:65], v[120:121]\n"
    "v_pk_add_f32 v[66:67], v[66:67], v[122:123]\n"
    "s_cmp_eq_u32 s24, 1\n"
    "s_cbranch_scc1 .LS_have_5\n"
    "s_waitcnt vmcnt(0)\n"
    "v_cmp_eq_u64 vcc, v[164:165], v[2:3]\n"
    "s_nop 0\n"
    "s_and_b32 s96, vcc_lo, 0xf0\n"
    "s_and_b32 s97, s96, 192\n"
    "s_cmp_eq_u32 s97, 192\n"
    "s_cbranch_scc1 .LS_ok_7\n"
    "s_mov_b64 exec, 0xf0\n"
    "s_mov_b32 s95, 524288\n"
    "buffer_load_dwordx2 v[76:77], v73, s[68:71], s27 offen sc1\n"
    "s_sleep 4\n"
    "buffer_load_dwordx2 v[78:79], v73, s[68:71], s27 offen sc1\n"
    "s_sleep 4\n"
    ".LS_poll_6:\n"
    "buffer_load_dwordx2 v[74:75], v73, s[68:71], s27 offen sc1\n"
    "s_waitcnt vmcnt(2)\n"
    "v_cmp_eq_u64 vcc, v[164:165], v[76:77]\n"
    "s_nop 0\n"
    "s_and_b32 s96, vcc_lo, 0xf0\n"
    "s_and_b32 s97, s96, 192\n"
    "s_cmp_eq_u32 s97, 192\n"
    "s_cbranch_scc1 .LS_ok_7\n"
    "buffer_load_dwordx2 v[76:77], v73, s[68:71], s27 offen sc1\n"
    "s_waitcnt vmcnt(2)\n"
    "v_cmp_eq_u64 vcc, v[164:165], v[78:79]\n"
    "s_nop 0\n"
    "s_and_b32 s96, vcc_lo, 0xf0\n"
    "s_and_b32 s97, s96, 192\n"
    "s_cmp_eq_u32 s97, 192\n"
    "s_cbranch_scc1 .LS_ok_7\n"
    "buffer_load_dwordx2 v[78:79], v73, s[68:71], s27 offen sc1\n"
    "s_waitcnt vmcnt(2)\n"
    "v_cmp_eq_u64 vcc, v[164:165], v[74:75]\n"
    "s_nop 0\n"
    "s_and_b32 s96, vcc_lo, 0xf0\n"
    "s_and_b32 s97, s96, 192\n"
    "s_cmp_eq_u32 s97, 192\n"
    "s_cbranch_scc1 .LS_ok_7\n"
    "s_sub_u32 s95, s95, 1\n"
    "s_cmp_eq_u32 s95, 0\n"
    "s_cbranch_scc0 .LS_poll_6\n"
    ".LS_ok_7:\n"
    "s_mov_b64 exec, -1\n"
    "s_add_u32 s31, s92, 61440\n"
    "buffer_load_dwordx4 v[4:7], v156, s[68:71], s31 offen sc1\n"
    "s_add_u32 s31, s92, 71680\n"
    "buffer_load_dwordx4 v[16:19], v156, s[68:71], s31 offen sc1\n"
    "s_add_u32 s31, s93, 61440\n"
    "buffer_load_dwordx4 v[8:11], v156, s[68:71], s31 offen sc1\n"
    "s_add_u32 s31, s93, 71680\n"
    "buffer_load_dwordx4 v[20:23], v156, s[68:71], s31 offen sc1\n"
    "s_add_u32 s31, s94, 61440\n"
    "buffer_load_dwordx4 v[12:15], v156, s[68:71], s31 offen sc1\n"
    "s_add_u32 s31, s94, 71680\n"
    "buffer_load_dwordx4 v[24:27], v156, s[68:71], s31 offen sc1\n"
    ".LS_have_5:\n"
    "s_waitcnt vmcnt(5)\n"
    "v_lshlrev_b32 v84, 16, v4\n"
    "v_and_b32 v85, s5, v4\n"
    "v_lshlrev_b32 v86, 16, v5\n"
    "v_and_b32 v87, s5, v5\n"
    "v_lshlrev_b32 v88, 16, v6\n"
    "v_and_b32 v89, s5, v6\n"
    "v_lshlrev_b32 v90, 16, v7\n"
    "v_and_b32 v91, s5, v7\n"
    "v_pk_add_f32 v[44:45], v[44:45], v[84:85]\n"
    "v_pk_add_f32 v[46:47], v[46:47], v[86:87]\n"
    "v_pk_add_f32 v[48:49], v[48:49], v[88:89]\n"
    "v_pk_add_f32 v[50:51], v[50:51], v[90:91]\n"
    "s_waitcnt vmcnt(4)\n"
    "v_lshlrev_b32 v92, 16, v16\n"
    "v_and_b32 v93, s5, v16\n"
    "v_lshlrev_b32 v94, 16, v17\n"
    "v_and_b32 v95, s5, v17\n"
    "v_lshlrev_b32 v96, 16, v18\n"
    "v_and_b32 v97, s5, v18\n"
    "v_lshlrev_b32 v98, 16, v19\n"
    "v_and_b32 v99, s5, v19\n"
    "v_pk_add_f32 v[44:45], v[44:45], v[92:93]\n"
    "v_pk_add_f32 v[46:47], v[46:47], v[94:95]\n"
    "v_pk_add_f32 v[48:49], v[48:49], v[96:97]\n"
    "v_pk_add_f32 v[50:51], v[50:51], v[98:99]\n"
    "ds_write_b128 v69, v[44:47] offset:0\n"
    "ds_write_b128 v69, v[48:51] offset:1024\n"
    "ds_read_b128 v[44:47], v71 offset:0\n"
    "ds_read_b128 v[48:51], v71 offset:1024\n"
    "s_waitcnt vmcnt(3)\n"
    "v_lshlrev_b32 v84, 16, v8\n"
    "v_and_b32 v85, s5, v8\n"
    "v_lshlrev_b32 v86, 16, v9\n"
    "v_and_b32 v87, s5, v9\n"
    "v_lshlrev_b32 v88, 16, v10\n"
    "v_and_b32 v89, s5, v10\n"
    "v_lshlrev_b32 v90, 16, v11\n"
    "v_and_b32 v91, s5, v11\n"
    "v_pk_add_f32 v[52:53], v[52:53], v[84:85]\n"
    "v_pk_add_f32 v[54:55], v[54:55], v[86:87]\n"
    "v_pk_add_f32 v[56:57], v[56:57], v[88:89]\n"
    "v_pk_add_f32 v[58:59], v[58:59], v[90:91]\n"
    "s_waitcnt vmcnt(2)\n"
    "v_lshlrev_b32 v92, 16, v20\n"
    "v_and_b32 v93, s5, v20\n"
    "v_lshlrev_b32 v94, 16, v21\n"
    "v_and_b32 v95, s5, v21\n"
    "v_lshlrev_b32 v96, 16, v22\n"
    "v_and_b32 v97, s5, v22\n"
    "v_lshlrev_b32 v98, 16, v23\n"
    "v_and_b32 v99, s5, v23\n"
    "v_pk_add_f32 v[52:53], v[52:53], v[92:93]\n"
    "v_pk_add_f32 v[54:55], v[54:55], v[94:95]\n"
    "v_pk_add_f32 v[56:57], v[56:57], v[96:97]\n"
    "v_pk_add_f32 v[58:59], v[58:59], v[98:99]\n"
    "ds_write_b128 v69, v[52:55] offset:2048\n"
    "ds_write_b128 v69, v[56:59] offset:3072\n"
    "ds_read_b128 v[52:55], v71 offset:2048\n"
    "ds_read_b128 v[56:59], v71 offset:3072\n"
    "s_cmp_eq_u32 s14, 1\n"
    "s_cbranch_scc1 .LS_two_8\n"
    "s_waitcnt vmcnt(1)\n"
    "v_lshlrev_b32 v84, 16, v12\n"
    "v_and_b32 v85, s5, v12\n"
    "v_lshlrev_b32 v86, 16, v13\n"
    "v_and_b32 v87, s5, v13\n"
    "v_lshlrev_b32 v88, 16, v14\n"
    "v_and_b32 v89, s5, v14\n"
    "v_lshlrev_b32 v90, 16, v15\n"
    "v_and_b32 v91, s5, v15\n"
    "v_pk_add_f32 v[60:61], v[60:61], v[84:85]\n"
    "v_pk_add_f32 v[62:63], v[62:63], v[86:87]\n"
    "v_pk_add_f32 v[64:65], v[64:65], v[88:89]\n"
    "v_pk_add_f32 v[66:67], v[66:67], v[90:91]\n"
    "s_waitcnt vmcnt(0)\n"
    "v_lshlrev_b32 v92, 16, v24\n"
    "v_and_b32 v93, s5, v24\n"
    "v_lshlrev_b32 v94, 16, v25\n"
    "v_and_b32 v95, s5, v25\n"
    "v_lshlrev_b32 v96, 16, v26\n"
    "v_and_b32 v97, s5, v26\n"
    "v_lshlrev_b32 v98, 16, v27\n"
    "v_and_b32 v99, s5, v27\n"
    "v_pk_add_f32 v[60:61], v[60:61], v[92:93]\n"
    "v_pk_add_f32 v[62:63], v[62:63], v[94:95]\n"
    "v_pk_add_f32 v[64:65], v[64:65], v[96:97]\n"
    "v_pk_add_f32 v[66:67], v[66:67], v[98:99]\n"
    "ds_write_b128 v69, v[60:63] offset:4096\n"
    "ds_write_b128 v69, v[64:67] offset:5120\n"
    "ds_read_b128 v[60:63], v71 offset:4096\n"
    "ds_read_b128 v[64:67], v71 offset:5120\n"
    "s_waitcnt lgkmcnt(8)\n"
    "buffer_store_dwordx4 v[44:47], v70, s[88:91], s25 offen offset:0 sc1\n"
    "buffer_store_dwordx4 v[48:51], v70, s[88:91], s25 offen offset:64 sc1\n"
    "s_waitcnt lgkmcnt(4)\n"
    "buffer_store_dwordx4 v[52:55], v70, s[88:91], s30 offen offset:0 sc1\n"
    "buffer_store_dwordx4 v[56:59], v70, s[88:91], s30 offen offset:64 sc1\n"
    "s_mov_b32 exec_hi, 0x0fffffff\n"
    "s_waitcnt lgkmcnt(0)\n"
    "buffer_store_dwordx4 v[60:63], v70, s[88:91], s26 offen offset:0 sc1\n"
    "buffer_store_dwordx4 v[64:67], v70, s[88:91], s26 offen offset:64 sc1\n"
    "s_mov_b64 exec, -1\n"
    "s_lshl_b32 s30, s10, 4\n"
    "s_add_u32 s30, s30, s11\n"
    "s_lshl_b32 s30, s30, 4\n"
    "s_add_u32 s30, s30, s12\n"
    "s_cmp_eq_u32 s30, 6400\n"
    "s_cbranch_scc0 .LS_nobump_9\n"
    "s_mov_b64 exec, 1\n"
    "buffer_store_dword v164, off, s[68:71], 0 sc1\n"
    "s_mov_b64 exec, -1\n"
    ".LS_nobump_9:\n"
    "s_endpgm\n"
    ".LS_two_8:\n"
    "s_waitcnt lgkmcnt(4)\n"
    "buffer_store_dwordx4 v[44:47], v70, s[88:91], s25 offen offset:0 sc1\n"
    "buffer_store_dwordx4 v[48:51], v70, s[88:91], s25 offen offset:64 sc1\n"
    "s_waitcnt lgkmcnt(0)\n"
    "buffer_store_dwordx4 v[52:55], v70, s[88:91], s30 offen offset:0 sc1\n"
    "buffer_store_dwordx4 v[56:59], v70, s[88:91], s30 offen offset:64 sc1\n"
    "s_branch .LS_nobump_9\n"
    ".LroleE:\n"
    "v_and_b32 v4, 15, v0\n"
    "v_lshrrev_b32 v5, 4, v0\n"
    "v_and_b32 v157, 6, v0\n"
    "v_xor_b32 v5, v5, v157\n"
    "v_lshlrev_b32 v5, 4, v5\n"
    "v_lshl_add_u32 v156, v4, 7, v5\n"
    "v_xor_b32 v157, 64, v156\n"
    "s_lshl_b32 s30, s14, 12\n"
    "s_add_u32 s30, s30, 96256\n"
    "v_lshrrev_b32 v4, 5, v0\n"
    "v_lshlrev_b32 v4, 10, v4\n"
    "v_bfe_u32 v5, v0, 2, 3\n"
    "v_lshl_add_u32 v4, v5, 7, v4\n"
    "v_and_b32 v160, 3, v0\n"
    "v_lshl_add_u32 v4, v160, 3, v4\n"
    "v_add_u32 v4, s30, v4\n"
    "v_lshrrev_b32 v5, 1, v5\n"
    "s_lshl_b32 s30, s15, 1\n"
    "v_xor_b32 v5, s30, v5\n"
    "v_lshl_add_u32 v160, v5, 5, v4\n"
    "v_xor_b32 v161, 32, v160\n"
    "s_mov_b32 s20, 0x3eeff558\n"
    "s_mov_b32 s21, 0x409d30fc\n"
    "s_mov_b32 s30, 161792\n"
    "v_and_b32 v158, 15, v0\n"
    "v_lshl_add_u32 v158, v158, 3, s30\n"
    "s_lshl_b32 s30, s12, 7\n"
    "s_add_u32 s30, s30, 92160\n"
    "v_and_b32 v159, 48, v0\n"
    "v_add_u32 v159, s30, v159\n"
    "s_mul_i32 s30, s12, 5120\n"
    "s_add_u32 s30, s30, 0\n"
    "v_lshl_add_u32 v1, v0, 4, s30\n"
    "v_bfe_u32 v4, v0, 1, 2\n"
    "v_lshrrev_b32 v5, 4, v0\n"
    "v_xor_b32 v4, v4, v5\n"
    "v_and_b32 v5, 1, v0\n"
    "v_lshlrev_b32 v5, 4, v5\n"
    "v_lshl_add_u32 v2, v4, 5, v5\n"
    "v_lshrrev_b32 v4, 5, v0\n"
    "v_bfe_u32 v5, v0, 3, 2\n"
    "v_lshl_add_u32 v4, v4, 3, v5\n"
    "v_mul_u32_u24 v4, 9216, v4\n"
    "v_add_u32 v2, v2, v4\n"
    "v_add_u32 v3, 146432, v2\n"
    "s_mov_b32 s30, 81920\n"
    "v_and_b32 v4, 15, v0\n"
    "v_lshl_add_u32 v162, v4, 3, s30\n"
    "v_lshrrev_b32 v4, 4, v0\n"
    "v_lshl_add_u32 v162, v4, 8, v162\n"
    "v_and_b32 v4, 1, v4\n"
    "v_mul_u32_u24 v4, 144, v4\n"
    "v_xor_b32 v162, v162, v4\n"
    "v_xor_b32 v163, 136, v162\n"
    "s_waitcnt lgkmcnt(0)\n"
    "s_load_dword s17, s[64:65], 0x0\n"
    "s_mov_b32 s72, s54\n"
    "s_and_b32 s73, s55, 0xffff\n"
    "s_mov_b32 s74, 5308416\n"
    "s_mov_b32 s75, 0x20000\n"
    "s_mul_i32 s26, s15, 294912\n"
    "s_lshl_b32 s30, s11, 10\n"
    "s_add_u32 s26, s26, s30\n"
    "s_lshl_b32 s30, s14, 7\n"
    "s_add_u32 s26, s26, s30\n"
    "s_lshl_b32 s27, s12, 11\n"
    "s_add_u32 s27, s27, 96256\n"
    "s_waitcnt vmcnt(0)\n"
    "s_barrier\n"
    "s_setprio 2\n"
    "ds_read_b64_tr_b16 v[124:125], v160 offset:0\n"
    "ds_read_b64_tr_b16 v[126:127], v160 offset:8192\n"
    "ds_read_b64_tr_b16 v[128:129], v160 offset:2048\n"
    "ds_read_b64_tr_b16 v[130:131], v160 offset:10240\n"
    "ds_read_b64_tr_b16 v[136:137], v161 offset:2048\n"
    "ds_read_b64_tr_b16 v[138:139], v161 offset:10240\n"
    "ds_read_b64_tr_b16 v[132:133], v161 offset:0\n"
    "ds_read_b64_tr_b16 v[134:135], v161 offset:8192\n"
    "ds_read_b128 v[84:87], v156 offset:0\n"
    "ds_read_b128 v[88:91], v157 offset:0\n"
    "ds_read_b128 v[92:95], v156 offset:2048\n"
    "ds_read_b128 v[96:99], v157 offset:2048\n"
    "ds_read_b128 v[100:103], v156 offset:4096\n"
    "ds_read_b128 v[104:107], v157 offset:4096\n"
    "s_waitcnt lgkmcnt(4)\n"
    "s_barrier\n"
    "v_mfma_f32_16x16x32_bf16 v[4:7], v[124:127], v[84:87], 0\n"
    "ds_read_b128 v[108:111], v156 offset:6144\n"
    "v_mfma_f32_16x16x32_bf16 v[4:7], v[128:131], v[88:91], v[4:7]\n"
    "ds_read_b128 v[112:115], v157 offset:6144\n"
    "v_mfma_f32_16x16x32_bf16 v[8:11], v[136:139], v[88:91], 0\n"
    "ds_read_b64_tr_b16 v[140:141], v160 offset:16384\n"
    "v_mfma_f32_16x16x32_bf16 v[8:11], v[132:135], v[84:87], v[8:11]\n"
    "ds_read_b64_tr_b16 v[142:143], v160 offset:24576\n"
    "s_waitcnt lgkmcnt(6)\n"
    "v_mfma_f32_16x16x32_bf16 v[24:27], v[132:135], v[92:95], 0\n"
    "ds_read_b128 v[116:119], v156 offset:8192\n"
    "v_mfma_f32_16x16x32_bf16 v[24:27], v[136:139], v[96:99], v[24:27]\n"
    "ds_read_b128 v[120:123], v157 offset:8192\n"
    "v_mfma_f32_16x16x32_bf16 v[20:23], v[128:131], v[96:99], 0\n"
    "ds_read_b64_tr_b16 v[144:145], v160 offset:18432\n"
    "v_mfma_f32_16x16x32_bf16 v[20:23], v[124:127], v[92:95], v[20:23]\n"
    "ds_read_b64_tr_b16 v[146:147], v160 offset:26624\n"
    "s_waitcnt lgkmcnt(8)\n"
    "v_mfma_f32_16x16x32_bf16 v[36:39], v[124:127], v[100:103], 0\n"
    "ds_read_b64_tr_b16 v[152:153], v161 offset:18432\n"
    "v_mfma_f32_16x16x32_bf16 v[36:39], v[128:131], v[104:107], v[36:39]\n"
    "ds_read_b64_tr_b16 v[154:155], v161 offset:26624\n"
    "v_mfma_f32_16x16x32_bf16 v[40:43], v[136:139], v[104:107], 0\n"
    "ds_read_b64_tr_b16 v[148:149], v161 offset:16384\n"
    "v_mfma_f32_16x16x32_bf16 v[40:43], v[132:135], v[100:103], v[40:43]\n"
    "ds_read_b64_tr_b16 v[150:151], v161 offset:24576\n"
    "s_waitcnt lgkmcnt(10)\n"
    "v_mfma_f32_16x16x32_bf16 v[56:59], v[132:135], v[108:111], 0\n"
    "v_mfma_f32_16x16x32_bf16 v[56:59], v[136:139], v[112:115], v[56:59]\n"
    "v_mfma_f32_16x16x32_bf16 v[52:55], v[128:131], v[112:115], 0\n"
    "v_mfma_f32_16x16x32_bf16 v[52:55], v[124:127], v[108:111], v[52:55]\n"
    "s_waitcnt lgkmcnt(6)\n"
    "v_mfma_f32_16x16x32_bf16 v[68:71], v[124:127], v[116:119], 0\n"
    "v_mfma_f32_16x16x32_bf16 v[68:71], v[128:131], v[120:123], v[68:71]\n"
    "v_mfma_f32_16x16x32_bf16 v[72:75], v[136:139], v[120:123], 0\n"
    "s_waitcnt lgkmcnt(0)\n"
    "v_mfma_f32_16x16x32_bf16 v[72:75], v[132:135], v[116:119], v[72:75]\n"
    "s_barrier\n"
    "v_mfma_f32_16x16x32_bf16 v[12:15], v[140:143], v[84:87], 0\n"
    "ds_read_b64_tr_b16 v[124:125], v160 offset:32768\n"
    "v_mfma_f32_16x16x32_bf16 v[12:15], v[144:147], v[88:91], v[12:15]\n"
    "ds_read_b64_tr_b16 v[126:127], v160 offset:40960\n"
    "v_mfma_f32_16x16x32_bf16 v[16:19], v[152:155], v[88:91], 0\n"
    "ds_read_b64_tr_b16 v[128:129], v160 offset:34816\n"
    "v_mfma_f32_16x16x32_bf16 v[16:19], v[148:151], v[84:87], v[16:19]\n"
    "ds_read_b64_tr_b16 v[130:131], v160 offset:43008\n"
    "v_mfma_f32_16x16x32_bf16 v[32:35], v[148:151], v[92:95], 0\n"
    "ds_read_b64_tr_b16 v[136:137], v161 offset:34816\n"
    "v_mfma_f32_16x16x32_bf16 v[32:35], v[152:155], v[96:99], v[32:35]\n"
    "ds_read_b64_tr_b16 v[138:139], v161 offset:43008\n"
    "v_mfma_f32_16x16x32_bf16 v[28:31], v[144:147], v[96:99], 0\n"
    "ds_read_b64_tr_b16 v[132:133], v161 offset:32768\n"
    "v_mfma_f32_16x16x32_bf16 v[28:31], v[140:143], v[92:95], v[28:31]\n"
    "ds_read_b128 v[88:91], v157 offset:10240\n"
    "v_mfma_f32_16x16x32_bf16 v[44:47], v[140:143], v[100:103], 0\n"
    "ds_read_b128 v[84:87], v156 offset:10240\n"
    "v_mfma_f32_16x16x32_bf16 v[44:47], v[144:147], v[104:107], v[44:47]\n"
    "ds_read_b64_tr_b16 v[134:135], v161 offset:40960\n"
    "v_mfma_f32_16x16x32_bf16 v[48:51], v[152:155], v[104:107], 0\n"
    "ds_read_b128 v[96:99], v157 offset:12288\n"
    "v_mfma_f32_16x16x32_bf16 v[48:51], v[148:151], v[100:103], v[48:51]\n"
    "ds_read_b128 v[92:95], v156 offset:12288\n"
    "v_mfma_f32_16x16x32_bf16 v[64:67], v[148:151], v[108:111], 0\n"
    "ds_read_b128 v[104:107], v157 offset:14336\n"
    "v_mfma_f32_16x16x32_bf16 v[64:67], v[152:155], v[112:115], v[64:67]\n"
    "ds_read_b128 v[100:103], v156 offset:14336\n"
    "v_mfma_f32_16x16x32_bf16 v[60:63], v[144:147], v[112:115], 0\n"
    "v_mfma_f32_16x16x32_bf16 v[60:63], v[140:143], v[108:111], v[60:63]\n"
    "v_mfma_f32_16x16x32_bf16 v[76:79], v[140:143], v[116:119], 0\n"
    "v_mfma_f32_16x16x32_bf16 v[76:79], v[144:147], v[120:123], v[76:79]\n"
    "v_mfma_f32_16x16x32_bf16 v[80:83], v[152:155], v[120:123], 0\n"
    "s_waitcnt lgkmcnt(4)\n"
    "v_mfma_f32_16x16x32_bf16 v[80:83], v[148:151], v[116:119], v[80:83]\n"
    "s_barrier\n"
    "v_mfma_f32_16x16x32_bf16 v[4:7], v[124:127], v[84:87], v[4:7]\n"
    "ds_read_b128 v[112:115], v157 offset:16384\n"
    "v_mfma_f32_16x16x32_bf16 v[4:7], v[128:131], v[88:91], v[4:7]\n"
    "ds_read_b128 v[108:111], v156 offset:16384\n"
    "v_mfma_f32_16x16x32_bf16 v[8:11], v[136:139], v[88:91], v[8:11]\n"
    "ds_read_b64_tr_b16 v[140:141], v160 offset:49152\n"
    "v_mfma_f32_16x16x32_bf16 v[8:11], v[132:135], v[84:87], v[8:11]\n"
    "ds_read_b64_tr_b16 v[142:143], v160 offset:57344\n"
    "s_waitcnt lgkmcnt(6)\n"
    "v_mfma_f32_16x16x32_bf16 v[24:27], v[132:135], v[92:95], v[24:27]\n"
    "ds_read_b128 v[120:123], v157 offset:18432\n"
    "v_mfma_f32_16x16x32_bf16 v[24:27], v[136:139], v[96:99], v[24:27]\n"
    "ds_read_b128 v[116:119], v156 offset:18432\n"
    "v_mfma_f32_16x16x32_bf16 v[20:23], v[128:131], v[96:99], v[20:23]\n"
    "ds_read_b64_tr_b16 v[144:145], v160 offset:51200\n"
    "v_mfma_f32_16x16x32_bf16 v[20:23], v[124:127], v[92:95], v[20:23]\n"
    "ds_read_b64_tr_b16 v[146:147], v160 offset:59392\n"
    "s_waitcnt lgkmcnt(8)\n"
    "v_mfma_f32_16x16x32_bf16 v[36:39], v[124:127], v[100:103], v[36:39]\n"
    "ds_read_b64_tr_b16 v[152:153], v161 offset:51200\n"
    "v_mfma_f32_16x16x32_bf16 v[36:39], v[128:131], v[104:107], v[36:39]\n"
    "ds_read_b64_tr_b16 v[154:155], v161 offset:59392\n"
    "v_mfma_f32_16x16x32_bf16 v[40:43], v[136:139], v[104:107], v[40:43]\n"
    "ds_read_b64_tr_b16 v[148:149], v161 offset:49152\n"
    "v_mfma_f32_16x16x32_bf16 v[40:43], v[132:135], v[100:103], v[40:43]\n"
    "ds_read_b64_tr_b16 v[150:151], v161 offset:57344\n"
    "s_waitcnt lgkmcnt(10)\n"
    "v_mfma_f32_16x16x32_bf16 v[56:59], v[132:135], v[108:111], v[56:59]\n"
    "v_mfma_f32_16x16x32_bf16 v[56:59], v[136:139], v[112:115], v[56:59]\n"
    "v_mfma_f32_16x16x32_bf16 v[52:55], v[128:131], v[112:115], v[52:55]\n"
    "v_mfma_f32_16x16x32_bf16 v[52:55], v[124:127], v[108:111], v[52:55]\n"
    "s_waitcnt lgkmcnt(6)\n"
    "v_mfma_f32_16x16x32_bf16 v[68:71], v[124:127], v[116:119], v[68:71]\n"
    "v_mfma_f32_16x16x32_bf16 v[68:71], v[128:131], v[120:123], v[68:71]\n"
    "v_mfma_f32_16x16x32_bf16 v[72:75], v[136:139], v[120:123], v[72:75]\n"
    "s_waitcnt lgkmcnt(0)\n"
    "v_mfma_f32_16x16x32_bf16 v[72:75], v[132:135], v[116:119], v[72:75]\n"
    "s_barrier\n"
    "v_mfma_f32_16x16x32_bf16 v[12:15], v[140:143], v[84:87], v[12:15]\n"
    "ds_read_b64_tr_b16 v[124:125], v160 offset:0\n"
    "v_mfma_f32_16x16x32_bf16 v[12:15], v[144:147], v[88:91], v[12:15]\n"
    "ds_read_b64_tr_b16 v[126:127], v160 offset:8192\n"
    "v_mfma_f32_16x16x32_bf16 v[16:19], v[152:155], v[88:91], v[16:19]\n"
    "ds_read_b64_tr_b16 v[128:129], v160 offset:2048\n"
    "v_mfma_f32_16x16x32_bf16 v[16:19], v[148:151], v[84:87], v[16:19]\n"
    "ds_read_b64_tr_b16 v[130:131], v160 offset:10240\n"
    "v_mfma_f32_16x16x32_bf16 v[32:35], v[148:151], v[92:95], v[32:35]\n"
    "ds_read_b64_tr_b16 v[136:137], v161 offset:2048\n"
    "v_mfma_f32_16x16x32_bf16 v[32:35], v[152:155], v[96:99], v[32:35]\n"
    "ds_read_b64_tr_b16 v[138:139], v161 offset:10240\n"
    "v_mfma_f32_16x16x32_bf16 v[28:31], v[144:147], v[96:99], v[28:31]\n"
    "ds_read_b64_tr_b16 v[132:133], v161 offset:0\n"
    "v_mfma_f32_16x16x32_bf16 v[28:31], v[140:143], v[92:95], v[28:31]\n"
    "ds_read_b128 v[88:91], v157 offset:20480\n"
    "v_mfma_f32_16x16x32_bf16 v[44:47], v[140:143], v[100:103], v[44:47]\n"
    "ds_read_b128 v[84:87], v156 offset:20480\n"
    "v_mfma_f32_16x16x32_bf16 v[44:47], v[144:147], v[104:107], v[44:47]\n"
    "ds_read_b64_tr_b16 v[134:135], v161 offset:8192\n"
    "v_mfma_f32_16x16x32_bf16 v[48:51], v[152:155], v[104:107], v[48:51]\n"
    "ds_read_b128 v[96:99], v157 offset:22528\n"
    "v_mfma_f32_16x16x32_bf16 v[48:51], v[148:151], v[100:103], v[48:51]\n"
    "ds_read_b128 v[92:95], v156 offset:22528\n"
    "v_mfma_f32_16x16x32_bf16 v[64:67], v[148:151], v[108:111], v[64:67]\n"
    "ds_read_b128 v[104:107], v157 offset:24576\n"
    "v_mfma_f32_16x16x32_bf16 v[64:67], v[152:155], v[112:115], v[64:67]\n"
    "ds_read_b128 v[100:103], v156 offset:24576\n"
    "v_mfma_f32_16x16x32_bf16 v[60:63], v[144:147], v[112:115], v[60:63]\n"
    "v_mfma_f32_16x16x32_bf16 v[60:63], v[140:143], v[108:111], v[60:63]\n"
    "v_mfma_f32_16x16x32_bf16 v[76:79], v[140:143], v[116:119], v[76:79]\n"
    "v_mfma_f32_16x16x32_bf16 v[76:79], v[144:147], v[120:123], v[76:79]\n"
    "v_mfma_f32_16x16x32_bf16 v[80:83], v[152:155], v[120:123], v[80:83]\n"
    "s_waitcnt lgkmcnt(4)\n"
    "v_mfma_f32_16x16x32_bf16 v[80:83], v[148:151], v[116:119], v[80:83]\n"
    "s_barrier\n"
    "v_mfma_f32_16x16x32_bf16 v[4:7], v[124:127], v[84:87], v[4:7]\n"
    "ds_read_b128 v[112:115], v157 offset:26624\n"
    "v_mfma_f32_16x16x32_bf16 v[4:7], v[128:131], v[88:91], v[4:7]\n"
    "ds_read_b128 v[108:111], v156 offset:26624\n"
    "v_mfma_f32_16x16x32_bf16 v[8:11], v[136:139], v[88:91], v[8:11]\n"
    "ds_read_b64_tr_b16 v[140:141], v160 offset:16384\n"
    "v_mfma_f32_16x16x32_bf16 v[8:11], v[132:135], v[84:87], v[8:11]\n"
    "ds_read_b64_tr_b16 v[142:143], v160 offset:24576\n"
    "s_waitcnt lgkmcnt(6)\n"
    "v_mfma_f32_16x16x32_bf16 v[24:27], v[132:135], v[92:95], v[24:27]\n"
    "ds_read_b128 v[120:123], v157 offset:28672\n"
    "v_mfma_f32_16x16x32_bf16 v[24:27], v[136:139], v[96:99], v[24:27]\n"
    "ds_read_b128 v[116:119], v156 offset:28672\n"
    "v_mfma_f32_16x16x32_bf16 v[20:23], v[128:131], v[96:99], v[20:23]\n"
    "ds_read_b64_tr_b16 v[144:145], v160 offset:18432\n"
    "v_mfma_f32_16x16x32_bf16 v[20:23], v[124:127], v[92:95], v[20:23]\n"
    "ds_read_b64_tr_b16 v[146:147], v160 offset:26624\n"
    "s_waitcnt lgkmcnt(8)\n"
    "v_mfma_f32_16x16x32_bf16 v[36:39], v[124:127], v[100:103], v[36:39]\n"
    "ds_read_b64_tr_b16 v[152:153], v161 offset:18432\n"
    "v_mfma_f32_16x16x32_bf16 v[36:39], v[128:131], v[104:107], v[36:39]\n"
    "ds_read_b64_tr_b16 v[154:155], v161 offset:26624\n"
    "v_mfma_f32_16x16x32_bf16 v[40:43], v[136:139], v[104:107], v[40:43]\n"
    "ds_read_b64_tr_b16 v[148:149], v161 offset:16384\n"
    "v_mfma_f32_16x16x32_bf16 v[40:43], v[132:135], v[100:103], v[40:43]\n"
    "ds_read_b64_tr_b16 v[150:151], v161 offset:24576\n"
    "s_waitcnt lgkmcnt(10)\n"
    "v_mfma_f32_16x16x32_bf16 v[56:59], v[132:135], v[108:111], v[56:59]\n"
    "v_mfma_f32_16x16x32_bf16 v[56:59], v[136:139], v[112:115], v[56:59]\n"
    "v_mfma_f32_16x16x32_bf16 v[52:55], v[128:131], v[112:115], v[52:55]\n"
    "v_mfma_f32_16x16x32_bf16 v[52:55], v[124:127], v[108:111], v[52:55]\n"
    "s_waitcnt lgkmcnt(6)\n"
    "v_mfma_f32_16x16x32_bf16 v[68:71], v[124:127], v[116:119], v[68:71]\n"
    "v_mfma_f32_16x16x32_bf16 v[68:71], v[128:131], v[120:123], v[68:71]\n"
    "v_mfma_f32_16x16x32_bf16 v[72:75], v[136:139], v[120:123], v[72:75]\n"
    "s_waitcnt lgkmcnt(0)\n"
    "v_mfma_f32_16x16x32_bf16 v[72:75], v[132:135], v[116:119], v[72:75]\n"
    "s_barrier\n"
    "v_mfma_f32_16x16x32_bf16 v[12:15], v[140:143], v[84:87], v[12:15]\n"
    "ds_read_b64_tr_b16 v[124:125], v160 offset:32768\n"
    "v_mfma_f32_16x16x32_bf16 v[12:15], v[144:147], v[88:91], v[12:15]\n"
    "ds_read_b64_tr_b16 v[126:127], v160 offset:40960\n"
    "v_mfma_f32_16x16x32_bf16 v[16:19], v[152:155], v[88:91], v[16:19]\n"
    "ds_read_b64_tr_b16 v[128:129], v160 offset:34816\n"
    "v_mfma_f32_16x16x32_bf16 v[16:19], v[148:151], v[84:87], v[16:19]\n"
    "ds_read_b64_tr_b16 v[130:131], v160 offset:43008\n"
    "v_mfma_f32_16x16x32_bf16 v[32:35], v[148:151], v[92:95], v[32:35]\n"
    "ds_read_b64_tr_b16 v[136:137], v161 offset:34816\n"
    "v_mfma_f32_16x16x32_bf16 v[32:35], v[152:155], v[96:99], v[32:35]\n"
    "ds_read_b64_tr_b16 v[138:139], v161 offset:43008\n"
    "v_mfma_f32_16x16x32_bf16 v[28:31], v[144:147], v[96:99], v[28:31]\n"
    "ds_read_b64_tr_b16 v[132:133], v161 offset:32768\n"
    "v_mfma_f32_16x16x32_bf16 v[28:31], v[140:143], v[92:95], v[28:31]\n"
    "ds_read_b128 v[88:91], v157 offset:30720\n"
    "v_mfma_f32_16x16x32_bf16 v[44:47], v[140:143], v[100:103], v[44:47]\n"
    "ds_read_b128 v[84:87], v156 offset:30720\n"
    "v_mfma_f32_16x16x32_bf16 v[44:47], v[144:147], v[104:107], v[44:47]\n"
    "ds_read_b64_tr_b16 v[134:135], v161 offset:40960\n"
    "v_mfma_f32_16x16x32_bf16 v[48:51], v[152:155], v[104:107], v[48:51]\n"
    "ds_read_b128 v[96:99], v157 offset:32768\n"
    "v_mfma_f32_16x16x32_bf16 v[48:51], v[148:151], v[100:103], v[48:51]\n"
    "ds_read_b128 v[92:95], v156 offset:32768\n"
    "v_mfma_f32_16x16x32_bf16 v[64:67], v[148:151], v[108:111], v[64:67]\n"
    "ds_read_b128 v[104:107], v157 offset:34816\n"
    "v_mfma_f32_16x16x32_bf16 v[64:67], v[152:155], v[112:115], v[64:67]\n"
    ".Lpage10:\n"
    "s_cbranch_execz .Ltouch_next\n"
    "ds_read_b128 v[100:103], v156 offset:34816\n"
    "v_mfma_f32_16x16x32_bf16 v[60:63], v[144:147], v[112:115], v[60:63]\n"
    "v_mfma_f32_16x16x32_bf16 v[60:63], v[140:143], v[108:111], v[60:63]\n"
    "v_mfma_f32_16x16x32_bf16 v[76:79], v[140:143], v[116:119], v[76:79]\n"
    "v_mfma_f32_16x16x32_bf16 v[76:79], v[144:147], v[120:123], v[76:79]\n"
    "v_mfma_f32_16x16x32_bf16 v[80:83], v[152:155], v[120:123], v[80:83]\n"
    "s_waitcnt lgkmcnt(4)\n"
    "v_mfma_f32_16x16x32_bf16 v[80:83], v[148:151], v[116:119], v[80:83]\n"
    "s_barrier\n"
    "v_mfma_f32_16x16x32_bf16 v[4:7], v[124:127], v[84:87], v[4:7]\n"
    "ds_read_b128 v[112:115], v157 offset:36864\n"
    "v_mfma_f32_16x16x32_bf16 v[4:7], v[128:131], v[88:91], v[4:7]\n"
    "ds_read_b128 v[108:111], v156 offset:36864\n"
    "v_mfma_f32_16x16x32_bf16 v[8:11], v[136:139], v[88:91], v[8:11]\n"
    "ds_read_b64_tr_b16 v[140:141], v160 offset:49152\n"
    "v_mfma_f32_16x16x32_bf16 v[8:11], v[132:135], v[84:87], v[8:11]\n"
    "ds_read_b64_tr_b16 v[142:143], v160 offset:57344\n"
    "s_waitcnt lgkmcnt(6)\n"
    "v_mfma_f32_16x16x32_bf16 v[24:27], v[132:135], v[92:95], v[24:27]\n"
    "ds_read_b128 v[120:123], v157 offset:38912\n"
    "v_mfma_f32_16x16x32_bf16 v[24:27], v[136:139], v[96:99], v[24:27]\n"
    "ds_read_b128 v[116:119], v156 offset:38912\n"
    "v_mfma_f32_16x16x32_bf16 v[20:23], v[128:131], v[96:99], v[20:23]\n"
    "ds_read_b64_tr_b16 v[144:145], v160 offset:51200\n"
    "v_mfma_f32_16x16x32_bf16 v[20:23], v[124:127], v[92:95], v[20:23]\n"
    "ds_read_b64_tr_b16 v[146:147], v160 offset:59392\n"
    "s_waitcnt lgkmcnt(8)\n"
    "v_mfma_f32_16x16x32_bf16 v[36:39], v[124:127], v[100:103], v[36:39]\n"
    "ds_read_b64_tr_b16 v[152:153], v161 offset:51200\n"
    "v_mfma_f32_16x16x32_bf16 v[36:39], v[128:131], v[104:107], v[36:39]\n"
    "ds_read_b64_tr_b16 v[154:155], v161 offset:59392\n"
    "v_mfma_f32_16x16x32_bf16 v[40:43], v[136:139], v[104:107], v[40:43]\n"
    "ds_read_b64_tr_b16 v[148:149], v161 offset:49152\n"
    "v_mfma_f32_16x16x32_bf16 v[40:43], v[132:135], v[100:103], v[40:43]\n"
    "ds_read_b64_tr_b16 v[150:151], v161 offset:57344\n"
    "s_waitcnt lgkmcnt(10)\n"
    "v_mfma_f32_16x16x32_bf16 v[56:59], v[132:135], v[108:111], v[56:59]\n"
    "v_mfma_f32_16x16x32_bf16 v[56:59], v[136:139], v[112:115], v[56:59]\n"
    "v_mfma_f32_16x16x32_bf16 v[52:55], v[128:131], v[112:115], v[52:55]\n"
    "v_mfma_f32_16x16x32_bf16 v[52:55], v[124:127], v[108:111], v[52:55]\n"
    "s_waitcnt lgkmcnt(6)\n"
    "v_mfma_f32_16x16x32_bf16 v[68:71], v[124:127], v[116:119], v[68:71]\n"
    "v_mfma_f32_16x16x32_bf16 v[68:71], v[128:131], v[120:123], v[68:71]\n"
    "v_mfma_f32_16x16x32_bf16 v[72:75], v[136:139], v[120:123], v[72:75]\n"
    "s_waitcnt lgkmcnt(0)\n"
    "v_mfma_f32_16x16x32_bf16 v[72:75], v[132:135], v[116:119], v[72:75]\n"
    "s_barrier\n"
    "v_mfma_f32_16x16x32_bf16 v[12:15], v[140:143], v[84:87], v[12:15]\n"
    "ds_read_b64_tr_b16 v[124:125], v160 offset:0\n"
    "v_mfma_f32_16x16x32_bf16 v[12:15], v[144:147], v[88:91], v[12:15]\n"
    "ds_read_b64_tr_b16 v[126:127], v160 offset:8192\n"
    "v_mfma_f32_16x16x32_bf16 v[16:19], v[152:155], v[88:91], v[16:19]\n"
    "ds_read_b64_tr_b16 v[128:129], v160 offset:2048\n"
    "v_mfma_f32_16x16x32_bf16 v[16:19], v[148:151], v[84:87], v[16:19]\n"
    "ds_read_b64_tr_b16 v[130:131], v160 offset:10240\n"
    "v_mfma_f32_16x16x32_bf16 v[32:35], v[148:151], v[92:95], v[32:35]\n"
    "ds_read_b64_tr_b16 v[136:137], v161 offset:2048\n"
    "v_mfma_f32_16x16x32_bf16 v[32:35], v[152:155], v[96:99], v[32:35]\n"
    "ds_read_b64_tr_b16 v[138:139], v161 offset:10240\n"
    "v_mfma_f32_16x16x32_bf16 v[28:31], v[144:147], v[96:99], v[28:31]\n"
    "ds_read_b64_tr_b16 v[132:133], v161 offset:0\n"
    "v_mfma_f32_16x16x32_bf16 v[28:31], v[140:143], v[92:95], v[28:31]\n"
    "ds_read_b128 v[88:91], v157 offset:40960\n"
    "v_mfma_f32_16x16x32_bf16 v[44:47], v[140:143], v[100:103], v[44:47]\n"
    "ds_read_b128 v[84:87], v156 offset:40960\n"
    "v_mfma_f32_16x16x32_bf16 v[44:47], v[144:147], v[104:107], v[44:47]\n"
    "ds_read_b64_tr_b16 v[134:135], v161 offset:8192\n"
    "v_mfma_f32_16x16x32_bf16 v[48:51], v[152:155], v[104:107], v[48:51]\n"
    "ds_read_b128 v[96:99], v157 offset:43008\n"
    "v_mfma_f32_16x16x32_bf16 v[48:51], v[148:151], v[100:103], v[48:51]\n"
    "ds_read_b128 v[92:95], v156 offset:43008\n"
    "v_mfma_f32_16x16x32_bf16 v[64:67], v[148:151], v[108:111], v[64:67]\n"
    "ds_read_b128 v[104:107], v157 offset:45056\n"
    "v_mfma_f32_16x16x32_bf16 v[64:67], v[152:155], v[112:115], v[64:67]\n"
    "ds_read_b128 v[100:103], v156 offset:45056\n"
    "v_mfma_f32_16x16x32_bf16 v[60:63], v[144:147], v[112:115], v[60:63]\n"
    "v_mfma_f32_16x16x32_bf16 v[60:63], v[140:143], v[108:111], v[60:63]\n"
    "v_mfma_f32_16x16x32_bf16 v[76:79], v[140:143], v[116:119], v[76:79]\n"
    "v_mfma_f32_16x16x32_bf16 v[76:79], v[144:147], v[120:123], v[76:79]\n"
    "v_mfma_f32_16x16x32_bf16 v[80:83], v[152:155], v[120:123], v[80:83]\n"
    "s_waitcnt lgkmcnt(4)\n"
    "v_mfma_f32_16x16x32_bf16 v[80:83], v[148:151], v[116:119], v[80:83]\n"
    "s_barrier\n"
    "v_mfma_f32_16x16x32_bf16 v[4:7], v[124:127], v[84:87], v[4:7]\n"
    "ds_read_b128 v[112:115], v157 offset:47104\n"
    "v_mfma_f32_16x16x32_bf16 v[4:7], v[128:131], v[88:91], v[4:7]\n"
    "ds_read_b128 v[108:111], v156 offset:47104\n"
    "v_mfma_f32_16x16x32_bf16 v[8:11], v[136:139], v[88:91], v[8:11]\n"
    "ds_read_b64_tr_b16 v[140:141], v160 offset:16384\n"
    "v_mfma_f32_16x16x32_bf16 v[8:11], v[132:135], v[84:87], v[8:11]\n"
    "ds_read_b64_tr_b16 v[142:143], v160 offset:24576\n"
    "s_waitcnt lgkmcnt(6)\n"
    "v_mfma_f32_16x16x32_bf16 v[24:27], v[132:135], v[92:95], v[24:27]\n"
    "ds_read_b128 v[120:123], v157 offset:49152\n"
    "v_mfma_f32_16x16x32_bf16 v[24:27], v[136:139], v[96:99], v[24:27]\n"
    "ds_read_b128 v[116:119], v156 offset:49152\n"
    "v_mfma_f32_16x16x32_bf16 v[20:23], v[128:131], v[96:99], v[20:23]\n"
    "ds_read_b64_tr_b16 v[144:145], v160 offset:18432\n"
    "v_mfma_f32_16x16x32_bf16 v[20:23], v[124:127], v[92:95], v[20:23]\n"
    "ds_read_b64_tr_b16 v[146:147], v160 offset:26624\n"
    "s_waitcnt lgkmcnt(8)\n"
    "v_mfma_f32_16x16x32_bf16 v[36:39], v[124:127], v[100:103], v[36:39]\n"
    "ds_read_b64_tr_b16 v[152:153], v161 offset:18432\n"
    "v_mfma_f32_16x16x32_bf16 v[36:39], v[128:131], v[104:107], v[36:39]\n"
    "ds_read_b64_tr_b16 v[154:155], v161 offset:26624\n"
    "v_mfma_f32_16x16x32_bf16 v[40:43], v[136:139], v[104:107], v[40:43]\n"
    "ds_read_b64_tr_b16 v[148:149], v161 offset:16384\n"
    "v_mfma_f32_16x16x32_bf16 v[40:43], v[132:135], v[100:103], v[40:43]\n"
    "ds_read_b64_tr_b16 v[150:151], v161 offset:24576\n"
    "s_waitcnt lgkmcnt(10)\n"
    "v_mfma_f32_16x16x32_bf16 v[56:59], v[132:135], v[108:111], v[56:59]\n"
    "v_mfma_f32_16x16x32_bf16 v[56:59], v[136:139], v[112:115], v[56:59]\n"
    "v_mfma_f32_16x16x32_bf16 v[52:55], v[128:131], v[112:115], v[52:55]\n"
    "v_mfma_f32_16x16x32_bf16 v[52:55], v[124:127], v[108:111], v[52:55]\n"
    "s_waitcnt lgkmcnt(6)\n"
    "v_mfma_f32_16x16x32_bf16 v[68:71], v[124:127], v[116:119], v[68:71]\n"
    "v_mfma_f32_16x16x32_bf16 v[68:71], v[128:131], v[120:123], v[68:71]\n"
    "v_mfma_f32_16x16x32_bf16 v[72:75], v[136:139], v[120:123], v[72:75]\n"
    "s_waitcnt lgkmcnt(0)\n"
    "v_mfma_f32_16x16x32_bf16 v[72:75], v[132:135], v[116:119], v[72:75]\n"
    "s_barrier\n"
    "v_mfma_f32_16x16x32_bf16 v[12:15], v[140:143], v[84:87], v[12:15]\n"
    "v_add_u32 v156, 51200, v156\n"
    "v_add_u32 v157, 51200, v157\n"
    "v_mfma_f32_16x16x32_bf16 v[12:15], v[144:147], v[88:91], v[12:15]\n"
    "ds_read_b64_tr_b16 v[124:125], v160 offset:32768\n"
    "v_mfma_f32_16x16x32_bf16 v[16:19], v[152:155], v[88:91], v[16:19]\n"
    "ds_read_b64_tr_b16 v[126:127], v160 offset:40960\n"
    "v_mfma_f32_16x16x32_bf16 v[16:19], v[148:151], v[84:87], v[16:19]\n"
    "ds_read_b64_tr_b16 v[128:129], v160 offset:34816\n"
    "v_mfma_f32_16x16x32_bf16 v[32:35], v[148:151], v[92:95], v[32:35]\n"
    "ds_read_b64_tr_b16 v[130:131], v160 offset:43008\n"
    "v_mfma_f32_16x16x32_bf16 v[32:35], v[152:155], v[96:99], v[32:35]\n"
    "ds_read_b64_tr_b16 v[136:137], v161 offset:34816\n"
    "v_mfma_f32_16x16x32_bf16 v[28:31], v[144:147], v[96:99], v[28:31]\n"
    "ds_read_b64_tr_b16 v[138:139], v161 offset:43008\n"
    "v_mfma_f32_16x16x32_bf16 v[28:31], v[140:143], v[92:95], v[28:31]\n"
    "ds_read_b64_tr_b16 v[132:133], v161 offset:32768\n"
    "v_mfma_f32_16x16x32_bf16 v[44:47], v[140:143], v[100:103], v[44:47]\n"
    "ds_read_b128 v[88:91], v157 offset:0\n"
    "v_mfma_f32_16x16x32_bf16 v[44:47], v[144:147], v[104:107], v[44:47]\n"
    "ds_read_b128 v[84:87], v156 offset:0\n"
    "v_mfma_f32_16x16x32_bf16 v[48:51], v[152:155], v[104:107], v[48:51]\n"
    "ds_read_b64_tr_b16 v[134:135], v161 offset:40960\n"
    "v_mfma_f32_16x16x32_bf16 v[48:51], v[148:151], v[100:103], v[48:51]\n"
    "ds_read_b128 v[96:99], v157 offset:2048\n"
    "v_mfma_f32_16x16x32_bf16 v[64:67], v[148:151], v[108:111], v[64:67]\n"
    "ds_read_b128 v[92:95], v156 offset:2048\n"
    "v_mfma_f32_16x16x32_bf16 v[64:67], v[152:155], v[112:115], v[64:67]\n"
    "ds_read_b128 v[104:107], v157 offset:4096\n"
    "v_mfma_f32_16x16x32_bf16 v[60:63], v[144:147], v[112:115], v[60:63]\n"
    "ds_read_b128 v[100:103], v156 offset:4096\n"
    "v_mfma_f32_16x16x32_bf16 v[60:63], v[140:143], v[108:111], v[60:63]\n"
    "v_mfma_f32_16x16x32_bf16 v[76:79], v[140:143], v[116:119], v[76:79]\n"
    "v_mfma_f32_16x16x32_bf16 v[76:79], v[144:147], v[120:123], v[76:79]\n"
    "v_mfma_f32_16x16x32_bf16 v[80:83], v[152:155], v[120:123], v[80:83]\n"
    "s_waitcnt lgkmcnt(4)\n"
    "v_mfma_f32_16x16x32_bf16 v[80:83], v[148:151], v[116:119], v[80:83]\n"
    "s_barrier\n"
    "v_mfma_f32_16x16x32_bf16 v[4:7], v[124:127], v[84:87], v[4:7]\n"
    "ds_read_b128 v[112:115], v157 offset:6144\n"
    "v_mfma_f32_16x16x32_bf16 v[4:7], v[128:131], v[88:91], v[4:7]\n"
    "ds_read_b128 v[108:111], v156 offset:6144\n"
    "v_mfma_f32_16x16x32_bf16 v[8:11], v[136:139], v[88:91], v[8:11]\n"
    "ds_read_b64_tr_b16 v[140:141], v160 offset:49152\n"
    "v_mfma_f32_16x16x32_bf16 v[8:11], v[132:135], v[84:87], v[8:11]\n"
    "ds_read_b64_tr_b16 v[142:143], v160 offset:57344\n"
    "s_waitcnt lgkmcnt(6)\n"
    "v_mfma_f32_16x16x32_bf16 v[24:27], v[132:135], v[92:95], v[24:27]\n"
    "ds_read_b128 v[120:123], v157 offset:8192\n"
    "v_mfma_f32_16x16x32_bf16 v[24:27], v[136:139], v[96:99], v[24:27]\n"
    "ds_read_b128 v[116:119], v156 offset:8192\n"
    "v_mfma_f32_16x16x32_bf16 v[20:23], v[128:131], v[96:99], v[20:23]\n"
    "ds_read_b64_tr_b16 v[144:145], v160 offset:51200\n"
    "v_mfma_f32_16x16x32_bf16 v[20:23], v[124:127], v[92:95], v[20:23]\n"
    "ds_read_b64_tr_b16 v[146:147], v160 offset:59392\n"
    "s_waitcnt lgkmcnt(8)\n"
    "v_mfma_f32_16x16x32_bf16 v[36:39], v[124:127], v[100:103], v[36:39]\n"
    "ds_read_b64_tr_b16 v[152:153], v161 offset:51200\n"
    "v_mfma_f32_16x16x32_bf16 v[36:39], v[128:131], v[104:107], v[36:39]\n"
    "ds_read_b64_tr_b16 v[154:155], v161 offset:59392\n"
    "v_mfma_f32_16x16x32_bf16 v[40:43], v[136:139], v[104:107], v[40:43]\n"
    "ds_read_b64_tr_b16 v[148:149], v161 offset:49152\n"
    "v_mfma_f32_16x16x32_bf16 v[40:43], v[132:135], v[100:103], v[40:43]\n"
    "ds_read_b64_tr_b16 v[150:151], v161 offset:57344\n"
    "s_waitcnt lgkmcnt(10)\n"
    "v_mfma_f32_16x16x32_bf16 v[56:59], v[132:135], v[108:111], v[56:59]\n"
    "v_mfma_f32_16x16x32_bf16 v[56:59], v[136:139], v[112:115], v[56:59]\n"
    "v_mfma_f32_16x16x32_bf16 v[52:55], v[128:131], v[112:115], v[52:55]\n"
    "v_mfma_f32_16x16x32_bf16 v[52:55], v[124:127], v[108:111], v[52:55]\n"
    "s_waitcnt lgkmcnt(6)\n"
    "v_mfma_f32_16x16x32_bf16 v[68:71], v[124:127], v[116:119], v[68:71]\n"
    "v_mfma_f32_16x16x32_bf16 v[68:71], v[128:131], v[120:123], v[68:71]\n"
    "v_mfma_f32_16x16x32_bf16 v[72:75], v[136:139], v[120:123], v[72:75]\n"
    "s_waitcnt lgkmcnt(0)\n"
    "v_mfma_f32_16x16x32_bf16 v[72:75], v[132:135], v[116:119], v[72:75]\n"
    "s_barrier\n"
    "v_mfma_f32_16x16x32_bf16 v[12:15], v[140:143], v[84:87], v[12:15]\n"
    "ds_read_b64_tr_b16 v[124:125], v160 offset:0\n"
    "v_mfma_f32_16x16x32_bf16 v[12:15], v[144:147], v[88:91], v[12:15]\n"
    "ds_read_b64_tr_b16 v[126:127], v160 offset:8192\n"
    "v_mfma_f32_16x16x32_bf16 v[16:19], v[152:155], v[88:91], v[16:19]\n"
    "ds_read_b64_tr_b16 v[128:129], v160 offset:2048\n"
    "v_mfma_f32_16x16x32_bf16 v[16:19], v[148:151], v[84:87], v[16:19]\n"
    "ds_read_b64_tr_b16 v[130:131], v160 offset:10240\n"
    "v_mfma_f32_16x16x32_bf16 v[32:35], v[148:151], v[92:95], v[32:35]\n"
    "ds_read_b64_tr_b16 v[136:137], v161 offset:2048\n"
    "v_mfma_f32_16x16x32_bf16 v[32:35], v[152:155], v[96:99], v[32:35]\n"
    "ds_read_b64_tr_b16 v[138:139], v161 offset:10240\n"
    "v_mfma_f32_16x16x32_bf16 v[28:31], v[144:147], v[96:99], v[28:31]\n"
    "ds_read_b64_tr_b16 v[132:133], v161 offset:0\n"
    "v_mfma_f32_16x16x32_bf16 v[28:31], v[140:143], v[92:95], v[28:31]\n"
    "ds_read_b128 v[88:91], v157 offset:10240\n"
    "v_mfma_f32_16x16x32_bf16 v[44:47], v[140:143], v[100:103], v[44:47]\n"
    "ds_read_b128 v[84:87], v156 offset:10240\n"
    "v_mfma_f32_16x16x32_bf16 v[44:47], v[144:147], v[104:107], v[44:47]\n"
    "ds_read_b64_tr_b16 v[134:135], v161 offset:8192\n"
    "v_mfma_f32_16x16x32_bf16 v[48:51], v[152:155], v[104:107], v[48:51]\n"
    "ds_read_b128 v[96:99], v157 offset:12288\n"
    "v_mfma_f32_16x16x32_bf16 v[48:51], v[148:151], v[100:103], v[48:51]\n"
    "ds_read_b128 v[92:95], v156 offset:12288\n"
    "v_mfma_f32_16x16x32_bf16 v[64:67], v[148:151], v[108:111], v[64:67]\n"
    "ds_read_b128 v[104:107], v157 offset:14336\n"
    "v_mfma_f32_16x16x32_bf16 v[64:67], v[152:155], v[112:115], v[64:67]\n"
    "ds_read_b128 v[100:103], v156 offset:14336\n"
    "v_mfma_f32_16x16x32_bf16 v[60:63], v[144:147], v[112:115], v[60:63]\n"
    "v_mfma_f32_16x16x32_bf16 v[60:63], v[140:143], v[108:111], v[60:63]\n"
    "v_mfma_f32_16x16x32_bf16 v[76:79], v[140:143], v[116:119], v[76:79]\n"
    "v_mfma_f32_16x16x32_bf16 v[76:79], v[144:147], v[120:123], v[76:79]\n"
    "v_mfma_f32_16x16x32_bf16 v[80:83], v[152:155], v[120:123], v[80:83]\n"
    "s_waitcnt lgkmcnt(4)\n"
    "v_mfma_f32_16x16x32_bf16 v[80:83], v[148:151], v[116:119], v[80:83]\n"
    "s_barrier\n"
    "v_mfma_f32_16x16x32_bf16 v[4:7], v[124:127], v[84:87], v[4:7]\n"
    "ds_read_b128 v[112:115], v157 offset:16384\n"
    "v_mfma_f32_16x16x32_bf16 v[4:7], v[128:131], v[88:91], v[4:7]\n"
    "ds_read_b128 v[108:111], v156 offset:16384\n"
    "v_mfma_f32_16x16x32_bf16 v[8:11], v[136:139], v[88:91], v[8:11]\n"
    "ds_read_b64_tr_b16 v[140:141], v160 offset:16384\n"
    "v_mfma_f32_16x16x32_bf16 v[8:11], v[132:135], v[84:87], v[8:11]\n"
    "ds_read_b64_tr_b16 v[142:143], v160 offset:24576\n"
    "s_waitcnt lgkmcnt(6)\n"
    "v_mfma_f32_16x16x32_bf16 v[24:27], v[132:135], v[92:95], v[24:27]\n"
    "ds_read_b128 v[120:123], v157 offset:18432\n"
    "v_mfma_f32_16x16x32_bf16 v[24:27], v[136:139], v[96:99], v[24:27]\n"
    "ds_read_b128 v[116:119], v156 offset:18432\n"
    "v_mfma_f32_16x16x32_bf16 v[20:23], v[128:131], v[96:99], v[20:23]\n"
    "ds_read_b64_tr_b16 v[144:145], v160 offset:18432\n"
    "v_mfma_f32_16x16x32_bf16 v[20:23], v[124:127], v[92:95], v[20:23]\n"
    "ds_read_b64_tr_b16 v[146:147], v160 offset:26624\n"
    "s_waitcnt lgkmcnt(8)\n"
    "v_mfma_f32_16x16x32_bf16 v[36:39], v[124:127], v[100:103], v[36:39]\n"
    "ds_read_b64_tr_b16 v[152:153], v161 offset:18432\n"
    "v_mfma_f32_16x16x32_bf16 v[36:39], v[128:131], v[104:107], v[36:39]\n"
    "ds_read_b64_tr_b16 v[154:155], v161 offset:26624\n"
    "v_mfma_f32_16x16x32_bf16 v[40:43], v[136:139], v[104:107], v[40:43]\n"
    "ds_read_b64_tr_b16 v[148:149], v161 offset:16384\n"
    "v_mfma_f32_16x16x32_bf16 v[40:43], v[132:135], v[100:103], v[40:43]\n"
    "ds_read_b64_tr_b16 v[150:151], v161 offset:24576\n"
    "s_waitcnt lgkmcnt(10)\n"
    "v_mfma_f32_16x16x32_bf16 v[56:59], v[132:135], v[108:111], v[56:59]\n"
    "v_mfma_f32_16x16x32_bf16 v[56:59], v[136:139], v[112:115], v[56:59]\n"
    "v_mfma_f32_16x16x32_bf16 v[52:55], v[128:131], v[112:115], v[52:55]\n"
    "v_mfma_f32_16x16x32_bf16 v[52:55], v[124:127], v[108:111], v[52:55]\n"
    "s_waitcnt lgkmcnt(6)\n"
    "v_mfma_f32_16x16x32_bf16 v[68:71], v[124:127], v[116:119], v[68:71]\n"
    "v_mfma_f32_16x16x32_bf16 v[68:71], v[128:131], v[120:123], v[68:71]\n"
    "v_mfma_f32_16x16x32_bf16 v[72:75], v[136:139], v[120:123], v[72:75]\n"
    "s_waitcnt lgkmcnt(0)\n"
    "v_mfma_f32_16x16x32_bf16 v[72:75], v[132:135], v[116:119], v[72:75]\n"
    "s_barrier\n"
    "v_mfma_f32_16x16x32_bf16 v[12:15], v[140:143], v[84:87], v[12:15]\n"
    "ds_read_b64_tr_b16 v[124:125], v160 offset:32768\n"
    "v_mfma_f32_16x16x32_bf16 v[12:15], v[144:147], v[88:91], v[12:15]\n"
    "ds_read_b64_tr_b16 v[126:127], v160 offset:40960\n"
    "v_mfma_f32_16x16x32_bf16 v[16:19], v[152:155], v[88:91], v[16:19]\n"
    "ds_read_b64_tr_b16 v[128:129], v160 offset:34816\n"
    "v_mfma_f32_16x16x32_bf16 v[16:19], v[148:151], v[84:87], v[16:19]\n"
    "ds_read_b64_tr_b16 v[130:131], v160 offset:43008\n"
    "v_mfma_f32_16x16x32_bf16 v[32:35], v[148:151], v[92:95], v[32:35]\n"
    "ds_read_b64_tr_b16 v[136:137], v161 offset:34816\n"
    "v_mfma_f32_16x16x32_bf16 v[32:35], v[152:155], v[96:99], v[32:35]\n"
    "ds_read_b64_tr_b16 v[138:139], v161 offset:43008\n"
    "v_mfma_f32_16x16x32_bf16 v[28:31], v[144:147], v[96:99], v[28:31]\n"
    "ds_read_b64_tr_b16 v[132:133], v161 offset:32768\n"
    "v_mfma_f32_16x16x32_bf16 v[28:31], v[140:143], v[92:95], v[28:31]\n"
    "ds_read_b128 v[88:91], v157 offset:20480\n"
    "v_mfma_f32_16x16x32_bf16 v[44:47], v[140:143], v[100:103], v[44:47]\n"
    "ds_read_b128 v[84:87], v156 offset:20480\n"
    "v_mfma_f32_16x16x32_bf16 v[44:47], v[144:147], v[104:107], v[44:47]\n"
    "ds_read_b64_tr_b16 v[134:135], v161 offset:40960\n"
    "v_mfma_f32_16x16x32_bf16 v[48:51], v[152:155], v[104:107], v[48:51]\n"
    "ds_read_b128 v[96:99], v157 offset:22528\n"
    "v_mfma_f32_16x16x32_bf16 v[48:51], v[148:151], v[100:103], v[48:51]\n"
    "ds_read_b128 v[92:95], v156 offset:22528\n"
    "v_mfma_f32_16x16x32_bf16 v[64:67], v[148:151], v[108:111], v[64:67]\n"
    "ds_read_b128 v[104:107], v157 offset:24576\n"
    "v_mfma_f32_16x16x32_bf16 v[64:67], v[152:155], v[112:115], v[64:67]\n"
    "ds_read_b128 v[100:103], v156 offset:24576\n"
    "v_mfma_f32_16x16x32_bf16 v[60:63], v[144:147], v[112:115], v[60:63]\n"
    "v_mfma_f32_16x16x32_bf16 v[60:63], v[140:143], v[108:111], v[60:63]\n"
    "v_mfma_f32_16x16x32_bf16 v[76:79], v[140:143], v[116:119], v[76:79]\n"
    "v_mfma_f32_16x16x32_bf16 v[76:79], v[144:147], v[120:123], v[76:79]\n"
    "v_mfma_f32_16x16x32_bf16 v[80:83], v[152:155], v[120:123], v[80:83]\n"
    "s_waitcnt lgkmcnt(4)\n"
    "v_mfma_f32_16x16x32_bf16 v[80:83], v[148:151], v[116:119], v[80:83]\n"
    "s_barrier\n"
    "v_mfma_f32_16x16x32_bf16 v[4:7], v[124:127], v[84:87], v[4:7]\n"
    "ds_read_b128 v[112:115], v157 offset:26624\n"
    "v_mfma_f32_16x16x32_bf16 v[4:7], v[128:131], v[88:91], v[4:7]\n"
    "ds_read_b128 v[108:111], v156 offset:26624\n"
    "v_mfma_f32_16x16x32_bf16 v[8:11], v[136:139], v[88:91], v[8:11]\n"
    "ds_read_b64_tr_b16 v[140:141], v160 offset:49152\n"
    "v_mfma_f32_16x16x32_bf16 v[8:11], v[132:135], v[84:87], v[8:11]\n"
    "ds_read_b64_tr_b16 v[142:143], v160 offset:57344\n"
    "s_waitcnt lgkmcnt(6)\n"
    "v_mfma_f32_16x16x32_bf16 v[24:27], v[132:135], v[92:95], v[24:27]\n"
    "ds_read_b128 v[120:123], v157 offset:28672\n"
    "v_mfma_f32_16x16x32_bf16 v[24:27], v[136:139], v[96:99], v[24:27]\n"
    "ds_read_b128 v[116:119], v156 offset:28672\n"
    "v_mfma_f32_16x16x32_bf16 v[20:23], v[128:131], v[96:99], v[20:23]\n"
    "ds_read_b64_tr_b16 v[144:145], v160 offset:51200\n"
    "v_mfma_f32_16x16x32_bf16 v[20:23], v[124:127], v[92:95], v[20:23]\n"
    "ds_read_b64_tr_b16 v[146:147], v160 offset:59392\n"
    "s_waitcnt lgkmcnt(8)\n"
    "v_mfma_f32_16x16x32_bf16 v[36:39], v[124:127], v[100:103], v[36:39]\n"
    "ds_read_b64_tr_b16 v[152:153], v161 offset:51200\n"
    "v_mfma_f32_16x16x32_bf16 v[36:39], v[128:131], v[104:107], v[36:39]\n"
    "ds_read_b64_tr_b16 v[154:155], v161 offset:59392\n"
    "v_mfma_f32_16x16x32_bf16 v[40:43], v[136:139], v[104:107], v[40:43]\n"
    "ds_read_b64_tr_b16 v[148:149], v161 offset:49152\n"
    "v_mfma_f32_16x16x32_bf16 v[40:43], v[132:135], v[100:103], v[40:43]\n"
    "ds_read_b64_tr_b16 v[150:151], v161 offset:57344\n"
    "s_waitcnt lgkmcnt(10)\n"
    "v_mfma_f32_16x16x32_bf16 v[56:59], v[132:135], v[108:111], v[56:59]\n"
    "v_mfma_f32_16x16x32_bf16 v[56:59], v[136:139], v[112:115], v[56:59]\n"
    "v_mfma_f32_16x16x32_bf16 v[52:55], v[128:131], v[112:115], v[52:55]\n"
    "v_mfma_f32_16x16x32_bf16 v[52:55], v[124:127], v[108:111], v[52:55]\n"
    "s_waitcnt lgkmcnt(6)\n"
    "v_mfma_f32_16x16x32_bf16 v[68:71], v[124:127], v[116:119], v[68:71]\n"
    "v_mfma_f32_16x16x32_bf16 v[68:71], v[128:131], v[120:123], v[68:71]\n"
    "v_mfma_f32_16x16x32_bf16 v[72:75], v[136:139], v[120:123], v[72:75]\n"
    "s_waitcnt lgkmcnt(0)\n"
    "v_mfma_f32_16x16x32_bf16 v[72:75], v[132:135], v[116:119], v[72:75]\n"
    "s_barrier\n"
    "v_mfma_f32_16x16x32_bf16 v[12:15], v[140:143], v[84:87], v[12:15]\n"
    "ds_read_b64_tr_b16 v[124:125], v160 offset:0\n"
    "v_mfma_f32_16x16x32_bf16 v[12:15], v[144:147], v[88:91], v[12:15]\n"
    "ds_read_b64_tr_b16 v[126:127], v160 offset:8192\n"
    "v_mfma_f32_16x16x32_bf16 v[16:19], v[152:155], v[88:91], v[16:19]\n"
    "ds_read_b64_tr_b16 v[128:129], v160 offset:2048\n"
    "v_mfma_f32_16x16x32_bf16 v[16:19], v[148:151], v[84:87], v[16:19]\n"
    "ds_read_b64_tr_b16 v[130:131], v160 offset:10240\n"
    "v_mfma_f32_16x16x32_bf16 v[32:35], v[148:151], v[92:95], v[32:35]\n"
    "ds_read_b64_tr_b16 v[136:137], v161 offset:2048\n"
    "v_mfma_f32_16x16x32_bf16 v[32:35], v[152:155], v[96:99], v[32:35]\n"
    "ds_read_b64_tr_b16 v[138:139], v161 offset:10240\n"
    "v_mfma_f32_16x16x32_bf16 v[28:31], v[144:147], v[96:99], v[28:31]\n"
    "ds_read_b64_tr_b16 v[132:133], v161 offset:0\n"
    "v_mfma_f32_16x16x32_bf16 v[28:31], v[140:143], v[92:95], v[28:31]\n"
    "ds_read_b128 v[88:91], v157 offset:30720\n"
    "v_mfma_f32_16x16x32_bf16 v[44:47], v[140:143], v[100:103], v[44:47]\n"
    "ds_read_b128 v[84:87], v156 offset:30720\n"
    "v_mfma_f32_16x16x32_bf16 v[44:47], v[144:147], v[104:107], v[44:47]\n"
    "ds_read_b64_tr_b16 v[134:135], v161 offset:8192\n"
    "v_mfma_f32_16x16x32_bf16 v[48:51], v[152:155], v[104:107], v[48:51]\n"
    "ds_read_b128 v[96:99], v157 offset:32768\n"
    "v_mfma_f32_16x16x32_bf16 v[48:51], v[148:151], v[100:103], v[48:51]\n"
    "ds_read_b128 v[92:95], v156 offset:32768\n"
    "v_mfma_f32_16x16x32_bf16 v[64:67], v[148:151], v[108:111], v[64:67]\n"
    "ds_read_b128 v[104:107], v157 offset:34816\n"
    "v_mfma_f32_16x16x32_bf16 v[64:67], v[152:155], v[112:115], v[64:67]\n"
    "ds_read_b128 v[100:103], v156 offset:34816\n"
    "v_mfma_f32_16x16x32_bf16 v[60:63], v[144:147], v[112:115], v[60:63]\n"
    "v_mfma_f32_16x16x32_bf16 v[60:63], v[140:143], v[108:111], v[60:63]\n"
    "v_mfma_f32_16x16x32_bf16 v[76:79], v[140:143], v[116:119], v[76:79]\n"
    "v_mfma_f32_16x16x32_bf16 v[76:79], v[144:147], v[120:123], v[76:79]\n"
    "v_mfma_f32_16x16x32_bf16 v[80:83], v[152:155], v[120:123], v[80:83]\n"
    "s_waitcnt lgkmcnt(4)\n"
    "v_mfma_f32_16x16x32_bf16 v[80:83], v[148:151], v[116:119], v[80:83]\n"
    "s_barrier\n"
    "v_mfma_f32_16x16x32_bf16 v[4:7], v[124:127], v[84:87], v[4:7]\n"
    "ds_read_b128 v[112:115], v157 offset:36864\n"
    "v_mfma_f32_16x16x32_bf16 v[4:7], v[128:131], v[88:91], v[4:7]\n"
    "ds_read_b128 v[108:111], v156 offset:36864\n"
    "v_mfma_f32_16x16x32_bf16 v[8:11], v[136:139], v[88:91], v[8:11]\n"
    "ds_read_b64_tr_b16 v[140:141], v160 offset:16384\n"
    "v_mfma_f32_16x16x32_bf16 v[8:11], v[132:135], v[84:87], v[8:11]\n"
    "ds_read_b64_tr_b16 v[142:143], v160 offset:24576\n"
    "s_waitcnt lgkmcnt(6)\n"
    "v_mfma_f32_16x16x32_bf16 v[24:27], v[132:135], v[92:95], v[24:27]\n"
    "ds_read_b128 v[120:123], v157 offset:38912\n"
    "v_mfma_f32_16x16x32_bf16 v[24:27], v[136:139], v[96:99], v[24:27]\n"
    "ds_read_b128 v[116:119], v156 offset:38912\n"
    "v_mfma_f32_16x16x32_bf16 v[20:23], v[128:131], v[96:99], v[20:23]\n"
    "ds_read_b64_tr_b16 v[144:145], v160 offset:18432\n"
    "v_mfma_f32_16x16x32_bf16 v[20:23], v[124:127], v[92:95], v[20:23]\n"
    "ds_read_b64_tr_b16 v[146:147], v160 offset:26624\n"
    "s_waitcnt lgkmcnt(8)\n"
    "v_mfma_f32_16x16x32_bf16 v[36:39], v[124:127], v[100:103], v[36:39]\n"
    "ds_read_b64_tr_b16 v[152:153], v161 offset:18432\n"
    "v_mfma_f32_16x16x32_bf16 v[36:39], v[128:131], v[104:107], v[36:39]\n"
    "ds_read_b64_tr_b16 v[154:155], v161 offset:26624\n"
    "v_mfma_f32_16x16x32_bf16 v[40:43], v[136:139], v[104:107], v[40:43]\n"
    "ds_read_b64_tr_b16 v[148:149], v161 offset:16384\n"
    "v_mfma_f32_16x16x32_bf16 v[40:43], v[132:135], v[100:103], v[40:43]\n"
    "ds_read_b64_tr_b16 v[150:151], v161 offset:24576\n"
    "s_waitcnt lgkmcnt(10)\n"
    "v_mfma_f32_16x16x32_bf16 v[56:59], v[132:135], v[108:111], v[56:59]\n"
    "v_mfma_f32_16x16x32_bf16 v[56:59], v[136:139], v[112:115], v[56:59]\n"
    "v_mfma_f32_16x16x32_bf16 v[52:55], v[128:131], v[112:115], v[52:55]\n"
    "v_mfma_f32_16x16x32_bf16 v[52:55], v[124:127], v[108:111], v[52:55]\n"
    "s_waitcnt lgkmcnt(6)\n"
    "v_mfma_f32_16x16x32_bf16 v[68:71], v[124:127], v[116:119], v[68:71]\n"
    "v_mfma_f32_16x16x32_bf16 v[68:71], v[128:131], v[120:123], v[68:71]\n"
    "v_mfma_f32_16x16x32_bf16 v[72:75], v[136:139], v[120:123], v[72:75]\n"
    "s_waitcnt lgkmcnt(0)\n"
    "v_mfma_f32_16x16x32_bf16 v[72:75], v[132:135], v[116:119], v[72:75]\n"
    "s_barrier\n"
    "v_mfma_f32_16x16x32_bf16 v[12:15], v[140:143], v[84:87], v[12:15]\n"
    "ds_read_b128 v[124:127], v159 offset:0\n"
    "v_mfma_f32_16x16x32_bf16 v[12:15], v[144:147], v[88:91], v[12:15]\n"
    "ds_read_b128 v[128:131], v159 offset:64\n"
    "v_mfma_f32_16x16x32_bf16 v[16:19], v[152:155], v[88:91], v[16:19]\n"
    "ds_read_b128 v[132:135], v159 offset:512\n"
    "v_mfma_f32_16x16x32_bf16 v[16:19], v[148:151], v[84:87], v[16:19]\n"
    "ds_read_b128 v[136:139], v159 offset:576\n"
    "v_mfma_f32_16x16x32_bf16 v[32:35], v[148:151], v[92:95], v[32:35]\n"
    "v_mfma_f32_16x16x32_bf16 v[32:35], v[152:155], v[96:99], v[32:35]\n"
    "v_mfma_f32_16x16x32_bf16 v[28:31], v[144:147], v[96:99], v[28:31]\n"
    "v_mfma_f32_16x16x32_bf16 v[28:31], v[140:143], v[92:95], v[28:31]\n"
    "ds_read_b64 v[84:85], v158 offset:0\n"
    "v_mfma_f32_16x16x32_bf16 v[44:47], v[140:143], v[100:103], v[44:47]\n"
    "ds_read_b64 v[86:87], v158 offset:128\n"
    "v_mfma_f32_16x16x32_bf16 v[44:47], v[144:147], v[104:107], v[44:47]\n"
    "ds_read_b64 v[88:89], v158 offset:256\n"
    "v_mfma_f32_16x16x32_bf16 v[48:51], v[152:155], v[104:107], v[48:51]\n"
    "ds_read_b64 v[90:91], v158 offset:384\n"
    "v_mfma_f32_16x16x32_bf16 v[48:51], v[148:151], v[100:103], v[48:51]\n"
    "ds_read_b64 v[92:93], v158 offset:512\n"
    "v_mfma_f32_16x16x32_bf16 v[64:67], v[148:151], v[108:111], v[64:67]\n"
    "v_mfma_f32_16x16x32_bf16 v[64:67], v[152:155], v[112:115], v[64:67]\n"
    "v_mfma_f32_16x16x32_bf16 v[60:63], v[144:147], v[112:115], v[60:63]\n"
    "v_mfma_f32_16x16x32_bf16 v[60:63], v[140:143], v[108:111], v[60:63]\n"
    "v_mfma_f32_16x16x32_bf16 v[76:79], v[140:143], v[116:119], v[76:79]\n"
    "v_mfma_f32_16x16x32_bf16 v[76:79], v[144:147], v[120:123], v[76:79]\n"
    "v_mfma_f32_16x16x32_bf16 v[80:83], v[152:155], v[120:123], v[80:83]\n"
    "v_mfma_f32_16x16x32_bf16 v[80:83], v[148:151], v[116:119], v[80:83]\n"
    "s_waitcnt lgkmcnt(5)\n"
    "s_setprio 0\n"
    "s_barrier\n"
    "s_waitcnt lgkmcnt(0)\n"
    "s_lshl_b32 s30, s12, 11\n"
    "s_add_u32 s30, s30, 45056\n"
    "v_add_u32 v158, s30, v156\n"
    "v_add_u32 v159, s30, v157\n"
    "v_lshlrev_b32 v156, 4, v0\n"
    "v_add_u32 v157, 51200, v156\n"
    "s_waitcnt lgkmcnt(0)\n"
    "s_add_u32 s17, s17, 1\n"
    "s_xor_b32 s18, s17, 1540483477\n"
    "v_mov_b32 v164, s17\n"
    "v_mov_b32 v165, s18\n"
    "v_fma_f32 v104, v84, v4, v124\n"
    "v_fma_f32 v105, v84, v5, v125\n"
    "v_fma_f32 v106, v84, v6, v126\n"
    "v_fma_f32 v107, v84, v7, v127\n"
    "v_fma_f32 v108, v84, v8, v128\n"
    "v_fma_f32 v109, v84, v9, v129\n"
    "v_fma_f32 v110, v84, v10, v130\n"
    "v_fma_f32 v111, v84, v11, v131\n"
    "v_fmac_f32_dpp v104, v68, v85 row_newbcast:15 row_mask:0xf bank_mask:0xf\n"
    "v_fmac_f32_dpp v105, v69, v85 row_newbcast:15 row_mask:0xf bank_mask:0xf\n"
    "v_fmac_f32_dpp v106, v70, v85 row_newbcast:15 row_mask:0xf bank_mask:0xf\n"
    "v_fmac_f32_dpp v107, v71, v85 row_newbcast:15 row_mask:0xf bank_mask:0xf\n"
    "v_fmac_f32_dpp v108, v72, v85 row_newbcast:15 row_mask:0xf bank_mask:0xf\n"
    "v_fmac_f32_dpp v109, v73, v85 row_newbcast:15 row_mask:0xf bank_mask:0xf\n"
    "v_fmac_f32_dpp v110, v74, v85 row_newbcast:15 row_mask:0xf bank_mask:0xf\n"
    "v_fmac_f32_dpp v111, v75, v85 row_newbcast:15 row_mask:0xf bank_mask:0xf\n"
    "v_fma_f32 v112, -v104, v104, -s21\n"
    "v_fma_f32 v113, -v105, v105, -s21\n"
    "v_fma_f32 v114, -v106, v106, -s21\n"
    "v_fma_f32 v115, -v107, v107, -s21\n"
    "v_fma_f32 v116, -v108, v108, -s21\n"
    "v_fma_f32 v117, -v109, v109, -s21\n"
    "v_fma_f32 v118, -v110, v110, -s21\n"
    "v_fma_f32 v119, -v111, v111, -s21\n"
    "v_mul_f32 v112, v104, v112\n"
    "v_mul_f32 v113, v105, v113\n"
    "v_mul_f32 v114, v106, v114\n"
    "v_mul_f32 v115, v107, v115\n"
    "v_mul_f32 v116, v108, v116\n"
    "v_mul_f32 v117, v109, v117\n"
    "v_mul_f32 v118, v110, v118\n"
    "v_mul_f32 v119, v111, v119\n"
    "s_barrier\n"
    "v_exp_f32 v112, v112\n"
    "v_exp_f32 v113, v113\n"
    "v_exp_f32 v114, v114\n"
    "v_exp_f32 v115, v115\n"
    "v_exp_f32 v116, v116\n"
    "v_exp_f32 v117, v117\n"
    "v_exp_f32 v118, v118\n"
    "v_exp_f32 v119, v119\n"
    "v_fma_f32 v112, v112, s20, s20\n"
    "v_fma_f32 v113, v113, s20, s20\n"
    "v_fma_f32 v114, v114, s20, s20\n"
    "v_fma_f32 v115, v115, s20, s20\n"
    "v_fma_f32 v116, v116, s20, s20\n"
    "v_fma_f32 v117, v117, s20, s20\n"
    "v_fma_f32 v118, v118, s20, s20\n"
    "v_fma_f32 v119, v119, s20, s20\n"
    "v_rcp_f32 v112, v112\n"
    "v_rcp_f32 v113, v113\n"
    "v_rcp_f32 v114, v114\n"
    "v_rcp_f32 v115, v115\n"
    "v_rcp_f32 v116, v116\n"
    "v_rcp_f32 v117, v117\n"
    "v_rcp_f32 v118, v118\n"
    "s_barrier\n"
    "v_rcp_f32 v119, v119\n"
    "v_mul_f32 v104, v104, v112\n"
    "v_mul_f32 v105, v105, v113\n"
    "v_mul_f32 v106, v106, v114\n"
    "v_mul_f32 v107, v107, v115\n"
    "v_mul_f32 v108, v108, v116\n"
    "v_mul_f32 v109, v109, v117\n"
    "v_mul_f32 v110, v110, v118\n"
    "v_mul_f32 v111, v111, v119\n"
    "v_cvt_pk_bf16_f32 v4, v104, v105\n"
    "v_cvt_pk_bf16_f32 v5, v106, v107\n"
    "v_cvt_pk_bf16_f32 v6, v108, v109\n"
    "v_cvt_pk_bf16_f32 v7, v110, v111\n"
    "v_fma_f32 v104, v86, v20, v124\n"
    "v_fma_f32 v105, v86, v21, v125\n"
    "v_fma_f32 v106, v86, v22, v126\n"
    "v_fma_f32 v107, v86, v23, v127\n"
    "v_fma_f32 v108, v86, v24, v128\n"
    "v_fma_f32 v109, v86, v25, v129\n"
    "v_fma_f32 v110, v86, v26, v130\n"
    "v_fma_f32 v111, v86, v27, v131\n"
    "v_fmac_f32_dpp v104, v68, v87 row_newbcast:15 row_mask:0xf bank_mask:0xf\n"
    "v_fmac_f32_dpp v105, v69, v87 row_newbcast:15 row_mask:0xf bank_mask:0xf\n"
    "v_fmac_f32_dpp v106, v70, v87 row_newbcast:15 row_mask:0xf bank_mask:0xf\n"
    "v_fmac_f32_dpp v107, v71, v87 row_newbcast:15 row_mask:0xf bank_mask:0xf\n"
    "v_fmac_f32_dpp v108, v72, v87 row_newbcast:15 row_mask:0xf bank_mask:0xf\n"
    "v_fmac_f32_dpp v109, v73, v87 row_newbcast:15 row_mask:0xf bank_mask:0xf\n"
    "v_fmac_f32_dpp v110, v74, v87 row_newbcast:15 row_mask:0xf bank_mask:0xf\n"
    "v_fmac_f32_dpp v111, v75, v87 row_newbcast:15 row_mask:0xf bank_mask:0xf\n"
    "v_fma_f32 v112, -v104, v104, -s21\n"
    "v_fma_f32 v113, -v105, v105, -s21\n"
    "v_fma_f32 v114, -v106, v106, -s21\n"
    "v_fma_f32 v115, -v107, v107, -s21\n"
    "v_fma_f32 v116, -v108, v108, -s21\n"
    "v_fma_f32 v117, -v109, v109, -s21\n"
    "v_fma_f32 v118, -v110, v110, -s21\n"
    "v_fma_f32 v119, -v111, v111, -s21\n"
    "s_barrier\n"
    "v_mul_f32 v112, v104, v112\n"
    "v_mul_f32 v113, v105, v113\n"
    ".Lpage11:\n"
    "s_cbranch_execz .Ltouch_next\n"
    "v_mul_f32 v114, v106, v114\n"
    "v_mul_f32 v115, v107, v115\n"
    "v_mul_f32 v116, v108, v116\n"
    "v_mul_f32 v117, v109, v117\n"
    "v_mul_f32 v118, v110, v118\n"
    "v_mul_f32 v119, v111, v119\n"
    "v_exp_f32 v112, v112\n"
    "v_exp_f32 v113, v113\n"
    "v_exp_f32 v114, v114\n"
    "v_exp_f32 v115, v115\n"
    "v_exp_f32 v116, v116\n"
    "v_exp_f32 v117, v117\n"
    "v_exp_f32 v118, v118\n"
    "v_exp_f32 v119, v119\n"
    "v_fma_f32 v112, v112, s20, s20\n"
    "v_fma_f32 v113, v113, s20, s20\n"
    "v_fma_f32 v114, v114, s20, s20\n"
    "v_fma_f32 v115, v115, s20, s20\n"
    "v_fma_f32 v116, v116, s20, s20\n"
    "v_fma_f32 v117, v117, s20, s20\n"
    "v_fma_f32 v118, v118, s20, s20\n"
    "v_fma_f32 v119, v119, s20, s20\n"
    "v_rcp_f32 v112, v112\n"
    "v_rcp_f32 v113, v113\n"
    "v_rcp_f32 v114, v114\n"
    "s_barrier\n"
    "v_rcp_f32 v115, v115\n"
    "v_rcp_f32 v116, v116\n"
    "v_rcp_f32 v117, v117\n"
    "v_rcp_f32 v118, v118\n"
    "v_rcp_f32 v119, v119\n"
    "v_mul_f32 v104, v104, v112\n"
    "v_mul_f32 v105, v105, v113\n"
    "v_mul_f32 v106, v106, v114\n"
    "v_mul_f32 v107, v107, v115\n"
    "v_mul_f32 v108, v108, v116\n"
    "v_mul_f32 v109, v109, v117\n"
    "v_mul_f32 v110, v110, v118\n"
    "v_mul_f32 v111, v111, v119\n"
    "v_cvt_pk_bf16_f32 v20, v104, v105\n"
    "v_cvt_pk_bf16_f32 v21, v106, v107\n"
    "v_cvt_pk_bf16_f32 v22, v108, v109\n"
    "v_cvt_pk_bf16_f32 v23, v110, v111\n"
    "v_fma_f32 v104, v88, v36, v124\n"
    "v_fma_f32 v105, v88, v37, v125\n"
    "v_fma_f32 v106, v88, v38, v126\n"
    "v_fma_f32 v107, v88, v39, v127\n"
    "v_fma_f32 v108, v88, v40, v128\n"
    "v_fma_f32 v109, v88, v41, v129\n"
    "v_fma_f32 v110, v88, v42, v130\n"
    "v_fma_f32 v111, v88, v43, v131\n"
    "v_fmac_f32_dpp v104, v68, v89 row_newbcast:15 row_mask:0xf bank_mask:0xf\n"
    "v_fmac_f32_dpp v105, v69, v89 row_newbcast:15 row_mask:0xf bank_mask:0xf\n"
    "v_fmac_f32_dpp v106, v70, v89 row_newbcast:15 row_mask:0xf bank_mask:0xf\n"
    "v_fmac_f32_dpp v107, v71, v89 row_newbcast:15 row_mask:0xf bank_mask:0xf\n"
    "v_fmac_f32_dpp v108, v72, v89 row_newbcast:15 row_mask:0xf bank_mask:0xf\n"
    "v_fmac_f32_dpp v109, v73, v89 row_newbcast:15 row_mask:0xf bank_mask:0xf\n"
    "v_fmac_f32_dpp v110, v74, v89 row_newbcast:15 row_mask:0xf bank_mask:0xf\n"
    "v_fmac_f32_dpp v111, v75, v89 row_newbcast:15 row_mask:0xf bank_mask:0xf\n"
    "s_barrier\n"
    "v_fma_f32 v112, -v104, v104, -s21\n"
    "v_fma_f32 v113, -v105, v105, -s21\n"
    "v_fma_f32 v114, -v106, v106, -s21\n"
    "v_fma_f32 v115, -v107, v107, -s21\n"
    "v_fma_f32 v116, -v108, v108, -s21\n"
    "v_fma_f32 v117, -v109, v109, -s21\n"
    "v_fma_f32 v118, -v110, v110, -s21\n"
    "v_fma_f32 v119, -v111, v111, -s21\n"
    "v_mul_f32 v112, v104, v112\n"
    "v_mul_f32 v113, v105, v113\n"
    "v_mul_f32 v114, v106, v114\n"
    "v_mul_f32 v115, v107, v115\n"
    "v_mul_f32 v116, v108, v116\n"
    "v_mul_f32 v117, v109, v117\n"
    "v_mul_f32 v118, v110, v118\n"
    "v_mul_f32 v119, v111, v119\n"
    "v_exp_f32 v112, v112\n"
    "v_exp_f32 v113, v113\n"
    "v_exp_f32 v114, v114\n"
    "v_exp_f32 v115, v115\n"
    "v_exp_f32 v116, v116\n"
    "v_exp_f32 v117, v117\n"
    "v_exp_f32 v118, v118\n"
    "v_exp_f32 v119, v119\n"
    "v_fma_f32 v112, v112, s20, s20\n"
    "v_fma_f32 v113, v113, s20, s20\n"
    "v_fma_f32 v114, v114, s20, s20\n"
    "v_fma_f32 v115, v115, s20, s20\n"
    "v_fma_f32 v116, v116, s20, s20\n"
    "v_fma_f32 v117, v117, s20, s20\n"
    "s_barrier\n"
    "v_fma_f32 v118, v118, s20, s20\n"
    "v_fma_f32 v119, v119, s20, s20\n"
    "v_rcp_f32 v112, v112\n"
    "v_rcp_f32 v113, v113\n"
    "v_rcp_f32 v114, v114\n"
    "v_rcp_f32 v115, v115\n"
    "v_rcp_f32 v116, v116\n"
    "v_rcp_f32 v117, v117\n"
    "v_rcp_f32 v118, v118\n"
    "v_rcp_f32 v119, v119\n"
    "v_mul_f32 v104, v104, v112\n"
    "v_mul_f32 v105, v105, v113\n"
    "v_mul_f32 v106, v106, v114\n"
    "v_mul_f32 v107, v107, v115\n"
    "v_mul_f32 v108, v108, v116\n"
    "v_mul_f32 v109, v109, v117\n"
    "v_mul_f32 v110, v110, v118\n"
    "v_mul_f32 v111, v111, v119\n"
    "v_cvt_pk_bf16_f32 v36, v104, v105\n"
    "v_cvt_pk_bf16_f32 v37, v106, v107\n"
    "v_cvt_pk_bf16_f32 v38, v108, v109\n"
    "v_cvt_pk_bf16_f32 v39, v110, v111\n"
    "v_fma_f32 v104, v90, v52, v124\n"
    "v_fma_f32 v105, v90, v53, v125\n"
    "v_fma_f32 v106, v90, v54, v126\n"
    "v_fma_f32 v107, v90, v55, v127\n"
    "v_fma_f32 v108, v90, v56, v128\n"
    "v_fma_f32 v109, v90, v57, v129\n"
    "v_fma_f32 v110, v90, v58, v130\n"
    "v_fma_f32 v111, v90, v59, v131\n"
    "s_barrier\n"
    "v_fmac_f32_dpp v104, v68, v91 row_newbcast:15 row_mask:0xf bank_mask:0xf\n"
    "v_fmac_f32_dpp v105, v69, v91 row_newbcast:15 row_mask:0xf bank_mask:0xf\n"
    "v_fmac_f32_dpp v106, v70, v91 row_newbcast:15 row_mask:0xf bank_mask:0xf\n"
    "v_fmac_f32_dpp v107, v71, v91 row_newbcast:15 row_mask:0xf bank_mask:0xf\n"
    "v_fmac_f32_dpp v108, v72, v91 row_newbcast:15 row_mask:0xf bank_mask:0xf\n"
    "v_fmac_f32_dpp v109, v73, v91 row_newbcast:15 row_mask:0xf bank_mask:0xf\n"
    "v_fmac_f32_dpp v110, v74, v91 row_newbcast:15 row_mask:0xf bank_mask:0xf\n"
    "v_fmac_f32_dpp v111, v75, v91 row_newbcast:15 row_mask:0xf bank_mask:0xf\n"
    "v_fma_f32 v112, -v104, v104, -s21\n"
    "v_fma_f32 v113, -v105, v105, -s21\n"
    "v_fma_f32 v114, -v106, v106, -s21\n"
    "v_fma_f32 v115, -v107, v107, -s21\n"
    "v_fma_f32 v116, -v108, v108, -s21\n"
    "v_fma_f32 v117, -v109, v109, -s21\n"
    "v_fma_f32 v118, -v110, v110, -s21\n"
    "v_fma_f32 v119, -v111, v111, -s21\n"
    "v_mul_f32 v112, v104, v112\n"
    "v_mul_f32 v113, v105, v113\n"
    "v_mul_f32 v114, v106, v114\n"
    "v_mul_f32 v115, v107, v115\n"
    "v_mul_f32 v116, v108, v116\n"
    "v_mul_f32 v117, v109, v117\n"
    "v_mul_f32 v118, v110, v118\n"
    "v_mul_f32 v119, v111, v119\n"
    "v_exp_f32 v112, v112\n"
    "v_exp_f32 v113, v113\n"
    "v_exp_f32 v114, v114\n"
    "v_exp_f32 v115, v115\n"
    "v_exp_f32 v116, v116\n"
    "v_exp_f32 v117, v117\n"
    "v_exp_f32 v118, v118\n"
    "s_barrier\n"
    "v_exp_f32 v119, v119\n"
    "v_fma_f32 v112, v112, s20, s20\n"
    "v_fma_f32 v113, v113, s20, s20\n"
    "v_fma_f32 v114, v114, s20, s20\n"
    "v_fma_f32 v115, v115, s20, s20\n"
    "v_fma_f32 v116, v116, s20, s20\n"
    "v_fma_f32 v117, v117, s20, s20\n"
    "v_fma_f32 v118, v118, s20, s20\n"
    "v_fma_f32 v119, v119, s20, s20\n"
    "v_rcp_f32 v112, v112\n"
    "v_rcp_f32 v113, v113\n"
    "v_rcp_f32 v114, v114\n"
    "v_rcp_f32 v115, v115\n"
    "v_rcp_f32 v116, v116\n"
    "v_rcp_f32 v117, v117\n"
    "v_rcp_f32 v118, v118\n"
    "v_rcp_f32 v119, v119\n"
    "v_mul_f32 v104, v104, v112\n"
    "v_mul_f32 v105, v105, v113\n"
    "v_mul_f32 v106, v106, v114\n"
    "v_mul_f32 v107, v107, v115\n"
    "v_mul_f32 v108, v108, v116\n"
    "v_mul_f32 v109, v109, v117\n"
    "v_mul_f32 v110, v110, v118\n"
    "v_mul_f32 v111, v111, v119\n"
    "v_cvt_pk_bf16_f32 v52, v104, v105\n"
    "v_cvt_pk_bf16_f32 v53, v106, v107\n"
    "v_cvt_pk_bf16_f32 v54, v108, v109\n"
    "v_cvt_pk_bf16_f32 v55, v110, v111\n"
    "s_barrier\n"
    "s_setprio 3\n"
    "s_add_u32 s25, s16, 2\n"
    "s_sub_u32 s19, s25, 9\n"
    "s_cmp_ge_u32 s25, 9\n"
    "s_cselect_b32 s25, s19, s25\n"
    "s_mul_i32 s25, s25, 589824\n"
    "s_add_u32 s25, s25, s26\n"
    "s_add_u32 m0, s27, 49152\n"
    "s_add_u32 s19, s25, 512\n"
    "buffer_load_dwordx4 v2, s[72:75], s19 offen offset:0 lds\n"
    "buffer_load_dwordx4 v3, s[72:75], s19 offen offset:1024 lds\n"
    "s_add_u32 s28, s11, s14\n"
    "s_add_u32 s28, s28, 1\n"
    "s_sub_u32 s19, s28, 9\n"
    "s_cmp_ge_u32 s28, 9\n"
    "s_cselect_b32 s28, s19, s28\n"
    "s_lshl_b32 s19, s11, 4\n"
    "s_add_u32 s19, s19, s15\n"
    "s_mul_i32 s19, s19, 36864\n"
    "s_lshl_b32 s29, s28, 7\n"
    "s_add_u32 s92, s19, s29\n"
    "s_add_u32 m0, s27, 57344\n"
    "s_add_u32 s19, s25, 37376\n"
    "buffer_load_dwordx4 v2, s[72:75], s19 offen offset:0 lds\n"
    "buffer_load_dwordx4 v3, s[72:75], s19 offen offset:1024 lds\n"
    "s_setprio 0\n"
    "v_fma_f32 v104, v92, v68, v124\n"
    "v_fma_f32 v105, v92, v69, v125\n"
    "v_fma_f32 v106, v92, v70, v126\n"
    "v_fma_f32 v107, v92, v71, v127\n"
    "v_fma_f32 v108, v92, v72, v128\n"
    "v_fma_f32 v109, v92, v73, v129\n"
    "v_fma_f32 v110, v92, v74, v130\n"
    "v_fma_f32 v111, v92, v75, v131\n"
    "v_fmac_f32_dpp v104, v68, v93 row_newbcast:15 row_mask:0xf bank_mask:0xf\n"
    "v_fmac_f32_dpp v105, v69, v93 row_newbcast:15 row_mask:0xf bank_mask:0xf\n"
    "v_fmac_f32_dpp v106, v70, v93 row_newbcast:15 row_mask:0xf bank_mask:0xf\n"
    "v_fmac_f32_dpp v107, v71, v93 row_newbcast:15 row_mask:0xf bank_mask:0xf\n"
    "v_fmac_f32_dpp v108, v72, v93 row_newbcast:15 row_mask:0xf bank_mask:0xf\n"
    "s_barrier\n"
    "s_setprio 3\n"
    "s_add_u32 m0, s27, 0\n"
    "s_add_u32 s19, s25, 768\n"
    "buffer_load_dwordx4 v2, s[72:75], s19 offen offset:0 lds\n"
    "buffer_load_dwordx4 v3, s[72:75], s19 offen offset:1024 lds\n"
    "s_add_u32 s28, s11, s14\n"
    "s_add_u32 s28, s28, 3\n"
    "s_sub_u32 s19, s28, 9\n"
    "s_cmp_ge_u32 s28, 9\n"
    "s_cselect_b32 s28, s19, s28\n"
    "s_lshl_b32 s19, s11, 4\n"
    "s_add_u32 s19, s19, s15\n"
    "s_mul_i32 s19, s19, 36864\n"
    "s_lshl_b32 s29, s28, 7\n"
    "s_add_u32 s93, s19, s29\n"
    "s_add_u32 m0, s27, 8192\n"
    "s_add_u32 s19, s25, 37632\n"
    "buffer_load_dwordx4 v2, s[72:75], s19 offen offset:0 lds\n"
    "buffer_load_dwordx4 v3, s[72:75], s19 offen offset:1024 lds\n"
    "s_setprio 0\n"
    "v_fmac_f32_dpp v109, v73, v93 row_newbcast:15 row_mask:0xf bank_mask:0xf\n"
    "v_fmac_f32_dpp v110, v74, v93 row_newbcast:15 row_mask:0xf bank_mask:0xf\n"
    "v_fmac_f32_dpp v111, v75, v93 row_newbcast:15 row_mask:0xf bank_mask:0xf\n"
    "v_fma_f32 v112, -v104, v104, -s21\n"
    "v_fma_f32 v113, -v105, v105, -s21\n"
    "v_fma_f32 v114, -v106, v106, -s21\n"
    "v_fma_f32 v115, -v107, v107, -s21\n"
    "v_fma_f32 v116, -v108, v108, -s21\n"
    "v_fma_f32 v117, -v109, v109, -s21\n"
    "v_fma_f32 v118, -v110, v110, -s21\n"
    "v_fma_f32 v119, -v111, v111, -s21\n"
    "v_mul_f32 v112, v104, v112\n"
    "v_mul_f32 v113, v105, v113\n"
    "s_barrier\n"
    "s_setprio 3\n"
    "s_add_u32 s25, s16, 3\n"
    "s_sub_u32 s19, s25, 9\n"
    "s_cmp_ge_u32 s25, 9\n"
    "s_cselect_b32 s25, s19, s25\n"
    "s_mul_i32 s25, s25, 589824\n"
    "s_add_u32 s25, s25, s26\n"
    "s_add_u32 m0, s27, 16384\n"
    "s_add_u32 s19, s25, 512\n"
    "buffer_load_dwordx4 v2, s[72:75], s19 offen offset:0 lds\n"
    "buffer_load_dwordx4 v3, s[72:75], s19 offen offset:1024 lds\n"
    "s_mov_b32 s28, s11\n"
    "s_lshl_b32 s19, s11, 4\n"
    "s_add_u32 s19, s19, s12\n"
    "s_mul_i32 s19, s19, 36864\n"
    "s_lshl_b32 s29, s28, 7\n"
    "s_add_u32 s94, s19, s29\n"
    "s_add_u32 m0, s27, 24576\n"
    "s_add_u32 s19, s25, 37376\n"
    "buffer_load_dwordx4 v2, s[72:75], s19 offen offset:0 lds\n"
    "buffer_load_dwordx4 v3, s[72:75], s19 offen offset:1024 lds\n"
    "s_setprio 0\n"
    "v_mul_f32 v114, v106, v114\n"
    "v_mul_f32 v115, v107, v115\n"
    "v_mul_f32 v116, v108, v116\n"
    "v_mul_f32 v117, v109, v117\n"
    "v_mul_f32 v118, v110, v118\n"
    "v_mul_f32 v119, v111, v119\n"
    "v_exp_f32 v112, v112\n"
    "v_exp_f32 v113, v113\n"
    "v_exp_f32 v114, v114\n"
    "s_waitcnt vmcnt(8)\n"
    "s_barrier\n"
    "s_setprio 3\n"
    "s_add_u32 m0, s27, 32768\n"
    "s_add_u32 s19, s25, 768\n"
    "buffer_load_dwordx4 v2, s[72:75], s19 offen offset:0 lds\n"
    "buffer_load_dwordx4 v3, s[72:75], s19 offen offset:1024 lds\n"
    "s_nop 7\n"
    "ds_write_b128 v1, v[4:7] offset:0\n"
    "s_mov_b32 s80, s58\n"
    "s_and_b32 s81, s59, 0xffff\n"
    "s_mov_b32 s82, 5308416\n"
    "s_mov_b32 s83, 0x20000\n"
    "s_add_u32 m0, s27, 40960\n"
    "s_add_u32 s19, s25, 37632\n"
    "buffer_load_dwordx4 v2, s[72:75], s19 offen offset:0 lds\n"
    "buffer_load_dwordx4 v3, s[72:75], s19 offen offset:1024 lds\n"
    "s_setprio 0\n"
    "v_exp_f32 v115, v115\n"
    "v_exp_f32 v116, v116\n"
    "v_exp_f32 v117, v117\n"
    "v_exp_f32 v118, v118\n"
    "v_exp_f32 v119, v119\n"
    "v_fma_f32 v112, v112, s20, s20\n"
    "v_fma_f32 v113, v113, s20, s20\n"
    "v_fma_f32 v114, v114, s20, s20\n"
    "s_waitcnt vmcnt(8)\n"
    "s_barrier\n"
    "s_setprio 3\n"
    "s_add_u32 s25, s16, 4\n"
    "s_sub_u32 s19, s25, 9\n"
    "s_cmp_ge_u32 s25, 9\n"
    "s_cselect_b32 s25, s19, s25\n"
    "s_mul_i32 s25, s25, 589824\n"
    "s_add_u32 s25, s25, s26\n"
    "s_add_u32 m0, s27, 49152\n"
    "s_add_u32 s19, s25, 512\n"
    "buffer_load_dwordx4 v2, s[72:75], s19 offen offset:0 lds\n"
    "buffer_load_dwordx4 v3, s[72:75], s19 offen offset:1024 lds\n"
    "s_nop 7\n"
    "ds_write_b128 v1, v[20:23] offset:1024\n"
    "s_add_u32 m0, s27, 57344\n"
    "s_add_u32 s19, s25, 37376\n"
    "buffer_load_dwordx4 v2, s[72:75], s19 offen offset:0 lds\n"
    "buffer_load_dwordx4 v3, s[72:75], s19 offen offset:1024 lds\n"
    "s_setprio 0\n"
    "v_fma_f32 v115, v115, s20, s20\n"
    "v_fma_f32 v116, v116, s20, s20\n"
    "v_fma_f32 v117, v117, s20, s20\n"
    "v_fma_f32 v118, v118, s20, s20\n"
    "v_fma_f32 v119, v119, s20, s20\n"
    "v_rcp_f32 v112, v112\n"
    "v_rcp_f32 v113, v113\n"
    "v_rcp_f32 v114, v114\n"
    "v_rcp_f32 v115, v115\n"
    "v_rcp_f32 v116, v116\n"
    "v_rcp_f32 v117, v117\n"
    "v_rcp_f32 v118, v118\n"
    "v_rcp_f32 v119, v119\n"
    "v_mul_f32 v104, v104, v112\n"
    "v_mul_f32 v105, v105, v113\n"
    "v_mul_f32 v106, v106, v114\n"
    "v_mul_f32 v107, v107, v115\n"
    "v_mul_f32 v108, v108, v116\n"
    "v_mul_f32 v109, v109, v117\n"
    "s_waitcnt vmcnt(8)\n"
    "s_barrier\n"
    "v_mul_f32 v110, v110, v118\n"
    "v_mul_f32 v111, v111, v119\n"
    "v_cvt_pk_bf16_f32 v68, v104, v105\n"
    "v_cvt_pk_bf16_f32 v69, v106, v107\n"
    "v_cvt_pk_bf16_f32 v70, v108, v109\n"
    "v_cvt_pk_bf16_f32 v71, v110, v111\n"
    "v_fma_f32 v104, v84, v12, v132\n"
    "v_fma_f32 v105, v84, v13, v133\n"
    "v_fma_f32 v106, v84, v14, v134\n"
    "v_fma_f32 v107, v84, v15, v135\n"
    "v_fma_f32 v108, v84, v16, v136\n"
    "v_fma_f32 v109, v84, v17, v137\n"
    "v_fma_f32 v110, v84, v18, v138\n"
    "v_fma_f32 v111, v84, v19, v139\n"
    "v_fmac_f32_dpp v104, v76, v85 row_newbcast:15 row_mask:0xf bank_mask:0xf\n"
    "v_fmac_f32_dpp v105, v77, v85 row_newbcast:15 row_mask:0xf bank_mask:0xf\n"
    "v_fmac_f32_dpp v106, v78, v85 row_newbcast:15 row_mask:0xf bank_mask:0xf\n"
    "v_fmac_f32_dpp v107, v79, v85 row_newbcast:15 row_mask:0xf bank_mask:0xf\n"
    "v_fmac_f32_dpp v108, v80, v85 row_newbcast:15 row_mask:0xf bank_mask:0xf\n"
    "v_fmac_f32_dpp v109, v81, v85 row_newbcast:15 row_mask:0xf bank_mask:0xf\n"
    "v_fmac_f32_dpp v110, v82, v85 row_newbcast:15 row_mask:0xf bank_mask:0xf\n"
    "v_fmac_f32_dpp v111, v83, v85 row_newbcast:15 row_mask:0xf bank_mask:0xf\n"
    "v_fma_f32 v112, -v104, v104, -s21\n"
    "v_fma_f32 v113, -v105, v105, -s21\n"
    "v_fma_f32 v114, -v106, v106, -s21\n"
    "v_fma_f32 v115, -v107, v107, -s21\n"
    "v_fma_f32 v116, -v108, v108, -s21\n"
    "v_fma_f32 v117, -v109, v109, -s21\n"
    "v_fma_f32 v118, -v110, v110, -s21\n"
    "v_fma_f32 v119, -v111, v111, -s21\n"
    "v_mul_f32 v112, v104, v112\n"
    "ds_write_b128 v1, v[36:39] offset:2048\n"
    "v_mul_f32 v113, v105, v113\n"
    "v_mul_f32 v114, v106, v114\n"
    "v_mul_f32 v115, v107, v115\n"
    "v_mul_f32 v116, v108, v116\n"
    "v_mul_f32 v117, v109, v117\n"
    "v_mul_f32 v118, v110, v118\n"
    "v_mul_f32 v119, v111, v119\n"
    "v_exp_f32 v112, v112\n"
    "v_exp_f32 v113, v113\n"
    "v_exp_f32 v114, v114\n"
    "v_exp_f32 v115, v115\n"
    "v_exp_f32 v116, v116\n"
    "v_exp_f32 v117, v117\n"
    "s_waitcnt vmcnt(4)\n"
    "s_barrier\n"
    "v_exp_f32 v118, v118\n"
    "v_exp_f32 v119, v119\n"
    "v_fma_f32 v112, v112, s20, s20\n"
    "v_fma_f32 v113, v113, s20, s20\n"
    "v_fma_f32 v114, v114, s20, s20\n"
    "v_fma_f32 v115, v115, s20, s20\n"
    "v_fma_f32 v116, v116, s20, s20\n"
    "v_fma_f32 v117, v117, s20, s20\n"
    "v_fma_f32 v118, v118, s20, s20\n"
    "v_fma_f32 v119, v119, s20, s20\n"
    "v_rcp_f32 v112, v112\n"
    "v_rcp_f32 v113, v113\n"
    "v_rcp_f32 v114, v114\n"
    "v_rcp_f32 v115, v115\n"
    "v_rcp_f32 v116, v116\n"
    "v_rcp_f32 v117, v117\n"
    "v_rcp_f32 v118, v118\n"
    "v_rcp_f32 v119, v119\n"
    "ds_write_b128 v1, v[52:55] offset:3072\n"
    "v_mul_f32 v104, v104, v112\n"
    "v_mul_f32 v105, v105, v113\n"
    "v_mul_f32 v106, v106, v114\n"
    "v_mul_f32 v107, v107, v115\n"
    "v_mul_f32 v108, v108, v116\n"
    "v_mul_f32 v109, v109, v117\n"
    "v_mul_f32 v110, v110, v118\n"
    "v_mul_f32 v111, v111, v119\n"
    "v_cvt_pk_bf16_f32 v12, v104, v105\n"
    "v_cvt_pk_bf16_f32 v13, v106, v107\n"
    "v_cvt_pk_bf16_f32 v14, v108, v109\n"
    "v_cvt_pk_bf16_f32 v15, v110, v111\n"
    "v_fma_f32 v104, v86, v28, v132\n"
    "v_fma_f32 v105, v86, v29, v133\n"
    "v_fma_f32 v106, v86, v30, v134\n"
    "v_fma_f32 v107, v86, v31, v135\n"
    "v_fma_f32 v108, v86, v32, v136\n"
    "v_fma_f32 v109, v86, v33, v137\n"
    "v_fma_f32 v110, v86, v34, v138\n"
    "v_fma_f32 v111, v86, v35, v139\n"
    "v_fmac_f32_dpp v104, v76, v87 row_newbcast:15 row_mask:0xf bank_mask:0xf\n"
    "v_fmac_f32_dpp v105, v77, v87 row_newbcast:15 row_mask:0xf bank_mask:0xf\n"
    "v_fmac_f32_dpp v106, v78, v87 row_newbcast:15 row_mask:0xf bank_mask:0xf\n"
    "v_fmac_f32_dpp v107, v79, v87 row_newbcast:15 row_mask:0xf bank_mask:0xf\n"
    "v_fmac_f32_dpp v108, v80, v87 row_newbcast:15 row_mask:0xf bank_mask:0xf\n"
    "v_fmac_f32_dpp v109, v81, v87 row_newbcast:15 row_mask:0xf bank_mask:0xf\n"
    "v_fmac_f32_dpp v110, v82, v87 row_newbcast:15 row_mask:0xf bank_mask:0xf\n"
    "v_fmac_f32_dpp v111, v83, v87 row_newbcast:15 row_mask:0xf bank_mask:0xf\n"
    "v_fma_f32 v112, -v104, v104, -s21\n"
    "s_waitcnt vmcnt(0)\n"
    "s_barrier\n"
    "v_fma_f32 v113, -v105, v105, -s21\n"
    "v_fma_f32 v114, -v106, v106, -s21\n"
    "v_fma_f32 v115, -v107, v107, -s21\n"
    "v_fma_f32 v116, -v108, v108, -s21\n"
    "v_fma_f32 v117, -v109, v109, -s21\n"
    "v_fma_f32 v118, -v110, v110, -s21\n"
    "v_fma_f32 v119, -v111, v111, -s21\n"
    "v_mul_f32 v112, v104, v112\n"
    "v_mul_f32 v113, v105, v113\n"
    "v_mul_f32 v114, v106, v114\n"
    "v_mul_f32 v115, v107, v115\n"
    "v_mul_f32 v116, v108, v116\n"
    "v_mul_f32 v117, v109, v117\n"
    "v_mul_f32 v118, v110, v118\n"
    "v_mul_f32 v119, v111, v119\n"
    "v_exp_f32 v112, v112\n"
    "v_exp_f32 v113, v113\n"
    "v_exp_f32 v114, v114\n"
    "v_exp_f32 v115, v115\n"
    "v_exp_f32 v116, v116\n"
    "v_exp_f32 v117, v117\n"
    "v_exp_f32 v118, v118\n"
    "v_exp_f32 v119, v119\n"
    "ds_write_b128 v1, v[68:71] offset:4096\n"
    "v_fma_f32 v112, v112, s20, s20\n"
    "v_fma_f32 v113, v113, s20, s20\n"
    "v_fma_f32 v114, v114, s20, s20\n"
    "v_fma_f32 v115, v115, s20, s20\n"
    "v_fma_f32 v116, v116, s20, s20\n"
    "v_fma_f32 v117, v117, s20, s20\n"
    "v_fma_f32 v118, v118, s20, s20\n"
    "v_fma_f32 v119, v119, s20, s20\n"
    "v_rcp_f32 v112, v112\n"
    "v_rcp_f32 v113, v113\n"
    "v_rcp_f32 v114, v114\n"
    "v_rcp_f32 v115, v115\n"
    "v_rcp_f32 v116, v116\n"
    "v_rcp_f32 v117, v117\n"
    "v_rcp_f32 v118, v118\n"
    "v_rcp_f32 v119, v119\n"
    "v_mul_f32 v104, v104, v112\n"
    "v_mul_f32 v105, v105, v113\n"
    "s_barrier\n"
    "v_mul_f32 v106, v106, v114\n"
    "v_mul_f32 v107, v107, v115\n"
    "v_mul_f32 v108, v108, v116\n"
    "v_mul_f32 v109, v109, v117\n"
    "v_mul_f32 v110, v110, v118\n"
    "v_mul_f32 v111, v111, v119\n"
    "v_cvt_pk_bf16_f32 v28, v104, v105\n"
    "v_cvt_pk_bf16_f32 v29, v106, v107\n"
    "v_cvt_pk_bf16_f32 v30, v108, v109\n"
    "v_cvt_pk_bf16_f32 v31, v110, v111\n"
    "v_fma_f32 v104, v88, v44, v132\n"
    "v_fma_f32 v105, v88, v45, v133\n"
    "v_fma_f32 v106, v88, v46, v134\n"
    "v_fma_f32 v107, v88, v47, v135\n"
    "v_fma_f32 v108, v88, v48, v136\n"
    "v_fma_f32 v109, v88, v49, v137\n"
    "v_fma_f32 v110, v88, v50, v138\n"
    "v_fma_f32 v111, v88, v51, v139\n"
    "v_fmac_f32_dpp v104, v76, v89 row_newbcast:15 row_mask:0xf bank_mask:0xf\n"
    "v_fmac_f32_dpp v105, v77, v89 row_newbcast:15 row_mask:0xf bank_mask:0xf\n"
    "v_fmac_f32_dpp v106, v78, v89 row_newbcast:15 row_mask:0xf bank_mask:0xf\n"
    "v_fmac_f32_dpp v107, v79, v89 row_newbcast:15 row_mask:0xf bank_mask:0xf\n"
    "v_fmac_f32_dpp v108, v80, v89 row_newbcast:15 row_mask:0xf bank_mask:0xf\n"
    "v_fmac_f32_dpp v109, v81, v89 row_newbcast:15 row_mask:0xf bank_mask:0xf\n"
    "v_fmac_f32_dpp v110, v82, v89 row_newbcast:15 row_mask:0xf bank_mask:0xf\n"
    "v_fmac_f32_dpp v111, v83, v89 row_newbcast:15 row_mask:0xf bank_mask:0xf\n"
    "v_fma_f32 v112, -v104, v104, -s21\n"
    "ds_write_b128 v1, v[12:15] offset:20480\n"
    "v_fma_f32 v113, -v105, v105, -s21\n"
    "v_fma_f32 v114, -v106, v106, -s21\n"
    "v_fma_f32 v115, -v107, v107, -s21\n"
    "v_fma_f32 v116, -v108, v108, -s21\n"
    "v_fma_f32 v117, -v109, v109, -s21\n"
    "v_fma_f32 v118, -v110, v110, -s21\n"
    "v_fma_f32 v119, -v111, v111, -s21\n"
    "v_mul_f32 v112, v104, v112\n"
    "v_mul_f32 v113, v105, v113\n"
    "v_mul_f32 v114, v106, v114\n"
    "v_mul_f32 v115, v107, v115\n"
    "v_mul_f32 v116, v108, v116\n"
    "v_mul_f32 v117, v109, v117\n"
    "v_mul_f32 v118, v110, v118\n"
    "v_mul_f32 v119, v111, v119\n"
    "v_exp_f32 v112, v112\n"
    "v_exp_f32 v113, v113\n"
    "v_exp_f32 v114, v114\n"
    "v_exp_f32 v115, v115\n"
    "v_exp_f32 v116, v116\n"
    "v_exp_f32 v117, v117\n"
    "v_exp_f32 v118, v118\n"
    "s_barrier\n"
    "v_exp_f32 v119, v119\n"
    "v_fma_f32 v112, v112, s20, s20\n"
    "v_fma_f32 v113, v113, s20, s20\n"
    "v_fma_f32 v114, v114, s20, s20\n"
    "v_fma_f32 v115, v115, s20, s20\n"
    "v_fma_f32 v116, v116, s20, s20\n"
    "v_fma_f32 v117, v117, s20, s20\n"
    "v_fma_f32 v118, v118, s20, s20\n"
    "v_fma_f32 v119, v119, s20, s20\n"
    "v_rcp_f32 v112, v112\n"
    "v_rcp_f32 v113, v113\n"
    "v_rcp_f32 v114, v114\n"
    "v_rcp_f32 v115, v115\n"
    "v_rcp_f32 v116, v116\n"
    "v_rcp_f32 v117, v117\n"
    "v_rcp_f32 v118, v118\n"
    "v_rcp_f32 v119, v119\n"
    "v_mul_f32 v104, v104, v112\n"
    "v_mul_f32 v105, v105, v113\n"
    "v_mul_f32 v106, v106, v114\n"
    "v_mul_f32 v107, v107, v115\n"
    "v_mul_f32 v108, v108, v116\n"
    "v_mul_f32 v109, v109, v117\n"
    "v_mul_f32 v110, v110, v118\n"
    "ds_write_b128 v1, v[28:31] offset:21504\n"
    "v_mul_f32 v111, v111, v119\n"
    "v_cvt_pk_bf16_f32 v44, v104, v105\n"
    "v_cvt_pk_bf16_f32 v45, v106, v107\n"
    "v_cvt_pk_bf16_f32 v46, v108, v109\n"
    "v_cvt_pk_bf16_f32 v47, v110, v111\n"
    "v_fma_f32 v104, v90, v60, v132\n"
    "v_fma_f32 v105, v90, v61, v133\n"
    "v_fma_f32 v106, v90, v62, v134\n"
    "v_fma_f32 v107, v90, v63, v135\n"
    "v_fma_f32 v108, v90, v64, v136\n"
    "v_fma_f32 v109, v90, v65, v137\n"
    "v_fma_f32 v110, v90, v66, v138\n"
    "v_fma_f32 v111, v90, v67, v139\n"
    "v_fmac_f32_dpp v104, v76, v91 row_newbcast:15 row_mask:0xf bank_mask:0xf\n"
    "v_fmac_f32_dpp v105, v77, v91 row_newbcast:15 row_mask:0xf bank_mask:0xf\n"
    "v_fmac_f32_dpp v106, v78, v91 row_newbcast:15 row_mask:0xf bank_mask:0xf\n"
    "v_fmac_f32_dpp v107, v79, v91 row_newbcast:15 row_mask:0xf bank_mask:0xf\n"
    "v_fmac_f32_dpp v108, v80, v91 row_newbcast:15 row_mask:0xf bank_mask:0xf\n"
    "v_fmac_f32_dpp v109, v81, v91 row_newbcast:15 row_mask:0xf bank_mask:0xf\n"
    "v_fmac_f32_dpp v110, v82, v91 row_newbcast:15 row_mask:0xf bank_mask:0xf\n"
    "v_fmac_f32_dpp v111, v83, v91 row_newbcast:15 row_mask:0xf bank_mask:0xf\n"
    "v_fma_f32 v112, -v104, v104, -s21\n"
    "v_fma_f32 v113, -v105, v105, -s21\n"
    "v_fma_f32 v114, -v106, v106, -s21\n"
    "v_fma_f32 v115, -v107, v107, -s21\n"
    "s_barrier\n"
    "v_fma_f32 v116, -v108, v108, -s21\n"
    "v_fma_f32 v117, -v109, v109, -s21\n"
    "v_fma_f32 v118, -v110, v110, -s21\n"
    "v_fma_f32 v119, -v111, v111, -s21\n"
    "v_mul_f32 v112, v104, v112\n"
    "v_mul_f32 v113, v105, v113\n"
    "v_mul_f32 v114, v106, v114\n"
    "v_mul_f32 v115, v107, v115\n"
    "v_mul_f32 v116, v108, v116\n"
    "v_mul_f32 v117, v109, v117\n"
    "v_mul_f32 v118, v110, v118\n"
    "v_mul_f32 v119, v111, v119\n"
    "v_exp_f32 v112, v112\n"
    "v_exp_f32 v113, v113\n"
    "v_exp_f32 v114, v114\n"
    "v_exp_f32 v115, v115\n"
    "v_exp_f32 v116, v116\n"
    "v_exp_f32 v117, v117\n"
    "v_exp_f32 v118, v118\n"
    "v_exp_f32 v119, v119\n"
    "ds_write_b128 v1, v[44:47] offset:22528\n"
    "v_fma_f32 v112, v112, s20, s20\n"
    "v_fma_f32 v113, v113, s20, s20\n"
    "v_fma_f32 v114, v114, s20, s20\n"
    "v_fma_f32 v115, v115, s20, s20\n"
    "v_fma_f32 v116, v116, s20, s20\n"
    "v_fma_f32 v117, v117, s20, s20\n"
    "v_fma_f32 v118, v118, s20, s20\n"
    "v_fma_f32 v119, v119, s20, s20\n"
    "v_rcp_f32 v112, v112\n"
    "v_rcp_f32 v113, v113\n"
    "v_rcp_f32 v114, v114\n"
    "v_rcp_f32 v115, v115\n"
    "v_rcp_f32 v116, v116\n"
    "v_rcp_f32 v117, v117\n"
    "v_rcp_f32 v118, v118\n"
    "v_rcp_f32 v119, v119\n"
    "v_mul_f32 v104, v104, v112\n"
    "v_mul_f32 v105, v105, v113\n"
    "v_mul_f32 v106, v106, v114\n"
    "v_mul_f32 v107, v107, v115\n"
    "v_mul_f32 v108, v108, v116\n"
    "s_barrier\n"
    "s_setprio 3\n"
    "s_add_u32 s25, s16, 7\n"
    "s_sub_u32 s19, s25, 9\n"
    "s_cmp_ge_u32 s25, 9\n"
    "s_cselect_b32 s25, s19, s25\n"
    "s_mul_i32 s25, s25, 589824\n"
    "s_add_u32 s25, s25, s26\n"
    "s_add_u32 m0, s27, 32768\n"
    "s_add_u32 s19, s25, 768\n"
    "buffer_load_dwordx4 v2, s[72:75], s19 offen offset:0 lds\n"
    "buffer_load_dwordx4 v3, s[72:75], s19 offen offset:1024 lds\n"
    "s_add_u32 m0, s27, 40960\n"
    "s_add_u32 s19, s25, 37632\n"
    "buffer_load_dwordx4 v2, s[72:75], s19 offen offset:0 lds\n"
    "buffer_load_dwordx4 v3, s[72:75], s19 offen offset:1024 lds\n"
    "v_bfe_u32 v98, v0, 1, 2\n"
    "v_lshrrev_b32 v99, 4, v0\n"
    "v_xor_b32 v98, v98, v99\n"
    "v_and_b32 v99, 1, v0\n"
    "v_lshlrev_b32 v99, 4, v99\n"
    "v_lshl_add_u32 v96, v98, 5, v99\n"
    "v_lshrrev_b32 v98, 3, v0\n"
    "v_mul_u32_u24 v98, 1152, v98\n"
    "v_add_u32 v96, v96, v98\n"
    "v_add_u32 v97, 8192, v96\n"
    "s_setprio 0\n"
    "v_mul_f32 v109, v109, v117\n"
    "v_mul_f32 v110, v110, v118\n"
    "v_mul_f32 v111, v111, v119\n"
    "v_cvt_pk_bf16_f32 v60, v104, v105\n"
    "v_cvt_pk_bf16_f32 v61, v106, v107\n"
    "v_cvt_pk_bf16_f32 v62, v108, v109\n"
    "v_cvt_pk_bf16_f32 v63, v110, v111\n"
    "v_fma_f32 v104, v92, v76, v132\n"
    "v_fma_f32 v105, v92, v77, v133\n"
    "v_fma_f32 v106, v92, v78, v134\n"
    "v_fma_f32 v107, v92, v79, v135\n"
    "v_fma_f32 v108, v92, v80, v136\n"
    "v_fma_f32 v109, v92, v81, v137\n"
    "v_fma_f32 v110, v92, v82, v138\n"
    "v_fma_f32 v111, v92, v83, v139\n"
    ".Lpage12:\n"
    "s_cbranch_execz .Ltouch_next\n"
    "v_fmac_f32_dpp v104, v76, v93 row_newbcast:15 row_mask:0xf bank_mask:0xf\n"
    "v_fmac_f32_dpp v105, v77, v93 row_newbcast:15 row_mask:0xf bank_mask:0xf\n"
    "v_fmac_f32_dpp v106, v78, v93 row_newbcast:15 row_mask:0xf bank_mask:0xf\n"
    "v_fmac_f32_dpp v107, v79, v93 row_newbcast:15 row_mask:0xf bank_mask:0xf\n"
    "s_barrier\n"
    "s_setprio 3\n"
    "s_add_u32 s25, s16, 8\n"
    "s_sub_u32 s19, s25, 9\n"
    "s_cmp_ge_u32 s25, 9\n"
    "s_cselect_b32 s25, s19, s25\n"
    "s_mul_i32 s25, s25, 589824\n"
    "s_add_u32 s25, s25, s26\n"
    "s_add_u32 m0, s27, 49152\n"
    "s_add_u32 s19, s25, 512\n"
    "buffer_load_dwordx4 v2, s[72:75], s19 offen offset:0 lds\n"
    "buffer_load_dwordx4 v3, s[72:75], s19 offen offset:1024 lds\n"
    "s_nop 7\n"
    "ds_write_b128 v1, v[60:63] offset:23552\n"
    "s_add_u32 m0, s27, 57344\n"
    "s_add_u32 s19, s25, 37376\n"
    "buffer_load_dwordx4 v2, s[72:75], s19 offen offset:0 lds\n"
    "buffer_load_dwordx4 v3, s[72:75], s19 offen offset:1024 lds\n"
    "s_setprio 0\n"
    "v_fmac_f32_dpp v108, v80, v93 row_newbcast:15 row_mask:0xf bank_mask:0xf\n"
    "v_fmac_f32_dpp v109, v81, v93 row_newbcast:15 row_mask:0xf bank_mask:0xf\n"
    "v_fmac_f32_dpp v110, v82, v93 row_newbcast:15 row_mask:0xf bank_mask:0xf\n"
    "v_fmac_f32_dpp v111, v83, v93 row_newbcast:15 row_mask:0xf bank_mask:0xf\n"
    "v_fma_f32 v112, -v104, v104, -s21\n"
    "v_fma_f32 v113, -v105, v105, -s21\n"
    "v_fma_f32 v114, -v106, v106, -s21\n"
    "v_fma_f32 v115, -v107, v107, -s21\n"
    "v_fma_f32 v116, -v108, v108, -s21\n"
    "v_fma_f32 v117, -v109, v109, -s21\n"
    "v_fma_f32 v118, -v110, v110, -s21\n"
    "v_fma_f32 v119, -v111, v111, -s21\n"
    "v_mul_f32 v112, v104, v112\n"
    "v_mul_f32 v113, v105, v113\n"
    "v_mul_f32 v114, v106, v114\n"
    "v_mul_f32 v115, v107, v115\n"
    "v_mul_f32 v116, v108, v116\n"
    "v_mul_f32 v117, v109, v117\n"
    "s_barrier\n"
    "s_setprio 3\n"
    "s_add_u32 m0, s27, 0\n"
    "s_add_u32 s19, s25, 768\n"
    "buffer_load_dwordx4 v2, s[72:75], s19 offen offset:0 lds\n"
    "buffer_load_dwordx4 v3, s[72:75], s19 offen offset:1024 lds\n"
    "s_add_u32 m0, s27, 8192\n"
    "s_add_u32 s19, s25, 37632\n"
    "buffer_load_dwordx4 v2, s[72:75], s19 offen offset:0 lds\n"
    "buffer_load_dwordx4 v3, s[72:75], s19 offen offset:1024 lds\n"
    "s_setprio 0\n"
    "v_mul_f32 v118, v110, v118\n"
    "v_mul_f32 v119, v111, v119\n"
    "v_exp_f32 v112, v112\n"
    "v_exp_f32 v113, v113\n"
    "v_exp_f32 v114, v114\n"
    "v_exp_f32 v115, v115\n"
    "v_exp_f32 v116, v116\n"
    "v_exp_f32 v117, v117\n"
    "v_exp_f32 v118, v118\n"
    "v_exp_f32 v119, v119\n"
    "s_waitcnt vmcnt(8)\n"
    "s_barrier\n"
    "s_setprio 3\n"
    "s_add_u32 m0, s27, 16384\n"
    "s_add_u32 s19, s92, 0\n"
    "buffer_load_dwordx4 v96, s[80:83], s19 offen offset:0 lds\n"
    "buffer_load_dwordx4 v97, s[80:83], s19 offen offset:1024 lds\n"
    "s_add_u32 m0, s27, 24576\n"
    "s_add_u32 s19, s92, 18432\n"
    "buffer_load_dwordx4 v96, s[80:83], s19 offen offset:0 lds\n"
    "buffer_load_dwordx4 v97, s[80:83], s19 offen offset:1024 lds\n"
    "s_add_u32 m0, s27, 4294912000\n"
    "s_add_u32 s19, s94, 0\n"
    "buffer_load_dwordx4 v96, s[80:83], s19 offen offset:0 lds\n"
    "buffer_load_dwordx4 v97, s[80:83], s19 offen offset:1024 lds\n"
    "s_setprio 0\n"
    "v_fma_f32 v112, v112, s20, s20\n"
    "v_fma_f32 v113, v113, s20, s20\n"
    "v_fma_f32 v114, v114, s20, s20\n"
    "v_fma_f32 v115, v115, s20, s20\n"
    "v_fma_f32 v116, v116, s20, s20\n"
    "v_fma_f32 v117, v117, s20, s20\n"
    "v_fma_f32 v118, v118, s20, s20\n"
    "v_fma_f32 v119, v119, s20, s20\n"
    "v_rcp_f32 v112, v112\n"
    "v_rcp_f32 v113, v113\n"
    "v_rcp_f32 v114, v114\n"
    "v_rcp_f32 v115, v115\n"
    "v_rcp_f32 v116, v116\n"
    "s_waitcnt vmcnt(10)\n"
    "s_barrier\n"
    "s_setprio 3\n"
    "s_add_u32 m0, s27, 32768\n"
    "s_add_u32 s19, s93, 0\n"
    "buffer_load_dwordx4 v96, s[80:83], s19 offen offset:0 lds\n"
    "buffer_load_dwordx4 v97, s[80:83], s19 offen offset:1024 lds\n"
    "s_add_u32 m0, s27, 40960\n"
    "s_add_u32 s19, s93, 18432\n"
    "buffer_load_dwordx4 v96, s[80:83], s19 offen offset:0 lds\n"
    "buffer_load_dwordx4 v97, s[80:83], s19 offen offset:1024 lds\n"
    "s_add_u32 m0, s27, 4294920192\n"
    "s_add_u32 s19, s94, 18432\n"
    "buffer_load_dwordx4 v96, s[80:83], s19 offen offset:0 lds\n"
    "buffer_load_dwordx4 v97, s[80:83], s19 offen offset:1024 lds\n"
    "s_setprio 0\n"
    "v_rcp_f32 v117, v117\n"
    "v_rcp_f32 v118, v118\n"
    "v_rcp_f32 v119, v119\n"
    "v_mul_f32 v104, v104, v112\n"
    "v_mul_f32 v105, v105, v113\n"
    "v_mul_f32 v106, v106, v114\n"
    "v_mul_f32 v107, v107, v115\n"
    "v_mul_f32 v108, v108, v116\n"
    "v_mul_f32 v109, v109, v117\n"
    "v_mul_f32 v110, v110, v118\n"
    "v_mul_f32 v111, v111, v119\n"
    "v_cvt_pk_bf16_f32 v76, v104, v105\n"
    "v_cvt_pk_bf16_f32 v77, v106, v107\n"
    "v_cvt_pk_bf16_f32 v78, v108, v109\n"
    "v_cvt_pk_bf16_f32 v79, v110, v111\n"
    "s_waitcnt vmcnt(12)\n"
    "s_barrier\n"
    "s_setprio 3\n"
    "s_add_u32 m0, s27, 49152\n"
    "s_add_u32 s19, s92, 73728\n"
    "buffer_load_dwordx4 v96, s[80:83], s19 offen offset:0 lds\n"
    "buffer_load_dwordx4 v97, s[80:83], s19 offen offset:1024 lds\n"
    "s_nop 7\n"
    "ds_write_b128 v1, v[76:79] offset:24576\n"
    "s_add_u32 m0, s27, 57344\n"
    "s_add_u32 s19, s92, 92160\n"
    "buffer_load_dwordx4 v96, s[80:83], s19 offen offset:0 lds\n"
    "buffer_load_dwordx4 v97, s[80:83], s19 offen offset:1024 lds\n"
    "s_add_u32 m0, s27, 4294936576\n"
    "s_add_u32 s19, s94, 147456\n"
    "buffer_load_dwordx4 v96, s[80:83], s19 offen offset:0 lds\n"
    "buffer_load_dwordx4 v97, s[80:83], s19 offen offset:1024 lds\n"
    "s_setprio 0\n"
    "s_waitcnt lgkmcnt(5)\n"
    "s_waitcnt vmcnt(14)\n"
    "s_barrier\n"
    "s_setprio 2\n"
    "ds_read_b64_tr_b16 v[140:141], v160 offset:16384\n"
    "ds_read_b64_tr_b16 v[142:143], v160 offset:24576\n"
    "ds_read_b64_tr_b16 v[144:145], v160 offset:18432\n"
    "ds_read_b64_tr_b16 v[146:147], v160 offset:26624\n"
    "ds_read_b64_tr_b16 v[152:153], v161 offset:18432\n"
    "ds_read_b64_tr_b16 v[154:155], v161 offset:26624\n"
    "ds_read_b64_tr_b16 v[148:149], v161 offset:16384\n"
    "ds_read_b64_tr_b16 v[150:151], v161 offset:24576\n"
    "ds_read_b128 v[84:87], v156 offset:0\n"
    "ds_read_b128 v[88:91], v156 offset:5120\n"
    "ds_read_b128 v[92:95], v156 offset:1024\n"
    "ds_read_b128 v[96:99], v156 offset:6144\n"
    "ds_read_b128 v[100:103], v156 offset:2048\n"
    "ds_read_b128 v[104:107], v156 offset:7168\n"
    "s_waitcnt lgkmcnt(4)\n"
    "s_waitcnt vmcnt(8)\n"
    "s_barrier\n"
    "v_mfma_f32_16x16x32_bf16 v[4:7], v[140:143], v[84:87], 0\n"
    "ds_read_b128 v[108:111], v156 offset:3072\n"
    "v_mfma_f32_16x16x32_bf16 v[4:7], v[144:147], v[88:91], v[4:7]\n"
    "ds_read_b128 v[112:115], v156 offset:8192\n"
    "v_mfma_f32_16x16x32_bf16 v[8:11], v[152:155], v[88:91], 0\n"
    "ds_read_b64_tr_b16 v[124:125], v160 offset:32768\n"
    "v_mfma_f32_16x16x32_bf16 v[8:11], v[148:151], v[84:87], v[8:11]\n"
    "ds_read_b64_tr_b16 v[126:127], v160 offset:40960\n"
    "s_waitcnt lgkmcnt(6)\n"
    "v_mfma_f32_16x16x32_bf16 v[24:27], v[148:151], v[92:95], 0\n"
    "ds_read_b128 v[116:119], v156 offset:4096\n"
    "v_mfma_f32_16x16x32_bf16 v[24:27], v[152:155], v[96:99], v[24:27]\n"
    "ds_read_b128 v[120:123], v156 offset:9216\n"
    "v_mfma_f32_16x16x32_bf16 v[20:23], v[144:147], v[96:99], 0\n"
    "ds_read_b64_tr_b16 v[128:129], v160 offset:34816\n"
    "v_mfma_f32_16x16x32_bf16 v[20:23], v[140:143], v[92:95], v[20:23]\n"
    "ds_read_b64_tr_b16 v[130:131], v160 offset:43008\n"
    "s_waitcnt lgkmcnt(8)\n"
    "v_mfma_f32_16x16x32_bf16 v[36:39], v[140:143], v[100:103], 0\n"
    "ds_read_b64_tr_b16 v[136:137], v161 offset:34816\n"
    "v_mfma_f32_16x16x32_bf16 v[36:39], v[144:147], v[104:107], v[36:39]\n"
    "ds_read_b64_tr_b16 v[138:139], v161 offset:43008\n"
    "v_mfma_f32_16x16x32_bf16 v[40:43], v[152:155], v[104:107], 0\n"
    "ds_read_b64_tr_b16 v[132:133], v161 offset:32768\n"
    "v_mfma_f32_16x16x32_bf16 v[40:43], v[148:151], v[100:103], v[40:43]\n"
    "ds_read_b64_tr_b16 v[134:135], v161 offset:40960\n"
    "s_waitcnt lgkmcnt(10)\n"
    "v_mfma_f32_16x16x32_bf16 v[56:59], v[148:151], v[108:111], 0\n"
    "v_mfma_f32_16x16x32_bf16 v[56:59], v[152:155], v[112:115], v[56:59]\n"
    "v_mfma_f32_16x16x32_bf16 v[52:55], v[144:147], v[112:115], 0\n"
    "v_mfma_f32_16x16x32_bf16 v[52:55], v[140:143], v[108:111], v[52:55]\n"
    "s_waitcnt lgkmcnt(6)\n"
    "v_mfma_f32_16x16x32_bf16 v[68:71], v[140:143], v[116:119], 0\n"
    "v_mfma_f32_16x16x32_bf16 v[68:71], v[144:147], v[120:123], v[68:71]\n"
    "v_mfma_f32_16x16x32_bf16 v[72:75], v[152:155], v[120:123], 0\n"
    "s_waitcnt lgkmcnt(0)\n"
    "s_waitcnt vmcnt(2)\n"
    "v_mfma_f32_16x16x32_bf16 v[72:75], v[148:151], v[116:119], v[72:75]\n"
    "s_barrier\n"
    "v_mfma_f32_16x16x32_bf16 v[12:15], v[124:127], v[84:87], 0\n"
    "ds_read_b64_tr_b16 v[140:141], v160 offset:49152\n"
    "v_mfma_f32_16x16x32_bf16 v[12:15], v[128:131], v[88:91], v[12:15]\n"
    "ds_read_b64_tr_b16 v[142:143], v160 offset:57344\n"
    "v_mfma_f32_16x16x32_bf16 v[16:19], v[136:139], v[88:91], 0\n"
    "ds_read_b64_tr_b16 v[144:145], v160 offset:51200\n"
    "v_mfma_f32_16x16x32_bf16 v[16:19], v[132:135], v[84:87], v[16:19]\n"
    "ds_read_b64_tr_b16 v[146:147], v160 offset:59392\n"
    "v_mfma_f32_16x16x32_bf16 v[32:35], v[132:135], v[92:95], 0\n"
    "ds_read_b64_tr_b16 v[152:153], v161 offset:51200\n"
    "v_mfma_f32_16x16x32_bf16 v[32:35], v[136:139], v[96:99], v[32:35]\n"
    "ds_read_b64_tr_b16 v[154:155], v161 offset:59392\n"
    "v_mfma_f32_16x16x32_bf16 v[28:31], v[128:131], v[96:99], 0\n"
    "ds_read_b64_tr_b16 v[148:149], v161 offset:49152\n"
    "v_mfma_f32_16x16x32_bf16 v[28:31], v[124:127], v[92:95], v[28:31]\n"
    "ds_read_b128 v[88:91], v156 offset:15360\n"
    "v_mfma_f32_16x16x32_bf16 v[44:47], v[124:127], v[100:103], 0\n"
    "ds_read_b128 v[84:87], v156 offset:10240\n"
    "v_mfma_f32_16x16x32_bf16 v[44:47], v[128:131], v[104:107], v[44:47]\n"
    "ds_read_b64_tr_b16 v[150:151], v161 offset:57344\n"
    "v_mfma_f32_16x16x32_bf16 v[48:51], v[136:139], v[104:107], 0\n"
    "ds_read_b128 v[96:99], v156 offset:16384\n"
    "v_mfma_f32_16x16x32_bf16 v[48:51], v[132:135], v[100:103], v[48:51]\n"
    "ds_read_b128 v[92:95], v156 offset:11264\n"
    "v_mfma_f32_16x16x32_bf16 v[64:67], v[132:135], v[108:111], 0\n"
    "ds_read_b128 v[104:107], v156 offset:17408\n"
    "v_mfma_f32_16x16x32_bf16 v[64:67], v[136:139], v[112:115], v[64:67]\n"
    "ds_read_b128 v[100:103], v156 offset:12288\n"
    "v_mfma_f32_16x16x32_bf16 v[60:63], v[128:131], v[112:115], 0\n"
    "v_mfma_f32_16x16x32_bf16 v[60:63], v[124:127], v[108:111], v[60:63]\n"
    "v_mfma_f32_16x16x32_bf16 v[76:79], v[124:127], v[116:119], 0\n"
    "v_mfma_f32_16x16x32_bf16 v[76:79], v[128:131], v[120:123], v[76:79]\n"
    "v_mfma_f32_16x16x32_bf16 v[80:83], v[136:139], v[120:123], 0\n"
    "s_waitcnt lgkmcnt(4)\n"
    "s_waitcnt vmcnt(0)\n"
    "v_mfma_f32_16x16x32_bf16 v[80:83], v[132:135], v[116:119], v[80:83]\n"
    "s_barrier\n"
    "v_mfma_f32_16x16x32_bf16 v[4:7], v[140:143], v[84:87], v[4:7]\n"
    "ds_read_b128 v[112:115], v156 offset:18432\n"
    "v_mfma_f32_16x16x32_bf16 v[4:7], v[144:147], v[88:91], v[4:7]\n"
    "ds_read_b128 v[108:111], v156 offset:13312\n"
    "v_mfma_f32_16x16x32_bf16 v[8:11], v[152:155], v[88:91], v[8:11]\n"
    "ds_read_b64_tr_b16 v[124:125], v160 offset:8192\n"
    "v_mfma_f32_16x16x32_bf16 v[8:11], v[148:151], v[84:87], v[8:11]\n"
    "ds_read_b64_tr_b16 v[126:127], v160 offset:16384\n"
    "s_waitcnt lgkmcnt(6)\n"
    "v_mfma_f32_16x16x32_bf16 v[24:27], v[148:151], v[92:95], v[24:27]\n"
    "ds_read_b128 v[120:123], v156 offset:19456\n"
    "v_mfma_f32_16x16x32_bf16 v[24:27], v[152:155], v[96:99], v[24:27]\n"
    "ds_read_b128 v[116:119], v156 offset:14336\n"
    "v_mfma_f32_16x16x32_bf16 v[20:23], v[144:147], v[96:99], v[20:23]\n"
    "ds_read_b64_tr_b16 v[128:129], v160 offset:10240\n"
    "v_mfma_f32_16x16x32_bf16 v[20:23], v[140:143], v[92:95], v[20:23]\n"
    "ds_read_b64_tr_b16 v[130:131], v160 offset:18432\n"
    "s_waitcnt lgkmcnt(8)\n"
    "v_mfma_f32_16x16x32_bf16 v[36:39], v[140:143], v[100:103], v[36:39]\n"
    "ds_read_b64_tr_b16 v[136:137], v161 offset:10240\n"
    "v_mfma_f32_16x16x32_bf16 v[36:39], v[144:147], v[104:107], v[36:39]\n"
    "ds_read_b64_tr_b16 v[138:139], v161 offset:18432\n"
    "v_mfma_f32_16x16x32_bf16 v[40:43], v[152:155], v[104:107], v[40:43]\n"
    "ds_read_b64_tr_b16 v[132:133], v161 offset:8192\n"
    "v_mfma_f32_16x16x32_bf16 v[40:43], v[148:151], v[100:103], v[40:43]\n"
    "ds_read_b64_tr_b16 v[134:135], v161 offset:16384\n"
    "s_waitcnt lgkmcnt(10)\n"
    "v_mfma_f32_16x16x32_bf16 v[56:59], v[148:151], v[108:111], v[56:59]\n"
    "v_mfma_f32_16x16x32_bf16 v[56:59], v[152:155], v[112:115], v[56:59]\n"
    "v_mfma_f32_16x16x32_bf16 v[52:55], v[144:147], v[112:115], v[52:55]\n"
    "v_mfma_f32_16x16x32_bf16 v[52:55], v[140:143], v[108:111], v[52:55]\n"
    "s_waitcnt lgkmcnt(6)\n"
    "v_mfma_f32_16x16x32_bf16 v[68:71], v[140:143], v[116:119], v[68:71]\n"
    "v_mfma_f32_16x16x32_bf16 v[68:71], v[144:147], v[120:123], v[68:71]\n"
    "v_mfma_f32_16x16x32_bf16 v[72:75], v[152:155], v[120:123], v[72:75]\n"
    "s_waitcnt lgkmcnt(0)\n"
    "v_mfma_f32_16x16x32_bf16 v[72:75], v[148:151], v[116:119], v[72:75]\n"
    "s_barrier\n"
    "v_mfma_f32_16x16x32_bf16 v[12:15], v[124:127], v[84:87], v[12:15]\n"
    "ds_read_b64_tr_b16 v[140:141], v160 offset:24576\n"
    "v_mfma_f32_16x16x32_bf16 v[12:15], v[128:131], v[88:91], v[12:15]\n"
    "ds_read_b64_tr_b16 v[142:143], v160 offset:32768\n"
    "v_mfma_f32_16x16x32_bf16 v[16:19], v[136:139], v[88:91], v[16:19]\n"
    "ds_read_b64_tr_b16 v[144:145], v160 offset:26624\n"
    "v_mfma_f32_16x16x32_bf16 v[16:19], v[132:135], v[84:87], v[16:19]\n"
    "ds_read_b64_tr_b16 v[146:147], v160 offset:34816\n"
    "v_mfma_f32_16x16x32_bf16 v[32:35], v[132:135], v[92:95], v[32:35]\n"
    "ds_read_b64_tr_b16 v[152:153], v161 offset:26624\n"
    "v_mfma_f32_16x16x32_bf16 v[32:35], v[136:139], v[96:99], v[32:35]\n"
    "ds_read_b64_tr_b16 v[154:155], v161 offset:34816\n"
    "v_mfma_f32_16x16x32_bf16 v[28:31], v[128:131], v[96:99], v[28:31]\n"
    "ds_read_b64_tr_b16 v[148:149], v161 offset:24576\n"
    "v_mfma_f32_16x16x32_bf16 v[28:31], v[124:127], v[92:95], v[28:31]\n"
    "ds_read_b128 v[88:91], v156 offset:25600\n"
    "v_mfma_f32_16x16x32_bf16 v[44:47], v[124:127], v[100:103], v[44:47]\n"
    "ds_read_b128 v[84:87], v156 offset:20480\n"
    "v_mfma_f32_16x16x32_bf16 v[44:47], v[128:131], v[104:107], v[44:47]\n"
    "ds_read_b64_tr_b16 v[150:151], v161 offset:32768\n"
    "v_mfma_f32_16x16x32_bf16 v[48:51], v[136:139], v[104:107], v[48:51]\n"
    "ds_read_b128 v[96:99], v156 offset:26624\n"
    "v_mfma_f32_16x16x32_bf16 v[48:51], v[132:135], v[100:103], v[48:51]\n"
    "ds_read_b128 v[92:95], v156 offset:21504\n"
    "v_mfma_f32_16x16x32_bf16 v[64:67], v[132:135], v[108:111], v[64:67]\n"
    "ds_read_b128 v[104:107], v156 offset:27648\n"
    "v_mfma_f32_16x16x32_bf16 v[64:67], v[136:139], v[112:115], v[64:67]\n"
    "ds_read_b128 v[100:103], v156 offset:22528\n"
    "v_mfma_f32_16x16x32_bf16 v[60:63], v[128:131], v[112:115], v[60:63]\n"
    "v_mfma_f32_16x16x32_bf16 v[60:63], v[124:127], v[108:111], v[60:63]\n"
    "v_mfma_f32_16x16x32_bf16 v[76:79], v[124:127], v[116:119], v[76:79]\n"
    "v_mfma_f32_16x16x32_bf16 v[76:79], v[128:131], v[120:123], v[76:79]\n"
    "v_mfma_f32_16x16x32_bf16 v[80:83], v[136:139], v[120:123], v[80:83]\n"
    "s_waitcnt lgkmcnt(4)\n"
    "v_mfma_f32_16x16x32_bf16 v[80:83], v[132:135], v[116:119], v[80:83]\n"
    "s_barrier\n"
    "v_mfma_f32_16x16x32_bf16 v[4:7], v[140:143], v[84:87], v[4:7]\n"
    "ds_read_b128 v[112:115], v156 offset:28672\n"
    "v_mfma_f32_16x16x32_bf16 v[4:7], v[144:147], v[88:91], v[4:7]\n"
    "ds_read_b128 v[108:111], v156 offset:23552\n"
    "v_mfma_f32_16x16x32_bf16 v[8:11], v[152:155], v[88:91], v[8:11]\n"
    "ds_read_b64_tr_b16 v[124:125], v160 offset:40960\n"
    "v_mfma_f32_16x16x32_bf16 v[8:11], v[148:151], v[84:87], v[8:11]\n"
    "ds_read_b64_tr_b16 v[126:127], v160 offset:49152\n"
    "s_waitcnt lgkmcnt(6)\n"
    "v_mfma_f32_16x16x32_bf16 v[24:27], v[148:151], v[92:95], v[24:27]\n"
    "ds_read_b128 v[120:123], v156 offset:29696\n"
    "v_mfma_f32_16x16x32_bf16 v[24:27], v[152:155], v[96:99], v[24:27]\n"
    "ds_read_b128 v[116:119], v156 offset:24576\n"
    "v_mfma_f32_16x16x32_bf16 v[20:23], v[144:147], v[96:99], v[20:23]\n"
    "ds_read_b64_tr_b16 v[128:129], v160 offset:43008\n"
    "v_mfma_f32_16x16x32_bf16 v[20:23], v[140:143], v[92:95], v[20:23]\n"
    "ds_read_b64_tr_b16 v[130:131], v160 offset:51200\n"
    "s_waitcnt lgkmcnt(8)\n"
    "v_mfma_f32_16x16x32_bf16 v[36:39], v[140:143], v[100:103], v[36:39]\n"
    "ds_read_b64_tr_b16 v[136:137], v161 offset:43008\n"
    "v_mfma_f32_16x16x32_bf16 v[36:39], v[144:147], v[104:107], v[36:39]\n"
    "ds_read_b64_tr_b16 v[138:139], v161 offset:51200\n"
    "v_mfma_f32_16x16x32_bf16 v[40:43], v[152:155], v[104:107], v[40:43]\n"
    "ds_read_b64_tr_b16 v[132:133], v161 offset:40960\n"
    "v_mfma_f32_16x16x32_bf16 v[40:43], v[148:151], v[100:103], v[40:43]\n"
    "ds_read_b64_tr_b16 v[134:135], v161 offset:49152\n"
    "s_waitcnt lgkmcnt(10)\n"
    "v_mfma_f32_16x16x32_bf16 v[56:59], v[148:151], v[108:111], v[56:59]\n"
    "v_mfma_f32_16x16x32_bf16 v[56:59], v[152:155], v[112:115], v[56:59]\n"
    "v_mfma_f32_16x16x32_bf16 v[52:55], v[144:147], v[112:115], v[52:55]\n"
    "v_mfma_f32_16x16x32_bf16 v[52:55], v[140:143], v[108:111], v[52:55]\n"
    "s_waitcnt lgkmcnt(6)\n"
    "v_mfma_f32_16x16x32_bf16 v[68:71], v[140:143], v[116:119], v[68:71]\n"
    "v_mfma_f32_16x16x32_bf16 v[68:71], v[144:147], v[120:123], v[68:71]\n"
    "v_mfma_f32_16x16x32_bf16 v[72:75], v[152:155], v[120:123], v[72:75]\n"
    "s_waitcnt lgkmcnt(0)\n"
    "v_mfma_f32_16x16x32_bf16 v[72:75], v[148:151], v[116:119], v[72:75]\n"
    "s_barrier\n"
    "v_mfma_f32_16x16x32_bf16 v[12:15], v[124:127], v[84:87], v[12:15]\n"
    "ds_read_b64_tr_b16 v[140:141], v160 offset:57344\n"
    "v_mfma_f32_16x16x32_bf16 v[12:15], v[128:131], v[88:91], v[12:15]\n"
    "ds_read_b64_tr_b16 v[142:143], v160 offset:8192\n"
    "v_mfma_f32_16x16x32_bf16 v[16:19], v[136:139], v[88:91], v[16:19]\n"
    "ds_read_b64_tr_b16 v[144:145], v160 offset:59392\n"
    "v_mfma_f32_16x16x32_bf16 v[16:19], v[132:135], v[84:87], v[16:19]\n"
    "ds_read_b64_tr_b16 v[146:147], v160 offset:10240\n"
    "v_mfma_f32_16x16x32_bf16 v[32:35], v[132:135], v[92:95], v[32:35]\n"
    "ds_read_b64_tr_b16 v[152:153], v161 offset:59392\n"
    "v_mfma_f32_16x16x32_bf16 v[32:35], v[136:139], v[96:99], v[32:35]\n"
    "ds_read_b64_tr_b16 v[154:155], v161 offset:10240\n"
    "v_mfma_f32_16x16x32_bf16 v[28:31], v[128:131], v[96:99], v[28:31]\n"
    "ds_read_b64_tr_b16 v[148:149], v161 offset:57344\n"
    "v_mfma_f32_16x16x32_bf16 v[28:31], v[124:127], v[92:95], v[28:31]\n"
    "ds_read_b128 v[88:91], v156 offset:35840\n"
    "v_mfma_f32_16x16x32_bf16 v[44:47], v[124:127], v[100:103], v[44:47]\n"
    "ds_read_b128 v[84:87], v156 offset:30720\n"
    "v_mfma_f32_16x16x32_bf16 v[44:47], v[128:131], v[104:107], v[44:47]\n"
    "ds_read_b64_tr_b16 v[150:151], v161 offset:8192\n"
    "v_mfma_f32_16x16x32_bf16 v[48:51], v[136:139], v[104:107], v[48:51]\n"
    "ds_read_b128 v[96:99], v156 offset:36864\n"
    "v_mfma_f32_16x16x32_bf16 v[48:51], v[132:135], v[100:103], v[48:51]\n"
    "ds_read_b128 v[92:95], v156 offset:31744\n"
    "v_mfma_f32_16x16x32_bf16 v[64:67], v[132:135], v[108:111], v[64:67]\n"
    "ds_read_b128 v[104:107], v156 offset:37888\n"
    "v_mfma_f32_16x16x32_bf16 v[64:67], v[136:139], v[112:115], v[64:67]\n"
    "ds_read_b128 v[100:103], v156 offset:32768\n"
    "v_mfma_f32_16x16x32_bf16 v[60:63], v[128:131], v[112:115], v[60:63]\n"
    "v_mfma_f32_16x16x32_bf16 v[60:63], v[124:127], v[108:111], v[60:63]\n"
    "v_mfma_f32_16x16x32_bf16 v[76:79], v[124:127], v[116:119], v[76:79]\n"
    "v_mfma_f32_16x16x32_bf16 v[76:79], v[128:131], v[120:123], v[76:79]\n"
    "v_mfma_f32_16x16x32_bf16 v[80:83], v[136:139], v[120:123], v[80:83]\n"
    "s_waitcnt lgkmcnt(4)\n"
    "v_mfma_f32_16x16x32_bf16 v[80:83], v[132:135], v[116:119], v[80:83]\n"
    "s_barrier\n"
    "v_mfma_f32_16x16x32_bf16 v[4:7], v[140:143], v[84:87], v[4:7]\n"
    "ds_read_b128 v[112:115], v156 offset:38912\n"
    "v_mfma_f32_16x16x32_bf16 v[4:7], v[144:147], v[88:91], v[4:7]\n"
    "ds_read_b128 v[108:111], v156 offset:33792\n"
    "v_mfma_f32_16x16x32_bf16 v[8:11], v[152:155], v[88:91], v[8:11]\n"
    "ds_read_b64_tr_b16 v[124:125], v160 offset:16384\n"
    "v_mfma_f32_16x16x32_bf16 v[8:11], v[148:151], v[84:87], v[8:11]\n"
    "ds_read_b64_tr_b16 v[126:127], v160 offset:24576\n"
    "s_waitcnt lgkmcnt(6)\n"
    "v_mfma_f32_16x16x32_bf16 v[24:27], v[148:151], v[92:95], v[24:27]\n"
    "ds_read_b128 v[120:123], v156 offset:39936\n"
    "v_mfma_f32_16x16x32_bf16 v[24:27], v[152:155], v[96:99], v[24:27]\n"
    "ds_read_b128 v[116:119], v156 offset:34816\n"
    "v_mfma_f32_16x16x32_bf16 v[20:23], v[144:147], v[96:99], v[20:23]\n"
    "ds_read_b64_tr_b16 v[128:129], v160 offset:18432\n"
    "v_mfma_f32_16x16x32_bf16 v[20:23], v[140:143], v[92:95], v[20:23]\n"
    "ds_read_b64_tr_b16 v[130:131], v160 offset:26624\n"
    "s_waitcnt lgkmcnt(8)\n"
    "v_mfma_f32_16x16x32_bf16 v[36:39], v[140:143], v[100:103], v[36:39]\n"
    "ds_read_b64_tr_b16 v[136:137], v161 offset:18432\n"
    "v_mfma_f32_16x16x32_bf16 v[36:39], v[144:147], v[104:107], v[36:39]\n"
    "ds_read_b64_tr_b16 v[138:139], v161 offset:26624\n"
    "v_mfma_f32_16x16x32_bf16 v[40:43], v[152:155], v[104:107], v[40:43]\n"
    "ds_read_b64_tr_b16 v[132:133], v161 offset:16384\n"
    "v_mfma_f32_16x16x32_bf16 v[40:43], v[148:151], v[100:103], v[40:43]\n"
    "ds_read_b64_tr_b16 v[134:135], v161 offset:24576\n"
    "s_waitcnt lgkmcnt(10)\n"
    "v_mfma_f32_16x16x32_bf16 v[56:59], v[148:151], v[108:111], v[56:59]\n"
    "v_mfma_f32_16x16x32_bf16 v[56:59], v[152:155], v[112:115], v[56:59]\n"
    "v_mfma_f32_16x16x32_bf16 v[52:55], v[144:147], v[112:115], v[52:55]\n"
    "v_mfma_f32_16x16x32_bf16 v[52:55], v[140:143], v[108:111], v[52:55]\n"
    "s_waitcnt lgkmcnt(6)\n"
    "v_mfma_f32_16x16x32_bf16 v[68:71], v[140:143], v[116:119], v[68:71]\n"
    "v_mfma_f32_16x16x32_bf16 v[68:71], v[144:147], v[120:123], v[68:71]\n"
    "v_mfma_f32_16x16x32_bf16 v[72:75], v[152:155], v[120:123], v[72:75]\n"
    "s_waitcnt lgkmcnt(0)\n"
    "v_mfma_f32_16x16x32_bf16 v[72:75], v[148:151], v[116:119], v[72:75]\n"
    "s_barrier\n"
    "v_mfma_f32_16x16x32_bf16 v[12:15], v[124:127], v[84:87], v[12:15]\n"
    "ds_read_b128 v[140:143], v158 offset:32768\n"
    "v_mfma_f32_16x16x32_bf16 v[12:15], v[128:131], v[88:91], v[12:15]\n"
    "ds_read_b128 v[144:147], v159 offset:32768\n"
    "v_mfma_f32_16x16x32_bf16 v[16:19], v[136:139], v[88:91], v[16:19]\n"
    "ds_read_b128 v[152:155], v159 offset:40960\n"
    "v_mfma_f32_16x16x32_bf16 v[16:19], v[132:135], v[84:87], v[16:19]\n"
    "ds_read_b128 v[148:151], v158 offset:40960\n"
    "v_mfma_f32_16x16x32_bf16 v[32:35], v[132:135], v[92:95], v[32:35]\n"
    "ds_read_b64_tr_b16 v[88:89], v162 offset:5120\n"
    "v_mfma_f32_16x16x32_bf16 v[32:35], v[136:139], v[96:99], v[32:35]\n"
    "ds_read_b64_tr_b16 v[90:91], v163 offset:5120\n"
    "v_mfma_f32_16x16x32_bf16 v[28:31], v[128:131], v[96:99], v[28:31]\n"
    "ds_read_b64_tr_b16 v[84:85], v162 offset:0\n"
    "v_mfma_f32_16x16x32_bf16 v[28:31], v[124:127], v[92:95], v[28:31]\n"
    "ds_read_b64_tr_b16 v[86:87], v163 offset:0\n"
    "v_mfma_f32_16x16x32_bf16 v[44:47], v[124:127], v[100:103], v[44:47]\n"
    "ds_read_b64_tr_b16 v[96:97], v162 offset:6144\n"
    "v_mfma_f32_16x16x32_bf16 v[44:47], v[128:131], v[104:107], v[44:47]\n"
    "ds_read_b64_tr_b16 v[98:99], v163 offset:6144\n"
    "v_mfma_f32_16x16x32_bf16 v[48:51], v[136:139], v[104:107], v[48:51]\n"
    "ds_read_b64_tr_b16 v[92:93], v162 offset:1024\n"
    "v_mfma_f32_16x16x32_bf16 v[48:51], v[132:135], v[100:103], v[48:51]\n"
    "ds_read_b64_tr_b16 v[94:95], v163 offset:1024\n"
    "v_mfma_f32_16x16x32_bf16 v[64:67], v[132:135], v[108:111], v[64:67]\n"
    "ds_read_b64_tr_b16 v[104:105], v162 offset:7168\n"
    "v_mfma_f32_16x16x32_bf16 v[64:67], v[136:139], v[112:115], v[64:67]\n"
    "ds_read_b64_tr_b16 v[106:107], v163 offset:7168\n"
    "v_mfma_f32_16x16x32_bf16 v[60:63], v[128:131], v[112:115], v[60:63]\n"
    "ds_read_b64_tr_b16 v[100:101], v162 offset:2048\n"
    "v_mfma_f32_16x16x32_bf16 v[60:63], v[124:127], v[108:111], v[60:63]\n"
    "ds_read_b64_tr_b16 v[102:103], v163 offset:2048\n"
    "v_mfma_f32_16x16x32_bf16 v[76:79], v[124:127], v[116:119], v[76:79]\n"
    "v_mfma_f32_16x16x32_bf16 v[76:79], v[128:131], v[120:123], v[76:79]\n"
    "v_mfma_f32_16x16x32_bf16 v[80:83], v[136:139], v[120:123], v[80:83]\n"
    "s_waitcnt lgkmcnt(8)\n"
    "v_mfma_f32_16x16x32_bf16 v[80:83], v[132:135], v[116:119], v[80:83]\n"
    "s_barrier\n"
    "v_mfma_f32_16x16x32_bf16 v[4:7], v[140:143], v[84:87], v[4:7]\n"
    "ds_read_b64_tr_b16 v[112:113], v162 offset:8192\n"
    "v_mfma_f32_16x16x32_bf16 v[4:7], v[144:147], v[88:91], v[4:7]\n"
    "ds_read_b64_tr_b16 v[114:115], v163 offset:8192\n"
    "v_mfma_f32_16x16x32_bf16 v[8:11], v[152:155], v[88:91], v[8:11]\n"
    "ds_read_b64_tr_b16 v[108:109], v162 offset:3072\n"
    "v_mfma_f32_16x16x32_bf16 v[8:11], v[148:151], v[84:87], v[8:11]\n"
    "ds_read_b64_tr_b16 v[110:111], v163 offset:3072\n"
    "s_waitcnt lgkmcnt(8)\n"
    "v_mfma_f32_16x16x32_bf16 v[24:27], v[148:151], v[92:95], v[24:27]\n"
    "ds_read_b128 v[124:127], v158 offset:49152\n"
    "v_mfma_f32_16x16x32_bf16 v[24:27], v[152:155], v[96:99], v[24:27]\n"
    "ds_read_b128 v[128:131], v159 offset:49152\n"
    "v_mfma_f32_16x16x32_bf16 v[20:23], v[144:147], v[96:99], v[20:23]\n"
    "ds_read_b64_tr_b16 v[120:121], v162 offset:9216\n"
    "v_mfma_f32_16x16x32_bf16 v[20:23], v[140:143], v[92:95], v[20:23]\n"
    "ds_read_b64_tr_b16 v[122:123], v163 offset:9216\n"
    "s_waitcnt lgkmcnt(8)\n"
    "v_mfma_f32_16x16x32_bf16 v[36:39], v[140:143], v[100:103], v[36:39]\n"
    "ds_read_b64_tr_b16 v[116:117], v162 offset:4096\n"
    "v_mfma_f32_16x16x32_bf16 v[36:39], v[144:147], v[104:107], v[36:39]\n"
    "ds_read_b64_tr_b16 v[118:119], v163 offset:4096\n"
    "ds_read_b128 v[136:139], v159 offset:57344\n"
    "v_mfma_f32_16x16x32_bf16 v[40:43], v[152:155], v[104:107], v[40:43]\n"
    "ds_read_b128 v[132:135], v158 offset:57344\n"
    "v_mfma_f32_16x16x32_bf16 v[40:43], v[148:151], v[100:103], v[40:43]\n"
    "s_waitcnt lgkmcnt(8)\n"
    "v_mfma_f32_16x16x32_bf16 v[56:59], v[148:151], v[108:111], v[56:59]\n"
    "v_mfma_f32_16x16x32_bf16 v[56:59], v[152:155], v[112:115], v[56:59]\n"
    "v_mfma_f32_16x16x32_bf16 v[52:55], v[144:147], v[112:115], v[52:55]\n"
    "v_mfma_f32_16x16x32_bf16 v[52:55], v[140:143], v[108:111], v[52:55]\n"
    "s_waitcnt lgkmcnt(2)\n"
    "v_mfma_f32_16x16x32_bf16 v[68:71], v[140:143], v[116:119], v[68:71]\n"
    "v_mfma_f32_16x16x32_bf16 v[68:71], v[144:147], v[120:123], v[68:71]\n"
    "v_mfma_f32_16x16x32_bf16 v[72:75], v[152:155], v[120:123], v[72:75]\n"
    "s_waitcnt lgkmcnt(0)\n"
    "v_mfma_f32_16x16x32_bf16 v[72:75], v[148:151], v[116:119], v[72:75]\n"
    "s_barrier\n"
    "v_mfma_f32_16x16x32_bf16 v[12:15], v[124:127], v[84:87], v[12:15]\n"
    "ds_read_b128 v[140:143], v158 offset:8192\n"
    "v_mfma_f32_16x16x32_bf16 v[12:15], v[128:131], v[88:91], v[12:15]\n"
    "ds_read_b128 v[144:147], v159 offset:8192\n"
    "v_mfma_f32_16x16x32_bf16 v[16:19], v[136:139], v[88:91], v[16:19]\n"
    "ds_read_b128 v[152:155], v159 offset:16384\n"
    "v_mfma_f32_16x16x32_bf16 v[16:19], v[132:135], v[84:87], v[16:19]\n"
    "ds_read_b128 v[148:151], v158 offset:16384\n"
    "v_mfma_f32_16x16x32_bf16 v[32:35], v[132:135], v[92:95], v[32:35]\n"
    "ds_read_b64_tr_b16 v[88:89], v162 offset:15360\n"
    "v_mfma_f32_16x16x32_bf16 v[32:35], v[136:139], v[96:99], v[32:35]\n"
    "ds_read_b64_tr_b16 v[90:91], v163 offset:15360\n"
    "v_mfma_f32_16x16x32_bf16 v[28:31], v[128:131], v[96:99], v[28:31]\n"
    "ds_read_b64_tr_b16 v[84:85], v162 offset:10240\n"
    "v_mfma_f32_16x16x32_bf16 v[28:31], v[124:127], v[92:95], v[28:31]\n"
    "ds_read_b64_tr_b16 v[86:87], v163 offset:10240\n"
    "v_mfma_f32_16x16x32_bf16 v[44:47], v[124:127], v[100:103], v[44:47]\n"
    "ds_read_b64_tr_b16 v[96:97], v162 offset:16384\n"
    "v_mfma_f32_16x16x32_bf16 v[44:47], v[128:131], v[104:107], v[44:47]\n"
    "ds_read_b64_tr_b16 v[98:99], v163 offset:16384\n"
    "v_mfma_f32_16x16x32_bf16 v[48:51], v[136:139], v[104:107], v[48:51]\n"
    "ds_read_b64_tr_b16 v[92:93], v162 offset:11264\n"
    "v_mfma_f32_16x16x32_bf16 v[48:51], v[132:135], v[100:103], v[48:51]\n"
    "ds_read_b64_tr_b16 v[94:95], v163 offset:11264\n"
    "v_mfma_f32_16x16x32_bf16 v[64:67], v[132:135], v[108:111], v[64:67]\n"
    "ds_read_b64_tr_b16 v[104:105], v162 offset:17408\n"
    "v_mfma_f32_16x16x32_bf16 v[64:67], v[136:139], v[112:115], v[64:67]\n"
    "ds_read_b64_tr_b16 v[106:107], v163 offset:17408\n"
    "v_mfma_f32_16x16x32_bf16 v[60:63], v[128:131], v[112:115], v[60:63]\n"
    "ds_read_b64_tr_b16 v[100:101], v162 offset:12288\n"
    "v_mfma_f32_16x16x32_bf16 v[60:63], v[124:127], v[108:111], v[60:63]\n"
    "ds_read_b64_tr_b16 v[102:103], v163 offset:12288\n"
    "v_mfma_f32_16x16x32_bf16 v[76:79], v[124:127], v[116:119], v[76:79]\n"
    "v_mfma_f32_16x16x32_bf16 v[76:79], v[128:131], v[120:123], v[76:79]\n"
    "v_mfma_f32_16x16x32_bf16 v[80:83], v[136:139], v[120:123], v[80:83]\n"
    "s_waitcnt lgkmcnt(8)\n"
    "v_mfma_f32_16x16x32_bf16 v[80:83], v[132:135], v[116:119], v[80:83]\n"
    "s_barrier\n"
    "v_mfma_f32_16x16x32_bf16 v[4:7], v[140:143], v[84:87], v[4:7]\n"
    "ds_read_b64_tr_b16 v[112:113], v162 offset:18432\n"
    "v_mfma_f32_16x16x32_bf16 v[4:7], v[144:147], v[88:91], v[4:7]\n"
    "ds_read_b64_tr_b16 v[114:115], v163 offset:18432\n"
    "v_mfma_f32_16x16x32_bf16 v[8:11], v[152:155], v[88:91], v[8:11]\n"
    "ds_read_b64_tr_b16 v[108:109], v162 offset:13312\n"
    "v_mfma_f32_16x16x32_bf16 v[8:11], v[148:151], v[84:87], v[8:11]\n"
    "ds_read_b64_tr_b16 v[110:111], v163 offset:13312\n"
    "s_waitcnt lgkmcnt(8)\n"
    "v_mfma_f32_16x16x32_bf16 v[24:27], v[148:151], v[92:95], v[24:27]\n"
    "ds_read_b128 v[124:127], v158 offset:24576\n"
    "v_mfma_f32_16x16x32_bf16 v[24:27], v[152:155], v[96:99], v[24:27]\n"
    "ds_read_b128 v[128:131], v159 offset:24576\n"
    "v_mfma_f32_16x16x32_bf16 v[20:23], v[144:147], v[96:99], v[20:23]\n"
    "ds_read_b64_tr_b16 v[120:121], v162 offset:19456\n"
    "v_mfma_f32_16x16x32_bf16 v[20:23], v[140:143], v[92:95], v[20:23]\n"
    "ds_read_b64_tr_b16 v[122:123], v163 offset:19456\n"
    "s_waitcnt lgkmcnt(8)\n"
    "v_mfma_f32_16x16x32_bf16 v[36:39], v[140:143], v[100:103], v[36:39]\n"
    "ds_read_b64_tr_b16 v[116:117], v162 offset:14336\n"
    "v_mfma_f32_16x16x32_bf16 v[36:39], v[144:147], v[104:107], v[36:39]\n"
    "ds_read_b64_tr_b16 v[118:119], v163 offset:14336\n"
    "ds_read_b128 v[136:139], v159 offset:32768\n"
    "v_mfma_f32_16x16x32_bf16 v[40:43], v[152:155], v[104:107], v[40:43]\n"
    "ds_read_b128 v[132:135], v158 offset:32768\n"
    "v_mfma_f32_16x16x32_bf16 v[40:43], v[148:151], v[100:103], v[40:43]\n"
    "s_waitcnt lgkmcnt(8)\n"
    "v_mfma_f32_16x16x32_bf16 v[56:59], v[148:151], v[108:111], v[56:59]\n"
    "v_mfma_f32_16x16x32_bf16 v[56:59], v[152:155], v[112:115], v[56:59]\n"
    "v_mfma_f32_16x16x32_bf16 v[52:55], v[144:147], v[112:115], v[52:55]\n"
    "v_mfma_f32_16x16x32_bf16 v[52:55], v[140:143], v[108:111], v[52:55]\n"
    ".Lpage13:\n"
    "s_cbranch_execz .Ltouch_next\n"
    "s_waitcnt lgkmcnt(2)\n"
    "v_mfma_f32_16x16x32_bf16 v[68:71], v[140:143], v[116:119], v[68:71]\n"
    "v_mfma_f32_16x16x32_bf16 v[68:71], v[144:147], v[120:123], v[68:71]\n"
    "v_mfma_f32_16x16x32_bf16 v[72:75], v[152:155], v[120:123], v[72:75]\n"
    "s_waitcnt lgkmcnt(0)\n"
    "v_mfma_f32_16x16x32_bf16 v[72:75], v[148:151], v[116:119], v[72:75]\n"
    "s_barrier\n"
    "v_mfma_f32_16x16x32_bf16 v[12:15], v[124:127], v[84:87], v[12:15]\n"
    "ds_read_b64_tr_b16 v[140:141], v160 offset:40960\n"
    "v_mfma_f32_16x16x32_bf16 v[12:15], v[128:131], v[88:91], v[12:15]\n"
    "ds_read_b64_tr_b16 v[142:143], v160 offset:49152\n"
    "v_mfma_f32_16x16x32_bf16 v[16:19], v[136:139], v[88:91], v[16:19]\n"
    "ds_read_b64_tr_b16 v[144:145], v160 offset:43008\n"
    "v_mfma_f32_16x16x32_bf16 v[16:19], v[132:135], v[84:87], v[16:19]\n"
    "ds_read_b64_tr_b16 v[146:147], v160 offset:51200\n"
    "v_mfma_f32_16x16x32_bf16 v[32:35], v[132:135], v[92:95], v[32:35]\n"
    "ds_read_b64_tr_b16 v[152:153], v161 offset:43008\n"
    "v_mfma_f32_16x16x32_bf16 v[32:35], v[136:139], v[96:99], v[32:35]\n"
    "ds_read_b64_tr_b16 v[154:155], v161 offset:51200\n"
    "v_mfma_f32_16x16x32_bf16 v[28:31], v[128:131], v[96:99], v[28:31]\n"
    "ds_read_b64_tr_b16 v[148:149], v161 offset:40960\n"
    "v_mfma_f32_16x16x32_bf16 v[28:31], v[124:127], v[92:95], v[28:31]\n"
    "ds_read_b128 v[88:91], v156 offset:46080\n"
    "v_mfma_f32_16x16x32_bf16 v[44:47], v[124:127], v[100:103], v[44:47]\n"
    "ds_read_b128 v[84:87], v156 offset:40960\n"
    "v_mfma_f32_16x16x32_bf16 v[44:47], v[128:131], v[104:107], v[44:47]\n"
    "ds_read_b64_tr_b16 v[150:151], v161 offset:49152\n"
    "v_mfma_f32_16x16x32_bf16 v[48:51], v[136:139], v[104:107], v[48:51]\n"
    "ds_read_b128 v[96:99], v156 offset:47104\n"
    "v_mfma_f32_16x16x32_bf16 v[48:51], v[132:135], v[100:103], v[48:51]\n"
    "ds_read_b128 v[92:95], v156 offset:41984\n"
    "v_mfma_f32_16x16x32_bf16 v[64:67], v[132:135], v[108:111], v[64:67]\n"
    "ds_read_b128 v[104:107], v156 offset:48128\n"
    "v_mfma_f32_16x16x32_bf16 v[64:67], v[136:139], v[112:115], v[64:67]\n"
    "ds_read_b128 v[100:103], v156 offset:43008\n"
    "v_mfma_f32_16x16x32_bf16 v[60:63], v[128:131], v[112:115], v[60:63]\n"
    "v_mfma_f32_16x16x32_bf16 v[60:63], v[124:127], v[108:111], v[60:63]\n"
    "v_mfma_f32_16x16x32_bf16 v[76:79], v[124:127], v[116:119], v[76:79]\n"
    "v_mfma_f32_16x16x32_bf16 v[76:79], v[128:131], v[120:123], v[76:79]\n"
    "v_mfma_f32_16x16x32_bf16 v[80:83], v[136:139], v[120:123], v[80:83]\n"
    "s_waitcnt lgkmcnt(4)\n"
    "v_mfma_f32_16x16x32_bf16 v[80:83], v[132:135], v[116:119], v[80:83]\n"
    "s_barrier\n"
    "v_mfma_f32_16x16x32_bf16 v[4:7], v[140:143], v[84:87], v[4:7]\n"
    "ds_read_b128 v[112:115], v156 offset:49152\n"
    "v_mfma_f32_16x16x32_bf16 v[4:7], v[144:147], v[88:91], v[4:7]\n"
    "ds_read_b128 v[108:111], v156 offset:44032\n"
    "v_mfma_f32_16x16x32_bf16 v[8:11], v[152:155], v[88:91], v[8:11]\n"
    "ds_read_b64_tr_b16 v[124:125], v160 offset:57344\n"
    "v_mfma_f32_16x16x32_bf16 v[8:11], v[148:151], v[84:87], v[8:11]\n"
    "ds_read_b64_tr_b16 v[126:127], v160 offset:8192\n"
    "s_waitcnt lgkmcnt(6)\n"
    "v_mfma_f32_16x16x32_bf16 v[24:27], v[148:151], v[92:95], v[24:27]\n"
    "ds_read_b128 v[120:123], v156 offset:50176\n"
    "v_mfma_f32_16x16x32_bf16 v[24:27], v[152:155], v[96:99], v[24:27]\n"
    "ds_read_b128 v[116:119], v156 offset:45056\n"
    "v_mfma_f32_16x16x32_bf16 v[20:23], v[144:147], v[96:99], v[20:23]\n"
    "ds_read_b64_tr_b16 v[128:129], v160 offset:59392\n"
    "v_mfma_f32_16x16x32_bf16 v[20:23], v[140:143], v[92:95], v[20:23]\n"
    "ds_read_b64_tr_b16 v[130:131], v160 offset:10240\n"
    "s_waitcnt lgkmcnt(8)\n"
    "v_mfma_f32_16x16x32_bf16 v[36:39], v[140:143], v[100:103], v[36:39]\n"
    "ds_read_b64_tr_b16 v[136:137], v161 offset:59392\n"
    "v_mfma_f32_16x16x32_bf16 v[36:39], v[144:147], v[104:107], v[36:39]\n"
    "ds_read_b64_tr_b16 v[138:139], v161 offset:10240\n"
    "v_mfma_f32_16x16x32_bf16 v[40:43], v[152:155], v[104:107], v[40:43]\n"
    "ds_read_b64_tr_b16 v[132:133], v161 offset:57344\n"
    "v_mfma_f32_16x16x32_bf16 v[40:43], v[148:151], v[100:103], v[40:43]\n"
    "ds_read_b64_tr_b16 v[134:135], v161 offset:8192\n"
    "s_waitcnt lgkmcnt(10)\n"
    "v_mfma_f32_16x16x32_bf16 v[56:59], v[148:151], v[108:111], v[56:59]\n"
    "v_mfma_f32_16x16x32_bf16 v[56:59], v[152:155], v[112:115], v[56:59]\n"
    "v_mfma_f32_16x16x32_bf16 v[52:55], v[144:147], v[112:115], v[52:55]\n"
    "v_mfma_f32_16x16x32_bf16 v[52:55], v[140:143], v[108:111], v[52:55]\n"
    "s_waitcnt lgkmcnt(6)\n"
    "v_mfma_f32_16x16x32_bf16 v[68:71], v[140:143], v[116:119], v[68:71]\n"
    "v_mfma_f32_16x16x32_bf16 v[68:71], v[144:147], v[120:123], v[68:71]\n"
    "v_mfma_f32_16x16x32_bf16 v[72:75], v[152:155], v[120:123], v[72:75]\n"
    "s_waitcnt lgkmcnt(0)\n"
    "v_mfma_f32_16x16x32_bf16 v[72:75], v[148:151], v[116:119], v[72:75]\n"
    "s_barrier\n"
    "v_mfma_f32_16x16x32_bf16 v[12:15], v[124:127], v[84:87], v[12:15]\n"
    "ds_read_b64_tr_b16 v[140:141], v160 offset:16384\n"
    "v_mfma_f32_16x16x32_bf16 v[12:15], v[128:131], v[88:91], v[12:15]\n"
    "ds_read_b64_tr_b16 v[142:143], v160 offset:24576\n"
    "v_mfma_f32_16x16x32_bf16 v[16:19], v[136:139], v[88:91], v[16:19]\n"
    "ds_read_b64_tr_b16 v[144:145], v160 offset:18432\n"
    "v_mfma_f32_16x16x32_bf16 v[16:19], v[132:135], v[84:87], v[16:19]\n"
    "ds_read_b64_tr_b16 v[146:147], v160 offset:26624\n"
    "v_mfma_f32_16x16x32_bf16 v[32:35], v[132:135], v[92:95], v[32:35]\n"
    "ds_read_b64_tr_b16 v[152:153], v161 offset:18432\n"
    "v_mfma_f32_16x16x32_bf16 v[32:35], v[136:139], v[96:99], v[32:35]\n"
    "ds_read_b64_tr_b16 v[154:155], v161 offset:26624\n"
    "v_mfma_f32_16x16x32_bf16 v[28:31], v[128:131], v[96:99], v[28:31]\n"
    "ds_read_b64_tr_b16 v[148:149], v161 offset:16384\n"
    "v_mfma_f32_16x16x32_bf16 v[28:31], v[124:127], v[92:95], v[28:31]\n"
    "ds_read_b128 v[88:91], v157 offset:5120\n"
    "v_mfma_f32_16x16x32_bf16 v[44:47], v[124:127], v[100:103], v[44:47]\n"
    "ds_read_b128 v[84:87], v157 offset:0\n"
    "v_mfma_f32_16x16x32_bf16 v[44:47], v[128:131], v[104:107], v[44:47]\n"
    "ds_read_b64_tr_b16 v[150:151], v161 offset:24576\n"
    "v_mfma_f32_16x16x32_bf16 v[48:51], v[136:139], v[104:107], v[48:51]\n"
    "ds_read_b128 v[96:99], v157 offset:6144\n"
    "v_mfma_f32_16x16x32_bf16 v[48:51], v[132:135], v[100:103], v[48:51]\n"
    "ds_read_b128 v[92:95], v157 offset:1024\n"
    "v_mfma_f32_16x16x32_bf16 v[64:67], v[132:135], v[108:111], v[64:67]\n"
    "ds_read_b128 v[104:107], v157 offset:7168\n"
    "v_mfma_f32_16x16x32_bf16 v[64:67], v[136:139], v[112:115], v[64:67]\n"
    "ds_read_b128 v[100:103], v157 offset:2048\n"
    "v_mfma_f32_16x16x32_bf16 v[60:63], v[128:131], v[112:115], v[60:63]\n"
    "v_mfma_f32_16x16x32_bf16 v[60:63], v[124:127], v[108:111], v[60:63]\n"
    "v_mfma_f32_16x16x32_bf16 v[76:79], v[124:127], v[116:119], v[76:79]\n"
    "v_mfma_f32_16x16x32_bf16 v[76:79], v[128:131], v[120:123], v[76:79]\n"
    "v_mfma_f32_16x16x32_bf16 v[80:83], v[136:139], v[120:123], v[80:83]\n"
    "s_waitcnt lgkmcnt(4)\n"
    "v_mfma_f32_16x16x32_bf16 v[80:83], v[132:135], v[116:119], v[80:83]\n"
    "s_barrier\n"
    "v_mfma_f32_16x16x32_bf16 v[4:7], v[140:143], v[84:87], v[4:7]\n"
    "ds_read_b128 v[112:115], v157 offset:8192\n"
    "v_mfma_f32_16x16x32_bf16 v[4:7], v[144:147], v[88:91], v[4:7]\n"
    "ds_read_b128 v[108:111], v157 offset:3072\n"
    "v_mfma_f32_16x16x32_bf16 v[8:11], v[152:155], v[88:91], v[8:11]\n"
    "ds_read_b64_tr_b16 v[124:125], v160 offset:32768\n"
    "v_mfma_f32_16x16x32_bf16 v[8:11], v[148:151], v[84:87], v[8:11]\n"
    "ds_read_b64_tr_b16 v[126:127], v160 offset:40960\n"
    "s_waitcnt lgkmcnt(6)\n"
    "v_mfma_f32_16x16x32_bf16 v[24:27], v[148:151], v[92:95], v[24:27]\n"
    "ds_read_b128 v[120:123], v157 offset:9216\n"
    "v_mfma_f32_16x16x32_bf16 v[24:27], v[152:155], v[96:99], v[24:27]\n"
    "ds_read_b128 v[116:119], v157 offset:4096\n"
    "v_mfma_f32_16x16x32_bf16 v[20:23], v[144:147], v[96:99], v[20:23]\n"
    "ds_read_b64_tr_b16 v[128:129], v160 offset:34816\n"
    "v_mfma_f32_16x16x32_bf16 v[20:23], v[140:143], v[92:95], v[20:23]\n"
    "ds_read_b64_tr_b16 v[130:131], v160 offset:43008\n"
    "s_waitcnt lgkmcnt(8)\n"
    "v_mfma_f32_16x16x32_bf16 v[36:39], v[140:143], v[100:103], v[36:39]\n"
    "ds_read_b64_tr_b16 v[136:137], v161 offset:34816\n"
    "v_mfma_f32_16x16x32_bf16 v[36:39], v[144:147], v[104:107], v[36:39]\n"
    "ds_read_b64_tr_b16 v[138:139], v161 offset:43008\n"
    "v_mfma_f32_16x16x32_bf16 v[40:43], v[152:155], v[104:107], v[40:43]\n"
    "ds_read_b64_tr_b16 v[132:133], v161 offset:32768\n"
    "v_mfma_f32_16x16x32_bf16 v[40:43], v[148:151], v[100:103], v[40:43]\n"
    "ds_read_b64_tr_b16 v[134:135], v161 offset:40960\n"
    "s_waitcnt lgkmcnt(10)\n"
    "v_mfma_f32_16x16x32_bf16 v[56:59], v[148:151], v[108:111], v[56:59]\n"
    "v_mfma_f32_16x16x32_bf16 v[56:59], v[152:155], v[112:115], v[56:59]\n"
    "v_mfma_f32_16x16x32_bf16 v[52:55], v[144:147], v[112:115], v[52:55]\n"
    "v_mfma_f32_16x16x32_bf16 v[52:55], v[140:143], v[108:111], v[52:55]\n"
    "s_waitcnt lgkmcnt(6)\n"
    "v_mfma_f32_16x16x32_bf16 v[68:71], v[140:143], v[116:119], v[68:71]\n"
    "v_mfma_f32_16x16x32_bf16 v[68:71], v[144:147], v[120:123], v[68:71]\n"
    "v_mfma_f32_16x16x32_bf16 v[72:75], v[152:155], v[120:123], v[72:75]\n"
    "s_waitcnt lgkmcnt(0)\n"
    "v_mfma_f32_16x16x32_bf16 v[72:75], v[148:151], v[116:119], v[72:75]\n"
    "s_barrier\n"
    "v_mfma_f32_16x16x32_bf16 v[12:15], v[124:127], v[84:87], v[12:15]\n"
    "ds_read_b64_tr_b16 v[140:141], v160 offset:49152\n"
    "v_mfma_f32_16x16x32_bf16 v[12:15], v[128:131], v[88:91], v[12:15]\n"
    "ds_read_b64_tr_b16 v[142:143], v160 offset:57344\n"
    "v_mfma_f32_16x16x32_bf16 v[16:19], v[136:139], v[88:91], v[16:19]\n"
    "ds_read_b64_tr_b16 v[144:145], v160 offset:51200\n"
    "v_mfma_f32_16x16x32_bf16 v[16:19], v[132:135], v[84:87], v[16:19]\n"
    "ds_read_b64_tr_b16 v[146:147], v160 offset:59392\n"
    "v_mfma_f32_16x16x32_bf16 v[32:35], v[132:135], v[92:95], v[32:35]\n"
    "ds_read_b64_tr_b16 v[152:153], v161 offset:51200\n"
    "v_mfma_f32_16x16x32_bf16 v[32:35], v[136:139], v[96:99], v[32:35]\n"
    "ds_read_b64_tr_b16 v[154:155], v161 offset:59392\n"
    "v_mfma_f32_16x16x32_bf16 v[28:31], v[128:131], v[96:99], v[28:31]\n"
    "ds_read_b64_tr_b16 v[148:149], v161 offset:49152\n"
    "v_mfma_f32_16x16x32_bf16 v[28:31], v[124:127], v[92:95], v[28:31]\n"
    "ds_read_b128 v[88:91], v157 offset:15360\n"
    "v_mfma_f32_16x16x32_bf16 v[44:47], v[124:127], v[100:103], v[44:47]\n"
    "ds_read_b128 v[84:87], v157 offset:10240\n"
    "v_mfma_f32_16x16x32_bf16 v[44:47], v[128:131], v[104:107], v[44:47]\n"
    "ds_read_b64_tr_b16 v[150:151], v161 offset:57344\n"
    "v_mfma_f32_16x16x32_bf16 v[48:51], v[136:139], v[104:107], v[48:51]\n"
    "ds_read_b128 v[96:99], v157 offset:16384\n"
    "v_mfma_f32_16x16x32_bf16 v[48:51], v[132:135], v[100:103], v[48:51]\n"
    "ds_read_b128 v[92:95], v157 offset:11264\n"
    "v_mfma_f32_16x16x32_bf16 v[64:67], v[132:135], v[108:111], v[64:67]\n"
    "ds_read_b128 v[104:107], v157 offset:17408\n"
    "v_mfma_f32_16x16x32_bf16 v[64:67], v[136:139], v[112:115], v[64:67]\n"
    "ds_read_b128 v[100:103], v157 offset:12288\n"
    "v_mfma_f32_16x16x32_bf16 v[60:63], v[128:131], v[112:115], v[60:63]\n"
    "v_mfma_f32_16x16x32_bf16 v[60:63], v[124:127], v[108:111], v[60:63]\n"
    "v_mfma_f32_16x16x32_bf16 v[76:79], v[124:127], v[116:119], v[76:79]\n"
    "v_mfma_f32_16x16x32_bf16 v[76:79], v[128:131], v[120:123], v[76:79]\n"
    "v_mfma_f32_16x16x32_bf16 v[80:83], v[136:139], v[120:123], v[80:83]\n"
    "s_waitcnt lgkmcnt(4)\n"
    "v_mfma_f32_16x16x32_bf16 v[80:83], v[132:135], v[116:119], v[80:83]\n"
    "s_barrier\n"
    "v_mfma_f32_16x16x32_bf16 v[4:7], v[140:143], v[84:87], v[4:7]\n"
    "ds_read_b128 v[112:115], v157 offset:18432\n"
    "v_mfma_f32_16x16x32_bf16 v[4:7], v[144:147], v[88:91], v[4:7]\n"
    "ds_read_b128 v[108:111], v157 offset:13312\n"
    "v_mfma_f32_16x16x32_bf16 v[8:11], v[152:155], v[88:91], v[8:11]\n"
    "ds_read_b64_tr_b16 v[124:125], v160 offset:8192\n"
    "v_mfma_f32_16x16x32_bf16 v[8:11], v[148:151], v[84:87], v[8:11]\n"
    "ds_read_b64_tr_b16 v[126:127], v160 offset:16384\n"
    "s_waitcnt lgkmcnt(6)\n"
    "v_mfma_f32_16x16x32_bf16 v[24:27], v[148:151], v[92:95], v[24:27]\n"
    "ds_read_b128 v[120:123], v157 offset:19456\n"
    "v_mfma_f32_16x16x32_bf16 v[24:27], v[152:155], v[96:99], v[24:27]\n"
    "ds_read_b128 v[116:119], v157 offset:14336\n"
    "v_mfma_f32_16x16x32_bf16 v[20:23], v[144:147], v[96:99], v[20:23]\n"
    "ds_read_b64_tr_b16 v[128:129], v160 offset:10240\n"
    "v_mfma_f32_16x16x32_bf16 v[20:23], v[140:143], v[92:95], v[20:23]\n"
    "ds_read_b64_tr_b16 v[130:131], v160 offset:18432\n"
    "s_waitcnt lgkmcnt(8)\n"
    "v_mfma_f32_16x16x32_bf16 v[36:39], v[140:143], v[100:103], v[36:39]\n"
    "ds_read_b64_tr_b16 v[136:137], v161 offset:10240\n"
    "v_mfma_f32_16x16x32_bf16 v[36:39], v[144:147], v[104:107], v[36:39]\n"
    "ds_read_b64_tr_b16 v[138:139], v161 offset:18432\n"
    "v_mfma_f32_16x16x32_bf16 v[40:43], v[152:155], v[104:107], v[40:43]\n"
    "ds_read_b64_tr_b16 v[132:133], v161 offset:8192\n"
    "v_mfma_f32_16x16x32_bf16 v[40:43], v[148:151], v[100:103], v[40:43]\n"
    "ds_read_b64_tr_b16 v[134:135], v161 offset:16384\n"
    "s_waitcnt lgkmcnt(10)\n"
    "v_mfma_f32_16x16x32_bf16 v[56:59], v[148:151], v[108:111], v[56:59]\n"
    "v_mfma_f32_16x16x32_bf16 v[56:59], v[152:155], v[112:115], v[56:59]\n"
    "v_mfma_f32_16x16x32_bf16 v[52:55], v[144:147], v[112:115], v[52:55]\n"
    "v_mfma_f32_16x16x32_bf16 v[52:55], v[140:143], v[108:111], v[52:55]\n"
    "s_waitcnt lgkmcnt(6)\n"
    "v_mfma_f32_16x16x32_bf16 v[68:71], v[140:143], v[116:119], v[68:71]\n"
    "v_mfma_f32_16x16x32_bf16 v[68:71], v[144:147], v[120:123], v[68:71]\n"
    "v_mfma_f32_16x16x32_bf16 v[72:75], v[152:155], v[120:123], v[72:75]\n"
    "s_waitcnt lgkmcnt(0)\n"
    "v_mfma_f32_16x16x32_bf16 v[72:75], v[148:151], v[116:119], v[72:75]\n"
    "s_barrier\n"
    "v_mfma_f32_16x16x32_bf16 v[12:15], v[124:127], v[84:87], v[12:15]\n"
    "ds_read_b64_tr_b16 v[140:141], v160 offset:24576\n"
    "v_mfma_f32_16x16x32_bf16 v[12:15], v[128:131], v[88:91], v[12:15]\n"
    "ds_read_b64_tr_b16 v[142:143], v160 offset:32768\n"
    "v_mfma_f32_16x16x32_bf16 v[16:19], v[136:139], v[88:91], v[16:19]\n"
    "ds_read_b64_tr_b16 v[144:145], v160 offset:26624\n"
    "v_mfma_f32_16x16x32_bf16 v[16:19], v[132:135], v[84:87], v[16:19]\n"
    "ds_read_b64_tr_b16 v[146:147], v160 offset:34816\n"
    "v_mfma_f32_16x16x32_bf16 v[32:35], v[132:135], v[92:95], v[32:35]\n"
    "ds_read_b64_tr_b16 v[152:153], v161 offset:26624\n"
    "v_mfma_f32_16x16x32_bf16 v[32:35], v[136:139], v[96:99], v[32:35]\n"
    "ds_read_b64_tr_b16 v[154:155], v161 offset:34816\n"
    "v_mfma_f32_16x16x32_bf16 v[28:31], v[128:131], v[96:99], v[28:31]\n"
    "ds_read_b64_tr_b16 v[148:149], v161 offset:24576\n"
    "v_mfma_f32_16x16x32_bf16 v[28:31], v[124:127], v[92:95], v[28:31]\n"
    "ds_read_b128 v[88:91], v157 offset:25600\n"
    "v_mfma_f32_16x16x32_bf16 v[44:47], v[124:127], v[100:103], v[44:47]\n"
    "ds_read_b128 v[84:87], v157 offset:20480\n"
    "v_mfma_f32_16x16x32_bf16 v[44:47], v[128:131], v[104:107], v[44:47]\n"
    "ds_read_b64_tr_b16 v[150:151], v161 offset:32768\n"
    "v_mfma_f32_16x16x32_bf16 v[48:51], v[136:139], v[104:107], v[48:51]\n"
    "ds_read_b128 v[96:99], v157 offset:26624\n"
    "v_mfma_f32_16x16x32_bf16 v[48:51], v[132:135], v[100:103], v[48:51]\n"
    "ds_read_b128 v[92:95], v157 offset:21504\n"
    "v_mfma_f32_16x16x32_bf16 v[64:67], v[132:135], v[108:111], v[64:67]\n"
    "ds_read_b128 v[104:107], v157 offset:27648\n"
    "v_mfma_f32_16x16x32_bf16 v[64:67], v[136:139], v[112:115], v[64:67]\n"
    "ds_read_b128 v[100:103], v157 offset:22528\n"
    "v_mfma_f32_16x16x32_bf16 v[60:63], v[128:131], v[112:115], v[60:63]\n"
    "v_mfma_f32_16x16x32_bf16 v[60:63], v[124:127], v[108:111], v[60:63]\n"
    "v_mfma_f32_16x16x32_bf16 v[76:79], v[124:127], v[116:119], v[76:79]\n"
    "v_mfma_f32_16x16x32_bf16 v[76:79], v[128:131], v[120:123], v[76:79]\n"
    "v_mfma_f32_16x16x32_bf16 v[80:83], v[136:139], v[120:123], v[80:83]\n"
    "s_waitcnt lgkmcnt(4)\n"
    "v_mfma_f32_16x16x32_bf16 v[80:83], v[132:135], v[116:119], v[80:83]\n"
    "s_barrier\n"
    "v_mfma_f32_16x16x32_bf16 v[4:7], v[140:143], v[84:87], v[4:7]\n"
    "ds_read_b128 v[112:115], v157 offset:28672\n"
    "v_mfma_f32_16x16x32_bf16 v[4:7], v[144:147], v[88:91], v[4:7]\n"
    "ds_read_b128 v[108:111], v157 offset:23552\n"
    "v_mfma_f32_16x16x32_bf16 v[8:11], v[152:155], v[88:91], v[8:11]\n"
    "ds_read_b64_tr_b16 v[124:125], v160 offset:40960\n"
    "v_mfma_f32_16x16x32_bf16 v[8:11], v[148:151], v[84:87], v[8:11]\n"
    "ds_read_b64_tr_b16 v[126:127], v160 offset:49152\n"
    "s_waitcnt lgkmcnt(6)\n"
    "v_mfma_f32_16x16x32_bf16 v[24:27], v[148:151], v[92:95], v[24:27]\n"
    "ds_read_b128 v[120:123], v157 offset:29696\n"
    "v_mfma_f32_16x16x32_bf16 v[24:27], v[152:155], v[96:99], v[24:27]\n"
    "ds_read_b128 v[116:119], v157 offset:24576\n"
    "v_mfma_f32_16x16x32_bf16 v[20:23], v[144:147], v[96:99], v[20:23]\n"
    "ds_read_b64_tr_b16 v[128:129], v160 offset:43008\n"
    "v_mfma_f32_16x16x32_bf16 v[20:23], v[140:143], v[92:95], v[20:23]\n"
    "ds_read_b64_tr_b16 v[130:131], v160 offset:51200\n"
    "s_waitcnt lgkmcnt(8)\n"
    "v_mfma_f32_16x16x32_bf16 v[36:39], v[140:143], v[100:103], v[36:39]\n"
    "ds_read_b64_tr_b16 v[136:137], v161 offset:43008\n"
    "v_mfma_f32_16x16x32_bf16 v[36:39], v[144:147], v[104:107], v[36:39]\n"
    "ds_read_b64_tr_b16 v[138:139], v161 offset:51200\n"
    "v_mfma_f32_16x16x32_bf16 v[40:43], v[152:155], v[104:107], v[40:43]\n"
    "ds_read_b64_tr_b16 v[132:133], v161 offset:40960\n"
    "v_mfma_f32_16x16x32_bf16 v[40:43], v[148:151], v[100:103], v[40:43]\n"
    "ds_read_b64_tr_b16 v[134:135], v161 offset:49152\n"
    "s_waitcnt lgkmcnt(10)\n"
    "v_mfma_f32_16x16x32_bf16 v[56:59], v[148:151], v[108:111], v[56:59]\n"
    "v_mfma_f32_16x16x32_bf16 v[56:59], v[152:155], v[112:115], v[56:59]\n"
    "v_mfma_f32_16x16x32_bf16 v[52:55], v[144:147], v[112:115], v[52:55]\n"
    "v_mfma_f32_16x16x32_bf16 v[52:55], v[140:143], v[108:111], v[52:55]\n"
    "s_waitcnt lgkmcnt(6)\n"
    "v_mfma_f32_16x16x32_bf16 v[68:71], v[140:143], v[116:119], v[68:71]\n"
    "v_mfma_f32_16x16x32_bf16 v[68:71], v[144:147], v[120:123], v[68:71]\n"
    "v_mfma_f32_16x16x32_bf16 v[72:75], v[152:155], v[120:123], v[72:75]\n"
    "s_waitcnt lgkmcnt(0)\n"
    "v_mfma_f32_16x16x32_bf16 v[72:75], v[148:151], v[116:119], v[72:75]\n"
    "s_barrier\n"
    "v_mfma_f32_16x16x32_bf16 v[12:15], v[124:127], v[84:87], v[12:15]\n"
    "v_mfma_f32_16x16x32_bf16 v[12:15], v[128:131], v[88:91], v[12:15]\n"
    "v_mfma_f32_16x16x32_bf16 v[16:19], v[136:139], v[88:91], v[16:19]\n"
    "v_mfma_f32_16x16x32_bf16 v[16:19], v[132:135], v[84:87], v[16:19]\n"
    "v_mfma_f32_16x16x32_bf16 v[32:35], v[132:135], v[92:95], v[32:35]\n"
    "v_mfma_f32_16x16x32_bf16 v[32:35], v[136:139], v[96:99], v[32:35]\n"
    "v_mfma_f32_16x16x32_bf16 v[28:31], v[128:131], v[96:99], v[28:31]\n"
    "v_mfma_f32_16x16x32_bf16 v[28:31], v[124:127], v[92:95], v[28:31]\n"
    "v_mfma_f32_16x16x32_bf16 v[44:47], v[124:127], v[100:103], v[44:47]\n"
    "v_mfma_f32_16x16x32_bf16 v[44:47], v[128:131], v[104:107], v[44:47]\n"
    "v_mfma_f32_16x16x32_bf16 v[48:51], v[136:139], v[104:107], v[48:51]\n"
    "v_mfma_f32_16x16x32_bf16 v[48:51], v[132:135], v[100:103], v[48:51]\n"
    "v_mfma_f32_16x16x32_bf16 v[64:67], v[132:135], v[108:111], v[64:67]\n"
    "v_mfma_f32_16x16x32_bf16 v[64:67], v[136:139], v[112:115], v[64:67]\n"
    "v_mfma_f32_16x16x32_bf16 v[60:63], v[128:131], v[112:115], v[60:63]\n"
    "v_mfma_f32_16x16x32_bf16 v[60:63], v[124:127], v[108:111], v[60:63]\n"
    "v_mfma_f32_16x16x32_bf16 v[76:79], v[124:127], v[116:119], v[76:79]\n"
    "v_mfma_f32_16x16x32_bf16 v[76:79], v[128:131], v[120:123], v[76:79]\n"
    "v_mfma_f32_16x16x32_bf16 v[80:83], v[136:139], v[120:123], v[80:83]\n"
    "v_mfma_f32_16x16x32_bf16 v[80:83], v[132:135], v[116:119], v[80:83]\n"
    "s_setprio 0\n"
    "s_barrier\n"
    "s_mov_b32 s68, s64\n"
    "s_and_b32 s69, s65, 0xffff\n"
    "s_mov_b32 s70, 50331648\n"
    "s_mov_b32 s71, 0x20000\n"
    "s_add_u32 s29, s11, s14\n"
    "s_add_u32 s29, s29, 1\n"
    "s_sub_u32 s30, s29, 9\n"
    "s_cmp_ge_u32 s29, 9\n"
    "s_cselect_b32 s29, s30, s29\n"
    "s_mul_i32 s28, s10, 9\n"
    "s_add_u32 s28, s28, s29\n"
    "s_add_u32 s29, s14, 0\n"
    "s_lshl_b32 s88, s28, 11\n"
    "s_lshl_b32 s30, s29, 8\n"
    "s_add_u32 s88, s88, s30\n"
    "s_lshl_b32 s30, s15, 7\n"
    "s_add_u32 s88, s88, s30\n"
    "s_add_u32 s88, s88, 524288\n"
    "s_lshl_b32 s28, s28, 3\n"
    "s_add_u32 s28, s28, s29\n"
    "s_mul_i32 s28, s28, 10\n"
    "s_add_u32 s28, s28, s15\n"
    "s_lshl_b32 s28, s28, 10\n"
    "s_add_u32 s84, s28, 1048576\n"
    "v_cvt_pk_bf16_f32 v4, v4, v5\n"
    "v_cvt_pk_bf16_f32 v5, v6, v7\n"
    "v_cvt_pk_bf16_f32 v6, v8, v9\n"
    "v_cvt_pk_bf16_f32 v7, v10, v11\n"
    "buffer_store_dwordx4 v[4:7], v156, s[68:71], s84 offen offset:0 sc1\n"
    "v_cvt_pk_bf16_f32 v20, v20, v21\n"
    "v_cvt_pk_bf16_f32 v21, v22, v23\n"
    "v_cvt_pk_bf16_f32 v22, v24, v25\n"
    "v_cvt_pk_bf16_f32 v23, v26, v27\n"
    "buffer_store_dwordx4 v[20:23], v156, s[68:71], s84 offen offset:2048 sc1\n"
    "s_barrier\n"
    "v_cvt_pk_bf16_f32 v36, v36, v37\n"
    "v_cvt_pk_bf16_f32 v37, v38, v39\n"
    "v_cvt_pk_bf16_f32 v38, v40, v41\n"
    "v_cvt_pk_bf16_f32 v39, v42, v43\n"
    "s_add_u32 s31, s84, 4096\n"
    "buffer_store_dwordx4 v[36:39], v156, s[68:71], s31 offen offset:0 sc1\n"
    "s_barrier\n"
    "v_cvt_pk_bf16_f32 v52, v52, v53\n"
    "v_cvt_pk_bf16_f32 v53, v54, v55\n"
    "v_cvt_pk_bf16_f32 v54, v56, v57\n"
    "v_cvt_pk_bf16_f32 v55, v58, v59\n"
    "s_add_u32 s31, s84, 4096\n"
    "buffer_store_dwordx4 v[52:55], v156, s[68:71], s31 offen offset:2048 sc1\n"
    "s_barrier\n"
    "v_cvt_pk_bf16_f32 v68, v68, v69\n"
    "v_cvt_pk_bf16_f32 v69, v70, v71\n"
    "v_cvt_pk_bf16_f32 v70, v72, v73\n"
    "v_cvt_pk_bf16_f32 v71, v74, v75\n"
    "s_add_u32 s31, s84, 8192\n"
    "buffer_store_dwordx4 v[68:71], v156, s[68:71], s31 offen offset:0 sc1\n"
    "s_add_u32 s29, s11, s14\n"
    "s_add_u32 s29, s29, 3\n"
    "s_sub_u32 s30, s29, 9\n"
    "s_cmp_ge_u32 s29, 9\n"
    "s_cselect_b32 s29, s30, s29\n"
    "s_mul_i32 s28, s10, 9\n"
    "s_add_u32 s28, s28, s29\n"
    "s_add_u32 s29, s14, 2\n"
    "s_lshl_b32 s89, s28, 11\n"
    "s_lshl_b32 s30, s29, 8\n"
    "s_add_u32 s89, s89, s30\n"
    "s_lshl_b32 s30, s15, 7\n"
    "s_add_u32 s89, s89, s30\n"
    "s_add_u32 s89, s89, 524288\n"
    "s_lshl_b32 s28, s28, 3\n"
    "s_add_u32 s28, s28, s29\n"
    "s_mul_i32 s28, s28, 10\n"
    "s_add_u32 s28, s28, s15\n"
    "s_lshl_b32 s28, s28, 10\n"
    "s_add_u32 s85, s28, 1048576\n"
    "s_add_u32 s29, s11, s14\n"
    "s_add_u32 s29, s29, 7\n"
    "s_sub_u32 s30, s29, 9\n"
    "s_cmp_ge_u32 s29, 9\n"
    "s_cselect_b32 s29, s30, s29\n"
    "s_mul_i32 s28, s10, 9\n"
    "s_add_u32 s28, s28, s29\n"
    "s_add_u32 s29, s14, 6\n"
    "s_lshl_b32 s90, s28, 11\n"
    "s_lshl_b32 s30, s29, 8\n"
    "s_add_u32 s90, s90, s30\n"
    "s_lshl_b32 s30, s15, 7\n"
    "s_add_u32 s90, s90, s30\n"
    "s_add_u32 s90, s90, 524288\n"
    "s_lshl_b32 s28, s28, 3\n"
    "s_add_u32 s28, s28, s29\n"
    "s_mul_i32 s28, s28, 10\n"
    "s_add_u32 s28, s28, s15\n"
    "s_lshl_b32 s28, s28, 10\n"
    "s_add_u32 s86, s28, 1048576\n"
    "s_barrier\n"
    "v_cvt_pk_bf16_f32 v12, v12, v13\n"
    "v_cvt_pk_bf16_f32 v13, v14, v15\n"
    "v_cvt_pk_bf16_f32 v14, v16, v17\n"
    "v_cvt_pk_bf16_f32 v15, v18, v19\n"
    "buffer_store_dwordx4 v[12:15], v156, s[68:71], s85 offen offset:0 sc1\n"
    "s_barrier\n"
    "v_cvt_pk_bf16_f32 v28, v28, v29\n"
    "v_cvt_pk_bf16_f32 v29, v30, v31\n"
    "v_cvt_pk_bf16_f32 v30, v32, v33\n"
    "v_cvt_pk_bf16_f32 v31, v34, v35\n"
    "buffer_store_dwordx4 v[28:31], v156, s[68:71], s85 offen offset:2048 sc1\n"
    "v_cvt_pk_bf16_f32 v44, v44, v45\n"
    "v_cvt_pk_bf16_f32 v45, v46, v47\n"
    "v_cvt_pk_bf16_f32 v46, v48, v49\n"
    "v_cvt_pk_bf16_f32 v47, v50, v51\n"
    "s_add_u32 s31, s85, 4096\n"
    "buffer_store_dwordx4 v[44:47], v156, s[68:71], s31 offen offset:0 sc1\n"
    "s_barrier\n"
    "v_cvt_pk_bf16_f32 v60, v60, v61\n"
    "v_cvt_pk_bf16_f32 v61, v62, v63\n"
    "v_cvt_pk_bf16_f32 v62, v64, v65\n"
    "v_cvt_pk_bf16_f32 v63, v66, v67\n"
    "s_add_u32 s31, s85, 4096\n"
    "buffer_store_dwordx4 v[60:63], v156, s[68:71], s31 offen offset:2048 sc1\n"
    "s_barrier\n"
    "s_waitcnt vmcnt(4)\n"
    "s_mov_b64 exec, 0xff\n"
    "buffer_store_dwordx4 v[162:165], v156, s[68:71], s88 offen sc1\n"
    "s_mov_b64 exec, -1\n"
    "v_cvt_pk_bf16_f32 v76, v76, v77\n"
    "v_cvt_pk_bf16_f32 v77, v78, v79\n"
    "v_cvt_pk_bf16_f32 v78, v80, v81\n"
    "v_cvt_pk_bf16_f32 v79, v82, v83\n"
    "s_add_u32 s31, s85, 8192\n"
    "buffer_store_dwordx4 v[76:79], v156, s[68:71], s31 offen offset:0 sc1\n"
    "s_barrier\n"
    "s_barrier\n"
    "s_setprio 2\n"
    "ds_read_b64_tr_b16 v[140:141], v160 offset:49152\n"
    "ds_read_b64_tr_b16 v[142:143], v160 offset:57344\n"
    "ds_read_b64_tr_b16 v[144:145], v160 offset:51200\n"
    "ds_read_b64_tr_b16 v[146:147], v160 offset:59392\n"
    "ds_read_b64_tr_b16 v[152:153], v161 offset:51200\n"
    "ds_read_b64_tr_b16 v[154:155], v161 offset:59392\n"
    "ds_read_b64_tr_b16 v[148:149], v161 offset:49152\n"
    "ds_read_b64_tr_b16 v[150:151], v161 offset:57344\n"
    "ds_read_b128 v[84:87], v156 offset:0\n"
    "ds_read_b128 v[88:91], v156 offset:5120\n"
    "ds_read_b128 v[92:95], v156 offset:1024\n"
    "ds_read_b128 v[96:99], v156 offset:6144\n"
    "ds_read_b128 v[100:103], v156 offset:2048\n"
    "ds_read_b128 v[104:107], v156 offset:7168\n"
    "ds_read_b128 v[108:111], v156 offset:3072\n"
    "ds_read_b128 v[112:115], v156 offset:8192\n"
    "s_waitcnt lgkmcnt(6)\n"
    "s_barrier\n"
    "v_mfma_f32_16x16x32_bf16 v[8:11], v[140:143], v[84:87], 0\n"
    "ds_read_b64_tr_b16 v[124:125], v160 offset:8192\n"
    "v_mfma_f32_16x16x32_bf16 v[8:11], v[144:147], v[88:91], v[8:11]\n"
    "ds_read_b64_tr_b16 v[126:127], v160 offset:16384\n"
    "v_mfma_f32_16x16x32_bf16 v[16:19], v[152:155], v[88:91], 0\n"
    "ds_read_b128 v[116:119], v156 offset:4096\n"
    "v_mfma_f32_16x16x32_bf16 v[16:19], v[148:151], v[84:87], v[16:19]\n"
    "ds_read_b128 v[120:123], v156 offset:9216\n"
    "s_waitcnt lgkmcnt(8)\n"
    "v_mfma_f32_16x16x32_bf16 v[32:35], v[148:151], v[92:95], 0\n"
    "ds_read_b64_tr_b16 v[128:129], v160 offset:10240\n"
    "v_mfma_f32_16x16x32_bf16 v[32:35], v[152:155], v[96:99], v[32:35]\n"
    "ds_read_b64_tr_b16 v[130:131], v160 offset:18432\n"
    "v_mfma_f32_16x16x32_bf16 v[24:27], v[144:147], v[96:99], 0\n"
    "ds_read_b64_tr_b16 v[136:137], v161 offset:10240\n"
    "v_mfma_f32_16x16x32_bf16 v[24:27], v[140:143], v[92:95], v[24:27]\n"
    "ds_read_b64_tr_b16 v[138:139], v161 offset:18432\n"
    "s_waitcnt lgkmcnt(10)\n"
    "v_mfma_f32_16x16x32_bf16 v[40:43], v[140:143], v[100:103], 0\n"
    "ds_read_b64_tr_b16 v[132:133], v161 offset:8192\n"
    "v_mfma_f32_16x16x32_bf16 v[40:43], v[144:147], v[104:107], v[40:43]\n"
    "ds_read_b128 v[88:91], v156 offset:15360\n"
    "v_mfma_f32_16x16x32_bf16 v[48:51], v[152:155], v[104:107], 0\n"
    "ds_read_b128 v[84:87], v156 offset:10240\n"
    "v_mfma_f32_16x16x32_bf16 v[48:51], v[148:151], v[100:103], v[48:51]\n"
    "ds_read_b64_tr_b16 v[134:135], v161 offset:16384\n"
    "s_waitcnt lgkmcnt(12)\n"
    "v_mfma_f32_16x16x32_bf16 v[64:67], v[148:151], v[108:111], 0\n"
    "ds_read_b128 v[96:99], v156 offset:16384\n"
    "v_mfma_f32_16x16x32_bf16 v[64:67], v[152:155], v[112:115], v[64:67]\n"
    "ds_read_b128 v[92:95], v156 offset:11264\n"
    "v_mfma_f32_16x16x32_bf16 v[56:59], v[144:147], v[112:115], 0\n"
    "ds_read_b128 v[104:107], v156 offset:17408\n"
    "v_mfma_f32_16x16x32_bf16 v[56:59], v[140:143], v[108:111], v[56:59]\n"
    "ds_read_b128 v[100:103], v156 offset:12288\n"
    "s_waitcnt lgkmcnt(12)\n"
    "v_mfma_f32_16x16x32_bf16 v[72:75], v[140:143], v[116:119], 0\n"
    "ds_read_b128 v[112:115], v156 offset:18432\n"
    "v_mfma_f32_16x16x32_bf16 v[72:75], v[144:147], v[120:123], v[72:75]\n"
    "ds_read_b128 v[108:111], v156 offset:13312\n"
    "v_mfma_f32_16x16x32_bf16 v[80:83], v[152:155], v[120:123], 0\n"
    "ds_read_b128 v[120:123], v156 offset:19456\n"
    "s_waitcnt lgkmcnt(7)\n"
    "v_mfma_f32_16x16x32_bf16 v[80:83], v[148:151], v[116:119], v[80:83]\n"
    "s_barrier\n"
    "v_mfma_f32_16x16x32_bf16 v[8:11], v[124:127], v[84:87], v[8:11]\n"
    "ds_read_b64_tr_b16 v[140:141], v160 offset:24576\n"
    "v_mfma_f32_16x16x32_bf16 v[8:11], v[128:131], v[88:91], v[8:11]\n"
    "s_waitcnt vmcnt(0)\n"
    "s_mov_b64 exec, 0xff\n"
    "buffer_store_dwordx4 v[162:165], v156, s[68:71], s89 offen sc1\n"
    "s_mov_b64 exec, -1\n"
    "v_mfma_f32_16x16x32_bf16 v[16:19], v[136:139], v[88:91], v[16:19]\n"
    "ds_read_b64_tr_b16 v[142:143], v160 offset:32768\n"
    "v_mfma_f32_16x16x32_bf16 v[16:19], v[132:135], v[84:87], v[16:19]\n"
    "ds_read_b128 v[116:119], v156 offset:14336\n"
    "s_waitcnt lgkmcnt(8)\n"
    "v_mfma_f32_16x16x32_bf16 v[32:35], v[132:135], v[92:95], v[32:35]\n"
    "ds_read_b64_tr_b16 v[144:145], v160 offset:26624\n"
    "v_mfma_f32_16x16x32_bf16 v[32:35], v[136:139], v[96:99], v[32:35]\n"
    "ds_read_b64_tr_b16 v[146:147], v160 offset:34816\n"
    "v_mfma_f32_16x16x32_bf16 v[24:27], v[128:131], v[96:99], v[24:27]\n"
    "ds_read_b64_tr_b16 v[152:153], v161 offset:26624\n"
    "v_mfma_f32_16x16x32_bf16 v[24:27], v[124:127], v[92:95], v[24:27]\n"
    "ds_read_b64_tr_b16 v[154:155], v161 offset:34816\n"
    "s_waitcnt lgkmcnt(10)\n"
    "v_mfma_f32_16x16x32_bf16 v[40:43], v[124:127], v[100:103], v[40:43]\n"
    "ds_read_b64_tr_b16 v[148:149], v161 offset:24576\n"
    "v_mfma_f32_16x16x32_bf16 v[40:43], v[128:131], v[104:107], v[40:43]\n"
    "ds_read_b128 v[88:91], v156 offset:25600\n"
    "v_mfma_f32_16x16x32_bf16 v[48:51], v[136:139], v[104:107], v[48:51]\n"
    "ds_read_b128 v[84:87], v156 offset:20480\n"
    "v_mfma_f32_16x16x32_bf16 v[48:51], v[132:135], v[100:103], v[48:51]\n"
    "ds_read_b64_tr_b16 v[150:151], v161 offset:32768\n"
    "s_waitcnt lgkmcnt(12)\n"
    "v_mfma_f32_16x16x32_bf16 v[64:67], v[132:135], v[108:111], v[64:67]\n"
    "ds_read_b128 v[96:99], v156 offset:26624\n"
    "v_mfma_f32_16x16x32_bf16 v[64:67], v[136:139], v[112:115], v[64:67]\n"
    "ds_read_b128 v[92:95], v156 offset:21504\n"
    "v_mfma_f32_16x16x32_bf16 v[56:59], v[128:131], v[112:115], v[56:59]\n"
    "ds_read_b128 v[104:107], v156 offset:27648\n"
    "v_mfma_f32_16x16x32_bf16 v[56:59], v[124:127], v[108:111], v[56:59]\n"
    "ds_read_b128 v[100:103], v156 offset:22528\n"
    "s_waitcnt lgkmcnt(12)\n"
    "v_mfma_f32_16x16x32_bf16 v[72:75], v[124:127], v[116:119], v[72:75]\n"
    "ds_read_b128 v[112:115], v156 offset:28672\n"
    "v_mfma_f32_16x16x32_bf16 v[72:75], v[128:131], v[120:123], v[72:75]\n"
    "ds_read_b128 v[108:111], v156 offset:23552\n"
    "v_mfma_f32_16x16x32_bf16 v[80:83], v[136:139], v[120:123], v[80:83]\n"
    "ds_read_b128 v[120:123], v156 offset:29696\n"
    "s_waitcnt lgkmcnt(7)\n"
    "v_mfma_f32_16x16x32_bf16 v[80:83], v[132:135], v[116:119], v[80:83]\n"
    "s_barrier\n"
    "v_mfma_f32_16x16x32_bf16 v[8:11], v[140:143], v[84:87], v[8:11]\n"
    "ds_read_b64_tr_b16 v[124:125], v160 offset:40960\n"
    ".Lpage14:\n"
    "s_cbranch_execz .Ltouch_next\n"
    "v_mfma_f32_16x16x32_bf16 v[8:11], v[144:147], v[88:91], v[8:11]\n"
    "ds_read_b64_tr_b16 v[126:127], v160 offset:49152\n"
    "v_mfma_f32_16x16x32_bf16 v[16:19], v[152:155], v[88:91], v[16:19]\n"
    "ds_read_b128 v[116:119], v156 offset:24576\n"
    "v_mfma_f32_16x16x32_bf16 v[16:19], v[148:151], v[84:87], v[16:19]\n"
    "ds_read_b64_tr_b16 v[128:129], v160 offset:43008\n"
    "s_waitcnt lgkmcnt(9)\n"
    "v_mfma_f32_16x16x32_bf16 v[32:35], v[148:151], v[92:95], v[32:35]\n"
    "ds_read_b64_tr_b16 v[130:131], v160 offset:51200\n"
    "v_mfma_f32_16x16x32_bf16 v[32:35], v[152:155], v[96:99], v[32:35]\n"
    "ds_read_b64_tr_b16 v[136:137], v161 offset:43008\n"
    "v_mfma_f32_16x16x32_bf16 v[24:27], v[144:147], v[96:99], v[24:27]\n"
    "ds_read_b64_tr_b16 v[138:139], v161 offset:51200\n"
    "v_mfma_f32_16x16x32_bf16 v[24:27], v[140:143], v[92:95], v[24:27]\n"
    "ds_read_b64_tr_b16 v[132:133], v161 offset:40960\n"
    "s_waitcnt lgkmcnt(11)\n"
    "v_mfma_f32_16x16x32_bf16 v[40:43], v[140:143], v[100:103], v[40:43]\n"
    "ds_read_b128 v[88:91], v156 offset:35840\n"
    "v_mfma_f32_16x16x32_bf16 v[40:43], v[144:147], v[104:107], v[40:43]\n"
    "ds_read_b128 v[84:87], v156 offset:30720\n"
    "v_mfma_f32_16x16x32_bf16 v[48:51], v[152:155], v[104:107], v[48:51]\n"
    "ds_read_b64_tr_b16 v[134:135], v161 offset:49152\n"
    "v_mfma_f32_16x16x32_bf16 v[48:51], v[148:151], v[100:103], v[48:51]\n"
    "ds_read_b128 v[96:99], v156 offset:36864\n"
    "s_waitcnt lgkmcnt(13)\n"
    "v_mfma_f32_16x16x32_bf16 v[64:67], v[148:151], v[108:111], v[64:67]\n"
    "ds_read_b128 v[92:95], v156 offset:31744\n"
    "v_mfma_f32_16x16x32_bf16 v[64:67], v[152:155], v[112:115], v[64:67]\n"
    "ds_read_b128 v[104:107], v156 offset:37888\n"
    "v_mfma_f32_16x16x32_bf16 v[56:59], v[144:147], v[112:115], v[56:59]\n"
    "ds_read_b128 v[100:103], v156 offset:32768\n"
    "v_mfma_f32_16x16x32_bf16 v[56:59], v[140:143], v[108:111], v[56:59]\n"
    "ds_read_b128 v[112:115], v156 offset:38912\n"
    "s_waitcnt lgkmcnt(13)\n"
    "v_mfma_f32_16x16x32_bf16 v[72:75], v[140:143], v[116:119], v[72:75]\n"
    "ds_read_b128 v[108:111], v156 offset:33792\n"
    "v_mfma_f32_16x16x32_bf16 v[72:75], v[144:147], v[120:123], v[72:75]\n"
    "v_mfma_f32_16x16x32_bf16 v[80:83], v[152:155], v[120:123], v[80:83]\n"
    "ds_read_b128 v[120:123], v156 offset:39936\n"
    "s_waitcnt lgkmcnt(7)\n"
    "v_mfma_f32_16x16x32_bf16 v[80:83], v[148:151], v[116:119], v[80:83]\n"
    "s_barrier\n"
    "v_mfma_f32_16x16x32_bf16 v[8:11], v[124:127], v[84:87], v[8:11]\n"
    "ds_read_b128 v[140:143], v158 offset:57344\n"
    "v_mfma_f32_16x16x32_bf16 v[8:11], v[128:131], v[88:91], v[8:11]\n"
    "ds_read_b128 v[144:147], v159 offset:57344\n"
    "v_mfma_f32_16x16x32_bf16 v[16:19], v[136:139], v[88:91], v[16:19]\n"
    "ds_read_b128 v[116:119], v156 offset:34816\n"
    "v_mfma_f32_16x16x32_bf16 v[16:19], v[132:135], v[84:87], v[16:19]\n"
    "ds_read_b128 v[152:155], v159 offset:8192\n"
    "s_waitcnt lgkmcnt(9)\n"
    "v_mfma_f32_16x16x32_bf16 v[32:35], v[132:135], v[92:95], v[32:35]\n"
    "ds_read_b128 v[148:151], v158 offset:8192\n"
    "v_mfma_f32_16x16x32_bf16 v[32:35], v[136:139], v[96:99], v[32:35]\n"
    "ds_read_b64_tr_b16 v[88:89], v162 offset:5120\n"
    "v_mfma_f32_16x16x32_bf16 v[24:27], v[128:131], v[96:99], v[24:27]\n"
    "ds_read_b64_tr_b16 v[90:91], v163 offset:5120\n"
    "v_mfma_f32_16x16x32_bf16 v[24:27], v[124:127], v[92:95], v[24:27]\n"
    "ds_read_b64_tr_b16 v[84:85], v162 offset:0\n"
    "s_waitcnt lgkmcnt(11)\n"
    "v_mfma_f32_16x16x32_bf16 v[40:43], v[124:127], v[100:103], v[40:43]\n"
    "ds_read_b64_tr_b16 v[86:87], v163 offset:0\n"
    "v_mfma_f32_16x16x32_bf16 v[40:43], v[128:131], v[104:107], v[40:43]\n"
    "ds_read_b64_tr_b16 v[96:97], v162 offset:6144\n"
    "v_mfma_f32_16x16x32_bf16 v[48:51], v[136:139], v[104:107], v[48:51]\n"
    "ds_read_b64_tr_b16 v[98:99], v163 offset:6144\n"
    "v_mfma_f32_16x16x32_bf16 v[48:51], v[132:135], v[100:103], v[48:51]\n"
    "ds_read_b64_tr_b16 v[92:93], v162 offset:1024\n"
    "s_waitcnt lgkmcnt(13)\n"
    "v_mfma_f32_16x16x32_bf16 v[64:67], v[132:135], v[108:111], v[64:67]\n"
    "ds_read_b64_tr_b16 v[94:95], v163 offset:1024\n"
    "v_mfma_f32_16x16x32_bf16 v[64:67], v[136:139], v[112:115], v[64:67]\n"
    "ds_read_b64_tr_b16 v[104:105], v162 offset:7168\n"
    "v_mfma_f32_16x16x32_bf16 v[56:59], v[128:131], v[112:115], v[56:59]\n"
    "ds_read_b64_tr_b16 v[106:107], v163 offset:7168\n"
    "v_mfma_f32_16x16x32_bf16 v[56:59], v[124:127], v[108:111], v[56:59]\n"
    "ds_read_b64_tr_b16 v[100:101], v162 offset:2048\n"
    "s_waitcnt lgkmcnt(13)\n"
    "v_mfma_f32_16x16x32_bf16 v[72:75], v[124:127], v[116:119], v[72:75]\n"
    "ds_read_b64_tr_b16 v[102:103], v163 offset:2048\n"
    "v_mfma_f32_16x16x32_bf16 v[72:75], v[128:131], v[120:123], v[72:75]\n"
    "ds_read_b64_tr_b16 v[112:113], v162 offset:8192\n"
    "v_mfma_f32_16x16x32_bf16 v[80:83], v[136:139], v[120:123], v[80:83]\n"
    "ds_read_b64_tr_b16 v[114:115], v163 offset:8192\n"
    "s_waitcnt lgkmcnt(10)\n"
    "v_mfma_f32_16x16x32_bf16 v[80:83], v[132:135], v[116:119], v[80:83]\n"
    "s_barrier\n"
    "v_mfma_f32_16x16x32_bf16 v[8:11], v[140:143], v[84:87], v[8:11]\n"
    "ds_read_b64_tr_b16 v[108:109], v162 offset:3072\n"
    "v_mfma_f32_16x16x32_bf16 v[8:11], v[144:147], v[88:91], v[8:11]\n"
    "ds_read_b64_tr_b16 v[110:111], v163 offset:3072\n"
    "v_mfma_f32_16x16x32_bf16 v[16:19], v[152:155], v[88:91], v[16:19]\n"
    "ds_read_b128 v[124:127], v158 offset:16384\n"
    "v_mfma_f32_16x16x32_bf16 v[16:19], v[148:151], v[84:87], v[16:19]\n"
    "ds_read_b128 v[128:131], v159 offset:16384\n"
    "s_waitcnt lgkmcnt(10)\n"
    "v_mfma_f32_16x16x32_bf16 v[32:35], v[148:151], v[92:95], v[32:35]\n"
    "ds_read_b64_tr_b16 v[120:121], v162 offset:9216\n"
    "v_mfma_f32_16x16x32_bf16 v[32:35], v[152:155], v[96:99], v[32:35]\n"
    "ds_read_b64_tr_b16 v[122:123], v163 offset:9216\n"
    "v_mfma_f32_16x16x32_bf16 v[24:27], v[144:147], v[96:99], v[24:27]\n"
    "ds_read_b64_tr_b16 v[116:117], v162 offset:4096\n"
    "v_mfma_f32_16x16x32_bf16 v[24:27], v[140:143], v[92:95], v[24:27]\n"
    "ds_read_b64_tr_b16 v[118:119], v163 offset:4096\n"
    "s_waitcnt lgkmcnt(10)\n"
    "v_mfma_f32_16x16x32_bf16 v[40:43], v[140:143], v[100:103], v[40:43]\n"
    "ds_read_b128 v[136:139], v159 offset:24576\n"
    "v_mfma_f32_16x16x32_bf16 v[40:43], v[144:147], v[104:107], v[40:43]\n"
    "ds_read_b128 v[132:135], v158 offset:24576\n"
    "v_mfma_f32_16x16x32_bf16 v[48:51], v[152:155], v[104:107], v[48:51]\n"
    "ds_read_b64_tr_b16 v[88:89], v162 offset:15360\n"
    "v_mfma_f32_16x16x32_bf16 v[48:51], v[148:151], v[100:103], v[48:51]\n"
    "ds_read_b64_tr_b16 v[90:91], v163 offset:15360\n"
    "s_waitcnt lgkmcnt(10)\n"
    "v_mfma_f32_16x16x32_bf16 v[64:67], v[148:151], v[108:111], v[64:67]\n"
    "ds_read_b64_tr_b16 v[84:85], v162 offset:10240\n"
    "v_mfma_f32_16x16x32_bf16 v[64:67], v[152:155], v[112:115], v[64:67]\n"
    "ds_read_b64_tr_b16 v[86:87], v163 offset:10240\n"
    "v_mfma_f32_16x16x32_bf16 v[56:59], v[144:147], v[112:115], v[56:59]\n"
    "ds_read_b64_tr_b16 v[96:97], v162 offset:16384\n"
    "v_mfma_f32_16x16x32_bf16 v[56:59], v[140:143], v[108:111], v[56:59]\n"
    "ds_read_b64_tr_b16 v[98:99], v163 offset:16384\n"
    "s_waitcnt lgkmcnt(8)\n"
    "v_mfma_f32_16x16x32_bf16 v[72:75], v[140:143], v[116:119], v[72:75]\n"
    "ds_read_b64_tr_b16 v[92:93], v162 offset:11264\n"
    "v_mfma_f32_16x16x32_bf16 v[72:75], v[144:147], v[120:123], v[72:75]\n"
    "ds_read_b64_tr_b16 v[94:95], v163 offset:11264\n"
    "v_mfma_f32_16x16x32_bf16 v[80:83], v[152:155], v[120:123], v[80:83]\n"
    "ds_read_b64_tr_b16 v[104:105], v162 offset:17408\n"
    "s_waitcnt lgkmcnt(5)\n"
    "v_mfma_f32_16x16x32_bf16 v[80:83], v[148:151], v[116:119], v[80:83]\n"
    "s_barrier\n"
    "v_mfma_f32_16x16x32_bf16 v[8:11], v[124:127], v[84:87], v[8:11]\n"
    "ds_read_b64_tr_b16 v[106:107], v163 offset:17408\n"
    "v_mfma_f32_16x16x32_bf16 v[8:11], v[128:131], v[88:91], v[8:11]\n"
    "ds_read_b64_tr_b16 v[100:101], v162 offset:12288\n"
    "ds_read_b64_tr_b16 v[102:103], v163 offset:12288\n"
    "v_mfma_f32_16x16x32_bf16 v[16:19], v[136:139], v[88:91], v[16:19]\n"
    "ds_read_b64_tr_b16 v[112:113], v162 offset:18432\n"
    "v_mfma_f32_16x16x32_bf16 v[16:19], v[132:135], v[84:87], v[16:19]\n"
    "ds_read_b64_tr_b16 v[114:115], v163 offset:18432\n"
    "s_waitcnt lgkmcnt(6)\n"
    "v_mfma_f32_16x16x32_bf16 v[32:35], v[132:135], v[92:95], v[32:35]\n"
    "ds_read_b64_tr_b16 v[108:109], v162 offset:13312\n"
    "v_mfma_f32_16x16x32_bf16 v[32:35], v[136:139], v[96:99], v[32:35]\n"
    "ds_read_b64_tr_b16 v[110:111], v163 offset:13312\n"
    "v_mfma_f32_16x16x32_bf16 v[24:27], v[128:131], v[96:99], v[24:27]\n"
    "ds_read_b64_tr_b16 v[140:141], v160 offset:32768\n"
    "v_mfma_f32_16x16x32_bf16 v[24:27], v[124:127], v[92:95], v[24:27]\n"
    "ds_read_b64_tr_b16 v[142:143], v160 offset:40960\n"
    "s_waitcnt lgkmcnt(6)\n"
    "v_mfma_f32_16x16x32_bf16 v[40:43], v[124:127], v[100:103], v[40:43]\n"
    "ds_read_b64_tr_b16 v[120:121], v162 offset:19456\n"
    "v_mfma_f32_16x16x32_bf16 v[40:43], v[128:131], v[104:107], v[40:43]\n"
    "ds_read_b64_tr_b16 v[122:123], v163 offset:19456\n"
    "ds_read_b64_tr_b16 v[116:117], v162 offset:14336\n"
    "v_mfma_f32_16x16x32_bf16 v[48:51], v[136:139], v[104:107], v[48:51]\n"
    "ds_read_b64_tr_b16 v[118:119], v163 offset:14336\n"
    "ds_read_b64_tr_b16 v[144:145], v160 offset:34816\n"
    "v_mfma_f32_16x16x32_bf16 v[48:51], v[132:135], v[100:103], v[48:51]\n"
    "ds_read_b64_tr_b16 v[146:147], v160 offset:43008\n"
    "ds_read_b64_tr_b16 v[152:153], v161 offset:34816\n"
    "s_waitcnt lgkmcnt(9)\n"
    "v_mfma_f32_16x16x32_bf16 v[64:67], v[132:135], v[108:111], v[64:67]\n"
    "ds_read_b64_tr_b16 v[154:155], v161 offset:43008\n"
    "v_mfma_f32_16x16x32_bf16 v[64:67], v[136:139], v[112:115], v[64:67]\n"
    "ds_read_b64_tr_b16 v[148:149], v161 offset:32768\n"
    "ds_read_b128 v[88:91], v156 offset:46080\n"
    "v_mfma_f32_16x16x32_bf16 v[56:59], v[128:131], v[112:115], v[56:59]\n"
    "ds_read_b128 v[84:87], v156 offset:40960\n"
    "ds_read_b64_tr_b16 v[150:151], v161 offset:40960\n"
    "v_mfma_f32_16x16x32_bf16 v[56:59], v[124:127], v[108:111], v[56:59]\n"
    "ds_read_b128 v[96:99], v156 offset:47104\n"
    "s_waitcnt lgkmcnt(9)\n"
    "v_mfma_f32_16x16x32_bf16 v[72:75], v[124:127], v[116:119], v[72:75]\n"
    "ds_read_b128 v[92:95], v156 offset:41984\n"
    "v_mfma_f32_16x16x32_bf16 v[72:75], v[128:131], v[120:123], v[72:75]\n"
    "ds_read_b128 v[104:107], v156 offset:48128\n"
    "v_mfma_f32_16x16x32_bf16 v[80:83], v[136:139], v[120:123], v[80:83]\n"
    "ds_read_b128 v[100:103], v156 offset:43008\n"
    "s_waitcnt lgkmcnt(4)\n"
    "v_mfma_f32_16x16x32_bf16 v[80:83], v[132:135], v[116:119], v[80:83]\n"
    "s_barrier\n"
    "v_mfma_f32_16x16x32_bf16 v[8:11], v[140:143], v[84:87], v[8:11]\n"
    "ds_read_b128 v[112:115], v156 offset:49152\n"
    "v_mfma_f32_16x16x32_bf16 v[8:11], v[144:147], v[88:91], v[8:11]\n"
    "ds_read_b128 v[108:111], v156 offset:44032\n"
    "v_mfma_f32_16x16x32_bf16 v[16:19], v[152:155], v[88:91], v[16:19]\n"
    "ds_read_b64_tr_b16 v[124:125], v160 offset:49152\n"
    "v_mfma_f32_16x16x32_bf16 v[16:19], v[148:151], v[84:87], v[16:19]\n"
    "ds_read_b64_tr_b16 v[126:127], v160 offset:57344\n"
    "s_waitcnt lgkmcnt(6)\n"
    "v_mfma_f32_16x16x32_bf16 v[32:35], v[148:151], v[92:95], v[32:35]\n"
    "ds_read_b128 v[120:123], v156 offset:50176\n"
    "v_mfma_f32_16x16x32_bf16 v[32:35], v[152:155], v[96:99], v[32:35]\n"
    "ds_read_b128 v[116:119], v156 offset:45056\n"
    "v_mfma_f32_16x16x32_bf16 v[24:27], v[144:147], v[96:99], v[24:27]\n"
    "ds_read_b64_tr_b16 v[128:129], v160 offset:51200\n"
    "v_mfma_f32_16x16x32_bf16 v[24:27], v[140:143], v[92:95], v[24:27]\n"
    "ds_read_b64_tr_b16 v[130:131], v160 offset:59392\n"
    "s_waitcnt lgkmcnt(8)\n"
    "v_mfma_f32_16x16x32_bf16 v[40:43], v[140:143], v[100:103], v[40:43]\n"
    "ds_read_b64_tr_b16 v[136:137], v161 offset:51200\n"
    "v_mfma_f32_16x16x32_bf16 v[40:43], v[144:147], v[104:107], v[40:43]\n"
    "ds_read_b64_tr_b16 v[138:139], v161 offset:59392\n"
    "v_mfma_f32_16x16x32_bf16 v[48:51], v[152:155], v[104:107], v[48:51]\n"
    "ds_read_b64_tr_b16 v[132:133], v161 offset:49152\n"
    "v_mfma_f32_16x16x32_bf16 v[48:51], v[148:151], v[100:103], v[48:51]\n"
    "ds_read_b128 v[88:91], v157 offset:5120\n"
    "s_waitcnt lgkmcnt(10)\n"
    "v_mfma_f32_16x16x32_bf16 v[64:67], v[148:151], v[108:111], v[64:67]\n"
    "ds_read_b128 v[84:87], v157 offset:0\n"
    "v_mfma_f32_16x16x32_bf16 v[64:67], v[152:155], v[112:115], v[64:67]\n"
    "ds_read_b64_tr_b16 v[134:135], v161 offset:57344\n"
    "v_mfma_f32_16x16x32_bf16 v[56:59], v[144:147], v[112:115], v[56:59]\n"
    "ds_read_b128 v[96:99], v157 offset:6144\n"
    "v_mfma_f32_16x16x32_bf16 v[56:59], v[140:143], v[108:111], v[56:59]\n"
    "ds_read_b128 v[92:95], v157 offset:1024\n"
    "s_waitcnt lgkmcnt(10)\n"
    "v_mfma_f32_16x16x32_bf16 v[72:75], v[140:143], v[116:119], v[72:75]\n"
    "ds_read_b128 v[104:107], v157 offset:7168\n"
    "v_mfma_f32_16x16x32_bf16 v[72:75], v[144:147], v[120:123], v[72:75]\n"
    "ds_read_b128 v[100:103], v157 offset:2048\n"
    "v_mfma_f32_16x16x32_bf16 v[80:83], v[152:155], v[120:123], v[80:83]\n"
    "ds_read_b128 v[112:115], v157 offset:8192\n"
    "s_waitcnt lgkmcnt(5)\n"
    "v_mfma_f32_16x16x32_bf16 v[80:83], v[148:151], v[116:119], v[80:83]\n"
    "s_barrier\n"
    "v_mfma_f32_16x16x32_bf16 v[8:11], v[124:127], v[84:87], v[8:11]\n"
    "ds_read_b128 v[108:111], v157 offset:3072\n"
    "v_mfma_f32_16x16x32_bf16 v[8:11], v[128:131], v[88:91], v[8:11]\n"
    "ds_read_b64_tr_b16 v[140:141], v160 offset:8192\n"
    "v_mfma_f32_16x16x32_bf16 v[16:19], v[136:139], v[88:91], v[16:19]\n"
    "ds_read_b64_tr_b16 v[142:143], v160 offset:16384\n"
    "v_mfma_f32_16x16x32_bf16 v[16:19], v[132:135], v[84:87], v[16:19]\n"
    "ds_read_b128 v[120:123], v157 offset:9216\n"
    "s_waitcnt lgkmcnt(7)\n"
    "v_mfma_f32_16x16x32_bf16 v[32:35], v[132:135], v[92:95], v[32:35]\n"
    "ds_read_b128 v[116:119], v157 offset:4096\n"
    "v_mfma_f32_16x16x32_bf16 v[32:35], v[136:139], v[96:99], v[32:35]\n"
    "ds_read_b64_tr_b16 v[144:145], v160 offset:10240\n"
    "v_mfma_f32_16x16x32_bf16 v[24:27], v[128:131], v[96:99], v[24:27]\n"
    "ds_read_b64_tr_b16 v[146:147], v160 offset:18432\n"
    "v_mfma_f32_16x16x32_bf16 v[24:27], v[124:127], v[92:95], v[24:27]\n"
    "ds_read_b64_tr_b16 v[152:153], v161 offset:10240\n"
    "s_waitcnt lgkmcnt(9)\n"
    "v_mfma_f32_16x16x32_bf16 v[40:43], v[124:127], v[100:103], v[40:43]\n"
    "ds_read_b64_tr_b16 v[154:155], v161 offset:18432\n"
    "v_mfma_f32_16x16x32_bf16 v[40:43], v[128:131], v[104:107], v[40:43]\n"
    "ds_read_b64_tr_b16 v[148:149], v161 offset:8192\n"
    "v_mfma_f32_16x16x32_bf16 v[48:51], v[136:139], v[104:107], v[48:51]\n"
    "ds_read_b128 v[88:91], v157 offset:15360\n"
    "v_mfma_f32_16x16x32_bf16 v[48:51], v[132:135], v[100:103], v[48:51]\n"
    "ds_read_b128 v[84:87], v157 offset:10240\n"
    "s_waitcnt lgkmcnt(11)\n"
    "v_mfma_f32_16x16x32_bf16 v[64:67], v[132:135], v[108:111], v[64:67]\n"
    "ds_read_b64_tr_b16 v[150:151], v161 offset:16384\n"
    "v_mfma_f32_16x16x32_bf16 v[64:67], v[136:139], v[112:115], v[64:67]\n"
    "ds_read_b128 v[96:99], v157 offset:16384\n"
    "v_mfma_f32_16x16x32_bf16 v[56:59], v[128:131], v[112:115], v[56:59]\n"
    "ds_read_b128 v[92:95], v157 offset:11264\n"
    "v_mfma_f32_16x16x32_bf16 v[56:59], v[124:127], v[108:111], v[56:59]\n"
    "ds_read_b128 v[104:107], v157 offset:17408\n"
    "s_waitcnt lgkmcnt(11)\n"
    "v_mfma_f32_16x16x32_bf16 v[72:75], v[124:127], v[116:119], v[72:75]\n"
    "ds_read_b128 v[100:103], v157 offset:12288\n"
    "v_mfma_f32_16x16x32_bf16 v[72:75], v[128:131], v[120:123], v[72:75]\n"
    "ds_read_b128 v[112:115], v157 offset:18432\n"
    "v_mfma_f32_16x16x32_bf16 v[80:83], v[136:139], v[120:123], v[80:83]\n"
    "ds_read_b128 v[108:111], v157 offset:13312\n"
    "s_waitcnt lgkmcnt(6)\n"
    "v_mfma_f32_16x16x32_bf16 v[80:83], v[132:135], v[116:119], v[80:83]\n"
    "s_barrier\n"
    "v_mfma_f32_16x16x32_bf16 v[8:11], v[140:143], v[84:87], v[8:11]\n"
    "ds_read_b64_tr_b16 v[124:125], v160 offset:24576\n"
    "v_mfma_f32_16x16x32_bf16 v[8:11], v[144:147], v[88:91], v[8:11]\n"
    "ds_read_b64_tr_b16 v[126:127], v160 offset:32768\n"
    "v_mfma_f32_16x16x32_bf16 v[16:19], v[152:155], v[88:91], v[16:19]\n"
    "ds_read_b128 v[120:123], v157 offset:19456\n"
    "v_mfma_f32_16x16x32_bf16 v[16:19], v[148:151], v[84:87], v[16:19]\n"
    "ds_read_b128 v[116:119], v157 offset:14336\n"
    "s_waitcnt lgkmcnt(8)\n"
    "v_mfma_f32_16x16x32_bf16 v[32:35], v[148:151], v[92:95], v[32:35]\n"
    "ds_read_b64_tr_b16 v[128:129], v160 offset:26624\n"
    "v_mfma_f32_16x16x32_bf16 v[32:35], v[152:155], v[96:99], v[32:35]\n"
    "ds_read_b64_tr_b16 v[130:131], v160 offset:34816\n"
    "v_mfma_f32_16x16x32_bf16 v[24:27], v[144:147], v[96:99], v[24:27]\n"
    "ds_read_b64_tr_b16 v[136:137], v161 offset:26624\n"
    "v_mfma_f32_16x16x32_bf16 v[24:27], v[140:143], v[92:95], v[24:27]\n"
    "ds_read_b64_tr_b16 v[138:139], v161 offset:34816\n"
    "s_waitcnt lgkmcnt(10)\n"
    "v_mfma_f32_16x16x32_bf16 v[40:43], v[140:143], v[100:103], v[40:43]\n"
    "ds_read_b64_tr_b16 v[132:133], v161 offset:24576\n"
    "v_mfma_f32_16x16x32_bf16 v[40:43], v[144:147], v[104:107], v[40:43]\n"
    "ds_read_b128 v[88:91], v157 offset:25600\n"
    "v_mfma_f32_16x16x32_bf16 v[48:51], v[152:155], v[104:107], v[48:51]\n"
    "ds_read_b128 v[84:87], v157 offset:20480\n"
    "v_mfma_f32_16x16x32_bf16 v[48:51], v[148:151], v[100:103], v[48:51]\n"
    "ds_read_b64_tr_b16 v[134:135], v161 offset:32768\n"
    "s_waitcnt lgkmcnt(12)\n"
    "v_mfma_f32_16x16x32_bf16 v[64:67], v[148:151], v[108:111], v[64:67]\n"
    "ds_read_b128 v[96:99], v157 offset:26624\n"
    "v_mfma_f32_16x16x32_bf16 v[64:67], v[152:155], v[112:115], v[64:67]\n"
    "ds_read_b128 v[92:95], v157 offset:21504\n"
    "v_mfma_f32_16x16x32_bf16 v[56:59], v[144:147], v[112:115], v[56:59]\n"
    "ds_read_b128 v[104:107], v157 offset:27648\n"
    "v_mfma_f32_16x16x32_bf16 v[56:59], v[140:143], v[108:111], v[56:59]\n"
    "ds_read_b128 v[100:103], v157 offset:22528\n"
    "s_waitcnt lgkmcnt(12)\n"
    "v_mfma_f32_16x16x32_bf16 v[72:75], v[140:143], v[116:119], v[72:75]\n"
    "ds_read_b128 v[112:115], v157 offset:28672\n"
    "v_mfma_f32_16x16x32_bf16 v[72:75], v[144:147], v[120:123], v[72:75]\n"
    "ds_read_b128 v[108:111], v157 offset:23552\n"
    "v_mfma_f32_16x16x32_bf16 v[80:83], v[152:155], v[120:123], v[80:83]\n"
    "ds_read_b128 v[120:123], v157 offset:29696\n"
    "s_waitcnt lgkmcnt(7)\n"
    "v_mfma_f32_16x16x32_bf16 v[80:83], v[148:151], v[116:119], v[80:83]\n"
    "s_barrier\n"
    "v_mfma_f32_16x16x32_bf16 v[8:11], v[124:127], v[84:87], v[8:11]\n"
    "ds_read_b128 v[116:119], v157 offset:24576\n"
    "v_mfma_f32_16x16x32_bf16 v[8:11], v[128:131], v[88:91], v[8:11]\n"
    "v_mfma_f32_16x16x32_bf16 v[16:19], v[136:139], v[88:91], v[16:19]\n"
    "v_mfma_f32_16x16x32_bf16 v[16:19], v[132:135], v[84:87], v[16:19]\n"
    "s_waitcnt lgkmcnt(6)\n"
    "v_mfma_f32_16x16x32_bf16 v[32:35], v[132:135], v[92:95], v[32:35]\n"
    "v_mfma_f32_16x16x32_bf16 v[32:35], v[136:139], v[96:99], v[32:35]\n"
    "v_mfma_f32_16x16x32_bf16 v[24:27], v[128:131], v[96:99], v[24:27]\n"
    "v_mfma_f32_16x16x32_bf16 v[24:27], v[124:127], v[92:95], v[24:27]\n"
    "s_nop 0\n"
    "v_cvt_pk_bf16_f32 v4, v8, v9\n"
    "v_cvt_pk_bf16_f32 v5, v10, v11\n"
    "s_waitcnt lgkmcnt(4)\n"
    "v_mfma_f32_16x16x32_bf16 v[40:43], v[124:127], v[100:103], v[40:43]\n"
    "v_mfma_f32_16x16x32_bf16 v[40:43], v[128:131], v[104:107], v[40:43]\n"
    "v_cvt_pk_bf16_f32 v6, v16, v17\n"
    "v_cvt_pk_bf16_f32 v7, v18, v19\n"
    "buffer_store_dwordx4 v[4:7], v156, s[68:71], s86 offen offset:0 sc1\n"
    "v_mfma_f32_16x16x32_bf16 v[48:51], v[136:139], v[104:107], v[48:51]\n"
    "v_mfma_f32_16x16x32_bf16 v[48:51], v[132:135], v[100:103], v[48:51]\n"
    "v_cvt_pk_bf16_f32 v22, v32, v33\n"
    "v_cvt_pk_bf16_f32 v23, v34, v35\n"
    "s_waitcnt lgkmcnt(2)\n"
    "v_mfma_f32_16x16x32_bf16 v[64:67], v[132:135], v[108:111], v[64:67]\n"
    "v_mfma_f32_16x16x32_bf16 v[64:67], v[136:139], v[112:115], v[64:67]\n"
    "v_cvt_pk_bf16_f32 v20, v24, v25\n"
    "v_cvt_pk_bf16_f32 v21, v26, v27\n"
    "buffer_store_dwordx4 v[20:23], v156, s[68:71], s86 offen offset:2048 sc1\n"
    "v_mfma_f32_16x16x32_bf16 v[56:59], v[128:131], v[112:115], v[56:59]\n"
    "v_mfma_f32_16x16x32_bf16 v[56:59], v[124:127], v[108:111], v[56:59]\n"
    "v_cvt_pk_bf16_f32 v36, v40, v41\n"
    "v_cvt_pk_bf16_f32 v37, v42, v43\n"
    "s_waitcnt lgkmcnt(0)\n"
    "v_mfma_f32_16x16x32_bf16 v[72:75], v[124:127], v[116:119], v[72:75]\n"
    "v_mfma_f32_16x16x32_bf16 v[72:75], v[128:131], v[120:123], v[72:75]\n"
    "v_cvt_pk_bf16_f32 v38, v48, v49\n"
    "v_cvt_pk_bf16_f32 v39, v50, v51\n"
    "s_add_u32 s31, s86, 4096\n"
    "buffer_store_dwordx4 v[36:39], v156, s[68:71], s31 offen offset:0 sc1\n"
    "v_mfma_f32_16x16x32_bf16 v[80:83], v[136:139], v[120:123], v[80:83]\n"
    "v_mfma_f32_16x16x32_bf16 v[80:83], v[132:135], v[116:119], v[80:83]\n"
    "s_setprio 0\n"
    "s_barrier\n"
    "v_cvt_pk_bf16_f32 v54, v64, v65\n"
    "v_cvt_pk_bf16_f32 v55, v66, v67\n"
    "v_cvt_pk_bf16_f32 v52, v56, v57\n"
    "v_cvt_pk_bf16_f32 v53, v58, v59\n"
    "s_add_u32 s31, s86, 4096\n"
    "buffer_store_dwordx4 v[52:55], v156, s[68:71], s31 offen offset:2048 sc1\n"
    "v_cvt_pk_bf16_f32 v68, v72, v73\n"
    "v_cvt_pk_bf16_f32 v69, v74, v75\n"
    "v_cvt_pk_bf16_f32 v70, v80, v81\n"
    "v_cvt_pk_bf16_f32 v71, v82, v83\n"
    "s_add_u32 s31, s86, 8192\n"
    "buffer_store_dwordx4 v[68:71], v156, s[68:71], s31 offen offset:0 sc1\n"
    "s_waitcnt vmcnt(0)\n"
    "s_mov_b64 exec, 0xff\n"
    "buffer_store_dwordx4 v[162:165], v156, s[68:71], s90 offen sc1\n"
    "s_mov_b64 exec, -1\n"
    "s_barrier\n"
    "s_endpgm\n"
    ".Lhelper:\n"
    "s_cmp_ge_u32 s9, 4\n"
    "s_cbranch_scc1 .Lhelp_end\n"
    "s_and_b32 s11, s8, 7\n"
    "s_cmp_lt_u32 s11, 2\n"
    "s_cselect_b32 s12, 8, 12\n"
    "s_sub_u32 s13, 255, s8\n"
    "s_lshr_b32 s13, s13, 3\n"
    "s_mul_i32 s13, s13, 4\n"
    "s_add_u32 s13, s13, s9\n"
    "v_lshlrev_b32 v1, 7, v0\n"
    "v_lshrrev_b32 v2, 2, v0\n"
    "v_mul_u32_u24 v2, 9216, v2\n"
    "v_and_b32 v3, 3, v0\n"
    "v_lshl_add_u32 v2, v3, 7, v2\n"
    "v_lshrrev_b32 v3, 0, v0\n"
    "v_mul_u32_u24 v3, 1152, v3\n"
    "v_and_b32 v4, 0, v0\n"
    "v_lshl_add_u32 v3, v4, 7, v3\n"
    "s_waitcnt lgkmcnt(0)\n"
    "s_sleep 70\n"
    "s_mov_b32 s15, s13\n"
    "s_mov_b32 s68, s42\n"
    "s_and_b32 s69, s43, 0xffff\n"
    "s_mov_b32 s70, 663552\n"
    "s_mov_b32 s71, 0x20000\n"
    "s_mul_i32 s20, s11, 73728\n"
    "s_add_u32 s20, s20, 0\n"
    "s_mul_i32 s21, s11, 9216\n"
    "s_add_u32 s21, s21, 516096\n"
    ".LH_w_1:\n"
    "s_cmp_ge_u32 s15, 11\n"
    "s_cbranch_scc1 .LH_n_2\n"
    "s_cmp_lt_u32 s15, 9\n"
    "s_cselect_b32 s17, s20, s21\n"
    "s_mul_i32 s16, s15, 8192\n"
    "s_add_u32 s16, s16, s17\n"
    "buffer_load_dword v8, v1, s[68:71], s16 offen\n"
    "s_waitcnt vmcnt(1)\n"
    "s_add_u32 s15, s15, s12\n"
    "s_branch .LH_w_1\n"
    ".LH_n_2:\n"
    "s_sub_u32 s15, s15, 11\n"
    "s_mov_b32 s68, s44\n"
    "s_and_b32 s69, s45, 0xffff\n"
    "s_mov_b32 s70, 663552\n"
    "s_mov_b32 s71, 0x20000\n"
    "s_mul_i32 s20, s11, 73728\n"
    "s_add_u32 s20, s20, 0\n"
    "s_mul_i32 s21, s11, 9216\n"
    "s_add_u32 s21, s21, 516096\n"
    ".LH_w_3:\n"
    "s_cmp_ge_u32 s15, 11\n"
    "s_cbranch_scc1 .LH_n_4\n"
    "s_cmp_lt_u32 s15, 9\n"
    "s_cselect_b32 s17, s20, s21\n"
    "s_mul_i32 s16, s15, 8192\n"
    "s_add_u32 s16, s16, s17\n"
    "buffer_load_dword v8, v1, s[68:71], s16 offen\n"
    "s_waitcnt vmcnt(1)\n"
    "s_add_u32 s15, s15, s12\n"
    "s_branch .LH_w_3\n"
    ".LH_n_4:\n"
    "s_sub_u32 s15, s15, 11\n"
    "s_mov_b32 s68, s54\n"
    "s_and_b32 s69, s55, 0xffff\n"
    "s_mov_b32 s70, 5308416\n"
    "s_mov_b32 s71, 0x20000\n"
    "s_mul_i32 s20, s11, 1024\n"
    "s_add_u32 s20, s20, 512\n"
    "s_mul_i32 s21, s11, 663552\n"
    "s_add_u32 s21, s21, 4289667584\n"
    ".LH_w_5:\n"
    "s_cmp_ge_u32 s15, 41\n"
    "s_cbranch_scc1 .LH_n_6\n"
    "s_cmp_lt_u32 s15, 36\n"
    "s_cselect_b32 s17, s20, s21\n"
    "s_mul_i32 s16, s15, 147456\n"
    "s_add_u32 s16, s16, s17\n"
    "buffer_load_dword v8, v2, s[68:71], s16 offen\n"
    "s_waitcnt vmcnt(1)\n"
    "s_add_u32 s15, s15, s12\n"
    "s_branch .LH_w_5\n"
    ".LH_n_6:\n"
    "s_sub_u32 s15, s15, 41\n"
    "s_mov_b32 s68, s58\n"
    "s_and_b32 s69, s59, 0xffff\n"
    "s_mov_b32 s70, 5308416\n"
    "s_mov_b32 s71, 0x20000\n"
    "s_mul_i32 s20, s11, 589824\n"
    "s_add_u32 s20, s20, 0\n"
    "s_mul_i32 s21, s11, 36864\n"
    "s_add_u32 s21, s21, 4423680\n"
    ".LH_w_7:\n"
    "s_cmp_ge_u32 s15, 41\n"
    "s_cbranch_scc1 .LH_n_8\n"
    "s_cmp_lt_u32 s15, 36\n"
    "s_cselect_b32 s17, s20, s21\n"
    "s_mul_i32 s16, s15, 8192\n"
    "s_add_u32 s16, s16, s17\n"
    "buffer_load_dword v8, v1, s[68:71], s16 offen\n"
    "s_waitcnt vmcnt(1)\n"
    "s_add_u32 s15, s15, s12\n"
    "s_branch .LH_w_7\n"
    ".LH_n_8:\n"
    "s_sub_u32 s15, s15, 41\n"
    "s_mov_b32 s68, s46\n"
    "s_and_b32 s69, s47, 0xffff\n"
    "s_mov_b32 s70, 663552\n"
    "s_mov_b32 s71, 0x20000\n"
    "s_mul_i32 s20, s11, 128\n"
    "s_add_u32 s20, s20, 0\n"
    "s_mul_i32 s21, s11, 82944\n"
    "s_add_u32 s21, s21, 4294304768\n"
    ".LH_w_9:\n"
    "s_cmp_ge_u32 s15, 11\n"
    "s_cbranch_scc1 .LH_n_10\n"
    "s_cmp_lt_u32 s15, 9\n"
    "s_cselect_b32 s17, s20, s21\n"
    "s_mul_i32 s16, s15, 73728\n"
    "s_add_u32 s16, s16, s17\n"
    "buffer_load_dword v8, v3, s[68:71], s16 offen\n"
    "s_waitcnt vmcnt(1)\n"
    "s_add_u32 s15, s15, s12\n"
    "s_branch .LH_w_9\n"
    ".LH_n_10:\n"
    "s_sub_u32 s15, s15, 11\n"
    "s_mov_b32 s68, s48\n"
    "s_and_b32 s69, s49, 0xffff\n"
    "s_mov_b32 s70, 663552\n"
    "s_mov_b32 s71, 0x20000\n"
    "s_mul_i32 s20, s11, 128\n"
    "s_add_u32 s20, s20, 0\n"
    "s_mul_i32 s21, s11, 82944\n"
    "s_add_u32 s21, s21, 4294304768\n"
    ".LH_w_11:\n"
    "s_cmp_ge_u32 s15, 11\n"
    "s_cbranch_scc1 .LH_n_12\n"
    "s_cmp_lt_u32 s15, 9\n"
    "s_cselect_b32 s17, s20, s21\n"
    "s_mul_i32 s16, s15, 73728\n"
    "s_add_u32 s16, s16, s17\n"
    "buffer_load_dword v8, v3, s[68:71], s16 offen\n"
    "s_waitcnt vmcnt(1)\n"
    "s_add_u32 s15, s15, s12\n"
    "s_branch .LH_w_11\n"
    ".LH_n_12:\n"
    "s_sub_u32 s15, s15, 11\n"
    "s_mov_b32 s68, s58\n"
    "s_and_b32 s69, s59, 0xffff\n"
    "s_mov_b32 s70, 5308416\n"
    "s_mov_b32 s71, 0x20000\n"
    "s_mul_i32 s20, s11, 589824\n"
    "s_add_u32 s20, s20, 294912\n"
    "s_mul_i32 s21, s11, 36864\n"
    "s_add_u32 s21, s21, 4718592\n"
    ".LH_w_13:\n"
    "s_cmp_ge_u32 s15, 41\n"
    "s_cbranch_scc1 .LH_n_14\n"
    "s_cmp_lt_u32 s15, 36\n"
    "s_cselect_b32 s17, s20, s21\n"
    "s_mul_i32 s16, s15, 8192\n"
    "s_add_u32 s16, s16, s17\n"
    "buffer_load_dword v8, v1, s[68:71], s16 offen\n"
    "s_waitcnt vmcnt(1)\n"
    "s_add_u32 s15, s15, s12\n"
    "s_branch .LH_w_13\n"
    ".LH_n_14:\n"
    "s_sub_u32 s15, s15, 41\n"
    ".Lhelp_end:\n"
    "s_endpgm\n"
    ".Lcode_end:\n"
    :
    : "s"(__builtin_amdgcn_kernarg_segment_ptr()), "s"(blockIdx.x), "s"(wave)
    : "v0", "v1", "v2", "v3", "v4", "v5", "v6", "v7", "v8", "v9", "v10", "v11", "v12", "v13", "v14", "v15", "v16", "v17", "v18", "v19", "v20", "v21", "v22", "v23", "v24", "v25", "v26", "v27", "v28", "v29", "v30", "v31", "v32", "v33", "v34", "v35", "v36", "v37", "v38", "v39", "v40", "v41", "v42", "v43", "v44", "v45", "v46", "v47", "v48", "v49", "v50", "v51", "v52", "v53", "v54", "v55", "v56", "v57", "v58", "v59", "v60", "v61", "v62", "v63", "v64", "v65", "v66", "v67", "v68", "v69", "v70", "v71", "v72", "v73", "v74", "v75", "v76", "v77", "v78", "v79", "v80", "v81", "v82", "v83", "v84", "v85", "v86", "v87", "v88", "v89", "v90", "v91", "v92", "v93", "v94", "v95", "v96", "v97", "v98", "v99", "v100", "v101", "v102", "v103", "v104", "v105", "v106", "v107", "v108", "v109", "v110", "v111", "v112", "v113", "v114", "v115", "v116", "v117", "v118", "v119", "v120", "v121", "v122", "v123", "v124", "v125", "v126", "v127", "v128", "v129", "v130", "v131", "v132", "v133", "v134", "v135", "v136", "v137", "v138", "v139", "v140", "v141", "v142", "v143", "v144", "v145", "v146", "v147", "v148", "v149", "v150", "v151", "v152", "v153", "v154", "v155", "v156", "v157", "v158", "v159", "v160", "v161", "v162", "v163", "v164", "v165", "v166", "v167", "s4", "s5", "s6", "s7", "s8", "s9", "s10", "s11", "s12", "s13", "s14", "s15", "s16", "s17", "s18", "s19", "s20", "s21", "s22", "s23", "s24", "s25", "s26", "s27", "s28", "s29", "s30", "s31", "s34", "s35", "s36", "s37", "s38", "s39", "s40", "s41", "s42", "s43", "s44", "s45", "s46", "s47", "s48", "s49", "s50", "s51", "s52", "s53", "s54", "s55", "s56", "s57", "s58", "s59", "s60", "s61", "s62", "s63", "s64", "s65", "s66", "s67", "s68", "s69", "s70", "s71", "s72", "s73", "s74", "s75", "s76", "s77", "s78", "s79", "s80", "s81", "s82", "s83", "s84", "s85", "s86", "s87", "s88", "s89", "s90", "s91", "s92", "s93", "s94", "s95", "s96", "s97", "s98", "s99", "vcc", "scc", "memory");
}

extern "C" void kernel_launch(void* const* d_in, const int* in_sizes, int n_in, void* d_out, int out_size,
                              void* d_ws, size_t ws_size, hipStream_t stream) {
  (void)hipFuncSetAttribute((const void*)fused_s5_mlp_kernel, hipFuncAttributeMaxDynamicSharedMemorySize, 163840);
  fused_s5_mlp_kernel<<<256, 768, 163840, stream>>>(d_in[0], d_in[1], d_in[2], d_in[3], d_in[4], d_in[5], d_in[6],
                                                    d_in[7], d_in[8], d_in[10], d_in[11], d_in[12], d_in[13],
                                                    d_out, d_ws, d_ws);
}
```
